```python
import torch

def _softmax(x, axis=-1):
    return torch.softmax(x, dim=axis)
def _gelu(x):
    return torch.nn.functional.gelu(x, approximate="tanh")
def _silu(x):
    return torch.nn.functional.silu(x)
def _sigmoid(x):
    return torch.sigmoid(x)
def _layernorm(x, axis=-1, eps=1e-5):
    m = x.mean(dim=axis, keepdim=True)
    v = x.var(dim=axis, keepdim=True, unbiased=False)
    return (x - m) / torch.sqrt(v + eps)
def _scatter_add(tgt, idx, src, axis=0):
    return tgt.clone().index_add_(axis, idx, src)
def _segment_sum(src, seg, n):
    out = torch.zeros((n,) + src.shape[1:], dtype=src.dtype)
    return out.index_add_(0, seg, src)
def _pad(x, pw):
    flat = tuple(v for pair in reversed(pw) for v in pair)
    return torch.nn.functional.pad(x, flat)

kernel_name = 'recurrent_scan__mha_block_cached__moe_arch_mod__prompt'

def setup_inputs():

    _w = (lambda a: (lambda t: t.to(torch.bfloat16) if t.is_floating_point() else t)(torch.as_tensor(a)))
    def _A_setup():
        B, Sq, D = 1, 2048, 512
        H, Sk, Dh = 32, 2048, 128
        return {
            "x": _w((lambda *s: torch.randn(*s, dtype=torch.bfloat16) * 0.05)(B, Sq, D)),
            "k_cache": _w((lambda *s: torch.randn(*s, dtype=torch.bfloat16) * 1.6)(B, H, Sk, Dh)),
            "v_cache": _w((lambda *s: torch.randn(*s, dtype=torch.bfloat16) * 0.2)(B, H, Sk, Dh)),
            "wq": _w((lambda *s: torch.randn(*s, dtype=torch.bfloat16) * 1.6)(D, H * Dh) * (D ** -0.5)),
            "wo": _w((lambda *s: torch.randn(*s, dtype=torch.bfloat16) * 0.2)(H * Dh, D) * ((H * Dh) ** -0.5)),
        }
    _A_in = _A_setup()

    def _B_setup():
        B, S, D, F, C = 1, 2048, 512, 1376, 1
        return {
            "x": _w((lambda *s: torch.randn(*s, dtype=torch.bfloat16) * 0.05)(B, S, D)),
            "w_r": _w((lambda *s: torch.randn(*s, dtype=torch.bfloat16) * 0.2)(D)),
            "w_gate": _w((lambda *s: torch.randn(*s, dtype=torch.bfloat16) * 0.1)(D, F)),
            "w_up": _w((lambda *s: torch.randn(*s, dtype=torch.bfloat16) * 0.1)(D, F)),
            "w_down": _w((lambda *s: torch.randn(*s, dtype=torch.bfloat16) * 0.1)(F, D)),
        }
    _B_in = _B_setup()

    return {
        "A_x": _A_in["x"],
        "A_k_cache": _A_in["k_cache"],
        "A_v_cache": _A_in["v_cache"],
        "A_wq": _A_in["wq"],
        "A_wo": _A_in["wo"],
        "B_x": _B_in["x"],
        "B_w_r": _B_in["w_r"],
        "B_w_gate": _B_in["w_gate"],
        "B_w_up": _B_in["w_up"],
        "B_w_down": _B_in["w_down"],
    }

def reference(A_x, A_k_cache, A_v_cache, A_wq, A_wo, B_x, B_w_r, B_w_gate, B_w_up, B_w_down):
    def _A_ref(x, k_cache, v_cache, wq, wo):
        B, Sq, D = x.shape
        H, Dh = 32, 128
        q = (lambda x, *d: x.permute(*d))((x @ wq).reshape(B, Sq, H, Dh), 0, 2, 1, 3)
        scores = torch.einsum("bhqd,bhkd->bhqk", q, k_cache) * (Dh ** -0.5)
        p = _softmax(scores, axis=-1)
        o = torch.einsum("bhqk,bhkd->bhqd", p, v_cache)
        return (lambda x, *d: x.permute(*d))(o, 0, 2, 1, 3).reshape(B, Sq, H * Dh) @ wo

    def _B_ref(x, w_r, w_gate, w_up, w_down):
        B, S, D = x.shape
        C = 1
        r = x @ w_r
        idx = torch.argsort(-r, axis=-1)[:, :C]
        idx_e = (lambda x, n, axis: torch.repeat_interleave(x, n, dim=axis))(idx.reshape(B, C, 1), D, axis=2)
        x_sel = (lambda x, i, axis: torch.take_along_dim(x, i, dim=axis))(x, idx_e, axis=1)
        g = _silu(x_sel @ w_gate)
        u = x_sel @ w_up
        y = (g * u) @ w_down
        r_sel = _sigmoid((lambda x, i, axis: torch.take_along_dim(x, i, dim=axis))(r, idx, axis=1))
        y = y * r_sel.reshape(B, C, 1)
        b_off = (torch.arange(B) * S).reshape(B, 1)
        flat_idx = (idx + b_off).reshape(B * C)
        zero = (x * 0.0).reshape(B * S, D)
        upd = _scatter_add(zero, flat_idx, y.reshape(B * C, D), axis=0)
        return x + upd.reshape(B, S, D)

    x = A_x
    xn = _layernorm(x, axis=-1)
    s = _A_ref(xn, A_k_cache, A_v_cache, A_wq, A_wo)
    g = _silu(_B_ref(xn, B_w_r, B_w_gate, B_w_up, B_w_down))
    return x + s * g
```

```cpp
#include <hip/hip_runtime.h>
#include <hip/hip_bf16.h>
#include <stdint.h>

typedef unsigned u32; typedef unsigned long long u64; typedef unsigned short u16;
typedef u32 u32x4 __attribute__((ext_vector_type(4)));
typedef u32 u32x2 __attribute__((ext_vector_type(2)));
typedef float f32x4 __attribute__((ext_vector_type(4)));
typedef float f32x2 __attribute__((ext_vector_type(2)));
typedef __bf16 bf16x2 __attribute__((ext_vector_type(2)));
typedef __attribute__((address_space(1))) u64 gu64;
typedef __attribute__((address_space(1))) u32 gu32;
#define DI __device__ __forceinline__
#define RLX __ATOMIC_RELAXED, __HIP_MEMORY_SCOPE_AGENT

constexpr u32 WS_E = 0, WS_FW = 0x400, WS_FO = 0x9000, WS_G1 = 0x1000, WS_G2 = 0x5000, WS_YP = 0x10000, WS_O = 0x200000, WS_WT = 0x1200000, WS_DBG = 0x3000000;
constexpr u32 L_GT = 0x5000, L_UP = 0x7000, L_DN = 0x9000, L_XT = 0xB000, L_G1 = 0xD000, L_XS = 0xD400, L_GS = 0xE400, L_SC = 0xE800, L_PT = 0x18000, WS_SQ = 0x1800000;
constexpr int SPIN = 200000;
constexpr int NFF = 172;
constexpr int LDS_BYTES = 163840;

DI u32 pk(float lo, float hi) { f32x2 v = {lo, hi}; bf16x2 b = __builtin_convertvector(v, bf16x2); return __builtin_bit_cast(u32, b); }
DI float lo16(u32 w) { return __uint_as_float(w << 16); }
DI float hi16(u32 w) { return __uint_as_float(w & 0xffff0000u); }
DI float rbf(float x) { return lo16(pk(x, 0.f)); }
template <int C> DI float dpp(float v) { return __int_as_float(__builtin_amdgcn_update_dpp(0, __float_as_int(v), C, 0xf, 0xf, false)); }
DI float sum16(float v) { v += dpp<0x128>(v); v += dpp<0x124>(v); v += dpp<0x122>(v); v += dpp<0x121>(v); return v; }
DI float sum32(float v) { v = sum16(v); auto r = __builtin_amdgcn_permlane16_swap(__float_as_uint(v), __float_as_uint(v), false, false); return __uint_as_float(r[0]) + __uint_as_float(r[1]); }
DI float sum64(float v) { v = sum32(v); auto r = __builtin_amdgcn_permlane32_swap(__float_as_uint(v), __float_as_uint(v), false, false); return __uint_as_float(r[0]) + __uint_as_float(r[1]); }
DI float rcp(float x) { return __builtin_amdgcn_rcpf(x); }
DI float xnorm(float x, float mb, float rsd) { return rbf(rbf(x - mb) * rsd); }
DI float silu(float a) { return a * rcp(1.f + __expf(-a)); }
DI u32 lane_id() { u32 l; asm volatile("v_mbcnt_lo_u32_b32 %0, -1, 0\n\tv_mbcnt_hi_u32_b32 %0, -1, %0" : "=v"(l)); return l; }

DI void slow_rows(char* lds, const u16* x, const u16* wq, const u16* K, const u16* V, char* ws, u32 h, u32 b, u32 wid, u32 lane) {
  const u32 row = 256 * b + 64 * wid + lane;
  const u32 st = (u32)__hip_atomic_load((gu64*)(ws + WS_G1) + row, RLX);
  const float mb = lo16(st), sd = rcp(hi16(st));
  u32* q = (u32*)(ws + WS_SQ) + ((b * 32 + h) * 256 + 64 * wid + lane) * 64;
  asm volatile("s_waitcnt vmcnt(0)" ::: "memory");
#pragma unroll 1
  for (int dc = 0; dc < 16; ++dc) {
    float a0 = 0, a1 = 0, a2 = 0, a3 = 0, a4 = 0, a5 = 0, a6 = 0, a7 = 0;
#pragma unroll 1
    for (int k = 0; k < 512; ++k) {
      const float xv = xnorm(lo16(x[row * 512 + k]), mb, sd);
      const u32x4 w = *(const u32x4*)(wq + (size_t)k * 4096 + h * 128 + dc * 8);
      a0 += xv * lo16(w[0]); a1 += xv * hi16(w[0]); a2 += xv * lo16(w[1]); a3 += xv * hi16(w[1]);
      a4 += xv * lo16(w[2]); a5 += xv * hi16(w[2]); a6 += xv * lo16(w[3]); a7 += xv * hi16(w[3]);
    }
    q[4 * dc] = pk(a0, a1); q[4 * dc + 1] = pk(a2, a3); q[4 * dc + 2] = pk(a4, a5); q[4 * dc + 3] = pk(a6, a7);
  }
  const u32* Kh = (const u32*)(K + (size_t)h * 262144); const u32* Vh = (const u32*)(V + (size_t)h * 262144);
  char* O = ws + WS_O + (size_t)((row >> 4) * 32 + h) * 4096 + (row & 15) * 16;
#pragma unroll 1
  for (int dc = 0; dc < 8; ++dc) {
    float m = -__builtin_inff(), l = 0; float o[16];
#pragma unroll
    for (int j = 0; j < 16; ++j) o[j] = 0;
#pragma unroll 1
    for (int key = 0; key < 2048; ++key) {
      float s = 0;
#pragma unroll 4
      for (int d = 0; d < 64; ++d) { const u32 kk = Kh[key * 64 + d], qq = q[d]; s += lo16(qq) * lo16(kk) + hi16(qq) * hi16(kk); }
      s *= 0.12751743082f;
      const float mn = fmaxf(m, s), al = exp2f(m - mn), p = exp2f(s - mn);
      m = mn; l = l * al + p;
#pragma unroll
      for (int j = 0; j < 8; ++j) { const u32 vv = Vh[key * 64 + dc * 8 + j]; o[2 * j] = o[2 * j] * al + p * lo16(vv); o[2 * j + 1] = o[2 * j + 1] * al + p * hi16(vv); }
    }
    const float il = 1.f / l;
#pragma unroll
    for (int g = 0; g < 4; ++g)
      __hip_atomic_store((gu64*)(O + (dc >> 1) * 1024 + g * 256 + (dc & 1) * 8), ((u64)pk(o[4 * g + 2] * il, o[4 * g + 3] * il) << 32) | pk(o[4 * g] * il, o[4 * g + 1] * il), RLX);
  }
  asm volatile("s_waitcnt vmcnt(0)" ::: "memory");
}


DI u64 granule(const char* lds, u32 loff, char* ws, u32 goff, u32 tag) {
  u64 g = *(const u64*)(lds + loff);
  for (int spin = 0; (u32)(g >> 32) != tag && spin < SPIN; ++spin) g = __hip_atomic_load((gu64*)(ws + goff), RLX);
  return g;
}
typedef const __attribute__((address_space(4))) u64* kptr;
DI kptr again(kptr p) { asm volatile("" : "+s"(p)); return p; }
template <class P> DI P* gp(u64 v) { return (P*)(__attribute__((address_space(1))) P*)v; }
DI u32 rkey(u32 v, u32 row) { return (((v & 0x8000u) ? (~v & 0xffffu) : (v | 0x8000u)) << 16) | (2047u - row); }
DI void bar() { asm volatile("s_waitcnt lgkmcnt(0)\n\ts_barrier" ::: "memory"); }

__global__ __launch_bounds__(256, 1) void mha_mod_kernel(const u16* __restrict__ x, const u16* __restrict__ K, const u16* __restrict__ V, const u16* __restrict__ wq, const u16* __restrict__ wo,
                                                         const u16* __restrict__ wr, const u16* __restrict__ wg, const u16* __restrict__ wu, const u16* __restrict__ wd, char* ws, u16* __restrict__ out) {
  extern __shared__ __attribute__((aligned(16))) char lds[];
  const u32 bx = blockIdx.x, wid = __builtin_amdgcn_readfirstlane(threadIdx.x >> 6);
  const u32 h = 4 * (bx & 7) + ((bx >> 3) & 3), b = bx >> 5;
  kptr ka = (kptr)__builtin_amdgcn_kernarg_segment_ptr();
  u32 tag;
  const u32 fj = (bx >> 3) < 24 ? 24 * (bx & 7) + (bx >> 3) : 255;
  const bool ffn = fj < NFF;
  u32 bad, best;
  {
    asm volatile(
    "s_nop 4\n\t"
    "s_load_dwordx16 s[40:55], %[ka], 0x0\n\t"
    "s_load_dwordx4 s[56:59], %[ka], 0x40\n\t"
    "s_waitcnt lgkmcnt(0)\n\t"
    "v_mbcnt_lo_u32_b32 v255, -1, 0\n\t"
    "v_mbcnt_hi_u32_b32 v255, -1, v255\n\t"
    "s_mul_i32 s33, %[bx], 8\n\t"
    "s_mul_i32 s34, %[wid], 2\n\t"
    "s_add_u32 s33, s33, s34\n\t"
    "v_lshrrev_b32_e32 v40, 5, v255\n\t"
    "v_add_u32_e32 v40, s33, v40\n\t"
    "v_and_b32_e32 v41, 31, v255\n\t"
    "v_lshlrev_b32_e32 v41, 4, v41\n\t"
    "v_lshl_add_u32 v42, v40, 10, v41\n\t"
    "v_mov_b32_e32 v43, 0\n\t"
    "global_load_dwordx4 v[24:27], v42, s[40:41]\n\t"
    "global_load_dwordx4 v[28:31], v42, s[40:41] offset:512\n\t"
    "global_load_dword v13, v43, s[58:59] sc1\n\t"
    "global_load_dwordx4 v[32:35], v41, s[50:51]\n\t"
    "global_load_dwordx4 v[36:39], v41, s[50:51] offset:512\n\t"
    "s_getpc_b64 s[60:61]\n\t"
    "Lmha_begin:\n\t"
    "s_and_b32 s61, s61, 0xffff\n\t"
    "s_and_b32 s60, s60, 0xffffff80\n\t"
    "s_mov_b32 s62, Lmha_end-Lmha_begin\n\t"
    "s_mov_b32 s63, 0x20000\n\t"
    "s_mul_i32 s33, %[pj], 4096\n\t"
    "s_mul_i32 s34, %[wid], 1024\n\t"
    "s_add_u32 s33, s33, s34\n\t"
    "s_mov_b32 m0, 0x18000\n\t"
    "v_lshlrev_b32_e32 v43, 4, v255\n\t"
    "buffer_load_dwordx4 v43, s[60:63], s33 offen lds\n\t"
    "s_lshl_b32 s96, %[wid], 12\n\t"
    "s_lshl_b32 s97, %[wid], 13\n\t"
    "s_mul_i32 s33, %[b], 262144\n\t"
    "s_mul_i32 s34, %[wid], 65536\n\t"
    "s_add_u32 s33, s33, s34\n\t"
    "s_add_u32 s60, s40, s33\n\t"
    "s_addc_u32 s61, s41, 0\n\t"
    "s_and_b32 s61, s61, 0xffff\n\t"
    "s_mov_b32 s62, 65536\n\t"
    "s_mov_b32 s63, 0x20000\n\t"
    "s_mul_i32 s33, %[h], 256\n\t"
    "s_add_u32 s64, s46, s33\n\t"
    "s_addc_u32 s65, s47, 0\n\t"
    "s_sub_u32 s64, s64, 4096\n\t"
    "s_subb_u32 s65, s65, 0\n\t"
    "s_and_b32 s65, s65, 0xffff\n\t"
    "s_mov_b32 s66, 0\n\t"
    "s_mov_b32 s67, 0x20000\n\t"
    "s_sub_u32 s66, 4198400, s33\n\t"
    "s_mul_i32 s33, %[h], 524288\n\t"
    "s_add_u32 s68, s42, s33\n\t"
    "s_addc_u32 s69, s43, 0\n\t"
    "s_and_b32 s69, s69, 0xffff\n\t"
    "s_mov_b32 s70, 524288\n\t"
    "s_mov_b32 s71, 0x20000\n\t"
    "s_mul_i32 s33, %[h], 524288\n\t"
    "s_add_u32 s72, s44, s33\n\t"
    "s_addc_u32 s73, s45, 0\n\t"
    "s_sub_u32 s72, s72, 1024\n\t"
    "s_subb_u32 s73, s73, 0\n\t"
    "s_and_b32 s73, s73, 0xffff\n\t"
    "s_mov_b32 s74, 525312\n\t"
    "s_mov_b32 s75, 0x20000\n\t"
    "s_add_u32 s90, s96, 114688\n\t"
    "s_add_u32 s93, s96, 98304\n\t"
    "s_add_u32 s91, s96, 131072\n\t"
    "s_add_u32 s94, s96, 81920\n\t"
    "s_add_u32 s92, s96, 147456\n\t"
    "s_add_u32 s95, s96, 65536\n\t"
    "v_mbcnt_lo_u32_b32 v255, -1, 0\n\t"
    "v_mbcnt_hi_u32_b32 v255, -1, v255\n\t"
    "v_lshrrev_b32_e32 v241, 4, v255\n\t"
    "v_and_b32_e32 v242, 7, v255\n\t"
    "v_xor_b32_e32 v242, v242, v241\n\t"
    "v_lshrrev_b32_e32 v243, 3, v255\n\t"
    "v_lshlrev_b32_e32 v243, 10, v243\n\t"
    "v_lshl_add_u32 v8, v242, 4, v243\n\t"
    "v_xor_b32_e32 v9, 64, v8\n\t"
    "v_bfe_u32 v241, v255, 2, 3\n\t"
    "v_and_b32_e32 v242, 3, v255\n\t"
    "v_lshlrev_b32_e32 v242, 4, v242\n\t"
    "v_lshrrev_b32_e32 v243, 5, v255\n\t"
    "v_lshl_add_u32 v242, v243, 6, v242\n\t"
    "v_lshl_add_u32 v10, v241, 13, v242\n\t"
    "s_lshl_b32 s87, %[wid], 17\n\t"
    "v_add_u32_e32 v10, s87, v10\n\t"
    "v_lshl_add_u32 v237, v241, 8, v242\n\t"
    "v_add_u32_e32 v237, s96, v237\n\t"
    "v_add_u32_e32 v238, 0x80, v237\n\t"
    "v_add_u32_e32 v237, 0x400, v237\n\t"
    "v_lshrrev_b32_e32 v241, 4, v255\n\t"
    "v_and_b32_e32 v242, 15, v255\n\t"
    "v_xor_b32_e32 v242, v242, v241\n\t"
    "v_lshlrev_b32_e32 v241, 8, v241\n\t"
    "v_lshl_add_u32 v242, v242, 4, v241\n\t"
    "v_add_u32_e32 v233, s96, v242\n\t"
    "v_xor_b32_e32 v234, 64, v233\n\t"
    "v_xor_b32_e32 v235, 128, v233\n\t"
    "v_xor_b32_e32 v236, 192, v233\n\t"
    "v_bfe_u32 v240, v255, 1, 3\n\t"
    "v_lshrrev_b32_e32 v241, 4, v255\n\t"
    "v_lshlrev_b32_e32 v241, 5, v241\n\t"
    "v_and_b32_e32 v242, 1, v255\n\t"
    "v_lshl_add_u32 v241, v242, 4, v241\n\t"
    "v_lshl_add_u32 v237, v240, 8, v241\n\t"
    "v_add_u32_e32 v237, s96, v237\n\t"
    "v_add_u32_e32 v238, 0x80, v237\n\t"
    "v_add_u32_e32 v237, 0x400, v237\n\t"
    "v_lshl_add_u32 v11, v240, 10, v241\n\t"
    "v_lshlrev_b32_e32 v40, 3, v40\n\t"
    "s_add_u32 m0, s96, 0\n\t"
    "s_mov_b32 s87, 4096\n\t"
    "buffer_load_dwordx4 v10, s[64:67], s87 offen offset:0 lds\n\t"
    "s_mov_b32 s88, 3200\n\t"
    "buffer_load_dwordx4 v10, s[64:67], s88 offen offset:1024 lds\n\t"
    "s_mov_b32 s87, 67584\n\t"
    "buffer_load_dwordx4 v10, s[64:67], s87 offen offset:2048 lds\n\t"
    "s_mov_b32 s88, 66688\n\t"
    "buffer_load_dwordx4 v10, s[64:67], s88 offen offset:3072 lds\n\t"
    "s_add_u32 m0, s97, 16384\n\t"
    "s_mov_b32 s87, 0\n\t"
    "buffer_load_dwordx4 v8, s[60:63], s87 offen offset:0 lds\n\t"
    "s_mov_b32 s88, 7168\n\t"
    "buffer_load_dwordx4 v9, s[60:63], s88 offen offset:1024 lds\n\t"
    "s_mov_b32 s87, 14336\n\t"
    "buffer_load_dwordx4 v8, s[60:63], s87 offen offset:2048 lds\n\t"
    "s_mov_b32 s88, 21504\n\t"
    "buffer_load_dwordx4 v9, s[60:63], s88 offen offset:3072 lds\n\t"
    "s_add_u32 m0, s97, 20480\n\t"
    "s_mov_b32 s87, 32768\n\t"
    "buffer_load_dwordx4 v8, s[60:63], s87 offen offset:0 lds\n\t"
    "s_mov_b32 s88, 39936\n\t"
    "buffer_load_dwordx4 v9, s[60:63], s88 offen offset:1024 lds\n\t"
    "s_mov_b32 s87, 47104\n\t"
    "buffer_load_dwordx4 v8, s[60:63], s87 offen offset:2048 lds\n\t"
    "s_mov_b32 s88, 54272\n\t"
    "buffer_load_dwordx4 v9, s[60:63], s88 offen offset:3072 lds\n\t"
    "s_mul_i32 s33, %[b], 2048\n\t"
    "s_mul_i32 s34, %[wid], 512\n\t"
    "s_add_u32 s33, s33, s34\n\t"
    "s_add_u32 s33, s33, 4096\n\t"
    "s_add_u32 s80, s58, s33\n\t"
    "s_addc_u32 s81, s59, 0\n\t"
    "s_and_b32 s81, s81, 0xffff\n\t"
    "s_mov_b32 s82, 512\n\t"
    "s_mov_b32 s83, 0x20000\n\t"
    "s_mul_i32 s33, %[h], 131072\n\t"
    "s_mul_i32 s34, %[wid], 32768\n\t"
    "s_add_u32 s33, s33, s34\n\t"
    "s_mul_i32 s34, %[b], 128\n\t"
    "s_add_u32 s33, s33, s34\n\t"
    "s_add_u32 s76, s48, s33\n\t"
    "s_addc_u32 s77, s49, 0\n\t"
    "s_and_b32 s77, s77, 0xffff\n\t"
    "s_mov_b32 s78, 32768\n\t"
    "s_mov_b32 s79, 0x20000\n\t"
    "s_mov_b32 s98, 0x67800000\n\t"
    "s_add_u32 m0, s96, 49152\n\t"
    "v_and_b32_e32 v240, 15, v255\n\t"
    "v_lshrrev_b32_e32 v241, 4, v255\n\t"
    "v_xor_b32_e32 v242, v240, v241\n\t"
    "v_lshlrev_b32_e32 v242, 4, v242\n\t"
    "v_lshl_add_u32 v242, v240, 8, v242\n\t"
    "s_mov_b32 s87, 528384\n\t"
    "buffer_load_dwordx4 v10, s[64:67], s87 offen offset:0 lds\n\t"
    "v_add_u32_e32 v228, 114688, v242\n\t"
    "v_xor_b32_e32 v229, 64, v228\n\t"
    "v_xor_b32_e32 v230, 128, v228\n\t"
    "v_xor_b32_e32 v231, 192, v228\n\t"
    "v_cmp_eq_u32_e32 vcc, 0, v240\n\t"
    "s_mov_b32 s88, 527488\n\t"
    "buffer_load_dwordx4 v10, s[64:67], s88 offen offset:1024 lds\n\t"
    "v_mov_b32_e32 v242, 0x3f803f80\n\t"
    "s_nop 1\n\t"
    "v_cndmask_b32_e32 v224, 0, v242, vcc\n\t"
    "v_cndmask_b32_e32 v225, 0, v242, vcc\n\t"
    "v_cndmask_b32_e32 v226, 0, v242, vcc\n\t"
    "s_mov_b32 s87, 591872\n\t"
    "buffer_load_dwordx4 v10, s[64:67], s87 offen offset:2048 lds\n\t"
    "v_cndmask_b32_e32 v227, 0, v242, vcc\n\t"
    "v_and_b32_e32 v242, 3, v255\n\t"
    "v_lshlrev_b32_e32 v242, 3, v242\n\t"
    "v_bfe_u32 v243, v255, 2, 3\n\t"
    "v_lshl_add_u32 v242, v243, 5, v242\n\t"
    "s_mov_b32 s88, 590976\n\t"
    "buffer_load_dwordx4 v10, s[64:67], s88 offen offset:3072 lds\n\t"
    "v_lshrrev_b32_e32 v241, 5, v255\n\t"
    "v_lshl_add_u32 v232, v241, 11, v242\n\t"
    "v_add_u32_e32 v232, 65536, v232\n\t"
    "v_lshl_add_u32 v23, v241, 10, v242\n\t"
    "v_add_u32_e32 v23, s96, v23\n\t"
    "s_add_u32 m0, s97, 65536\n\t"
    "s_mov_b32 s87, 128\n\t"
    "buffer_load_dwordx4 v8, s[60:63], s87 offen offset:0 lds\n\t"
    "v_and_b32_e32 v240, 31, v255\n\t"
    "v_bfe_u32 v242, v255, 1, 3\n\t"
    "v_xor_b32_e32 v242, v242, v241\n\t"
    "s_lshl_b32 s86, %[wid], 1\n\t"
    "v_xor_b32_e32 v242, s86, v242\n\t"
    "s_mov_b32 s88, 7296\n\t"
    "buffer_load_dwordx4 v9, s[60:63], s88 offen offset:1024 lds\n\t"
    "v_lshlrev_b32_e32 v242, 4, v242\n\t"
    "v_lshl_add_u32 v242, v240, 7, v242\n\t"
    "s_add_u32 s86, s97, 0x4000\n\t"
    "v_add_u32_e32 v4, s86, v242\n\t"
    "v_xor_b32_e32 v5, 32, v4\n\t"
    "s_mov_b32 s87, 14464\n\t"
    "buffer_load_dwordx4 v8, s[60:63], s87 offen offset:2048 lds\n\t"
    "v_xor_b32_e32 v6, 64, v4\n\t"
    "v_xor_b32_e32 v7, 96, v4\n\t"
    "v_bfe_u32 v242, v255, 2, 2\n\t"
    "v_lshlrev_b32_e32 v242, 6, v242\n\t"
    "v_bfe_u32 v243, v255, 4, 1\n\t"
    "s_mov_b32 s88, 21632\n\t"
    "buffer_load_dwordx4 v9, s[60:63], s88 offen offset:3072 lds\n\t"
    "v_lshl_add_u32 v242, v243, 4, v242\n\t"
    "v_and_b32_e32 v243, 1, v255\n\t"
    "v_lshl_add_u32 v242, v243, 5, v242\n\t"
    "v_bfe_u32 v243, v255, 1, 1\n\t"
    "v_lshl_add_u32 v242, v243, 3, v242\n\t"
    "s_add_u32 m0, s97, 69632\n\t"
    "s_mov_b32 s87, 32896\n\t"
    "buffer_load_dwordx4 v8, s[60:63], s87 offen offset:0 lds\n\t"
    "v_lshl_add_u32 v242, v241, 11, v242\n\t"
    "v_add_u32_e32 v0, s96, v242\n\t"
    "v_xor_b32_e32 v1, 4096, v0\n\t"
    "v_xor_b32_e32 v2, 8192, v0\n\t"
    "v_xor_b32_e32 v3, 12288, v0\n\t"
    "s_mov_b32 s88, 40064\n\t"
    "buffer_load_dwordx4 v9, s[60:63], s88 offen offset:1024 lds\n\t"
    "v_and_b32_e32 v242, 31, v255\n\t"
    "v_cmp_eq_u32_e32 vcc, 0, v242\n\t"
    "v_cmp_eq_u32_e64 s[34:35], 17, v242\n\t"
    "s_or_b64 vcc, vcc, s[34:35]\n\t"
    "v_mov_b32_e32 v242, 0x3f803f80\n\t"
    "s_mov_b32 s87, 47232\n\t"
    "buffer_load_dwordx4 v8, s[60:63], s87 offen offset:2048 lds\n\t"
    "s_nop 1\n\t"
    "v_cndmask_b32_e32 v44, 0, v242, vcc\n\t"
    "v_cndmask_b32_e32 v45, 0, v242, vcc\n\t"
    "v_cndmask_b32_e32 v46, 0, v242, vcc\n\t"
    "v_cndmask_b32_e32 v47, 0, v242, vcc\n\t"
    "s_mov_b32 s88, 54400\n\t"
    "buffer_load_dwordx4 v9, s[60:63], s88 offen offset:3072 lds\n\t"
    "v_lshlrev_b32_e32 v20, 3, v240\n\t"
    "v_lshlrev_b32_e32 v21, 4, v255\n\t"
    "s_lshl_b32 s86, %[wid], 9\n\t"
    "v_add_u32_e32 v22, s86, v21\n\t"
    "v_lshlrev_b32_e32 v12, 4, v240\n\t"
    "v_lshl_add_u32 v12, v241, 10, v12\n\t"
    "s_waitcnt vmcnt(12)\n\t"
    "v_readfirstlane_b32 %[tag], v13\n\t"
    "s_barrier\n\t"
    "s_add_u32 %[tag], %[tag], 1\n\t"
    "s_mov_b32 s34, 1\n\t"
    "s_mov_b32 s35, 1\n\t"
    "s_mov_b32 s84, 4096\n\t"
    "s_mov_b32 s85, 20480\n\t"
    "s_mov_b64 s[52:53], s[58:59]\n\t"
    "s_and_b32 s53, s53, 0xffff\n\t"
    "s_mov_b32 s54, 0x10000\n\t"
    "s_mov_b32 s55, 0x20000\n\t"
    "ds_read_b128 a[96:99], v4 offset:0\n\t"
    "ds_read_b128 a[112:115], v4 offset:4096\n\t"
    "ds_read_b64_tr_b16 v[192:193], v0 offset:0\n\t"
    "ds_read_b64_tr_b16 v[194:195], v0 offset:256\n\t"
    "s_add_u32 m0, s96, 114688\n\t"
    "ds_read_b64_tr_b16 v[196:197], v0 offset:512\n\t"
    "ds_read_b64_tr_b16 v[198:199], v0 offset:768\n\t"
    "ds_read_b64_tr_b16 v[200:201], v0 offset:1024\n\t"
    "ds_read_b64_tr_b16 v[202:203], v0 offset:1280\n\t"
    "s_mov_b32 s87, 1052672\n\t"
    "buffer_load_dwordx4 v10, s[64:67], s87 offen offset:0 lds\n\t"
    "ds_read_b64_tr_b16 v[204:205], v0 offset:1536\n\t"
    "ds_read_b64_tr_b16 v[206:207], v0 offset:1792\n\t"
    "ds_read_b128 a[100:103], v5 offset:0\n\t"
    "ds_read_b128 a[116:119], v5 offset:4096\n\t"
    "s_mov_b32 s88, 1051776\n\t"
    "buffer_load_dwordx4 v10, s[64:67], s88 offen offset:1024 lds\n\t"
    "ds_read_b64_tr_b16 a[0:1], v1 offset:0\n\t"
    "ds_read_b64_tr_b16 a[2:3], v1 offset:256\n\t"
    "ds_read_b64_tr_b16 a[4:5], v1 offset:512\n\t"
    "ds_read_b64_tr_b16 a[6:7], v1 offset:768\n\t"
    "s_mov_b32 s87, 1116160\n\t"
    "buffer_load_dwordx4 v10, s[64:67], s87 offen offset:2048 lds\n\t"
    "ds_read_b64_tr_b16 a[8:9], v1 offset:1024\n\t"
    "ds_read_b64_tr_b16 a[10:11], v1 offset:1280\n\t"
    "ds_read_b64_tr_b16 a[12:13], v1 offset:1536\n\t"
    "ds_read_b64_tr_b16 a[14:15], v1 offset:1792\n\t"
    "s_mov_b32 s88, 1115264\n\t"
    "buffer_load_dwordx4 v10, s[64:67], s88 offen offset:3072 lds\n\t"
    "ds_read_b128 a[104:107], v6 offset:0\n\t"
    "ds_read_b128 a[120:123], v6 offset:4096\n\t"
    "ds_read_b64_tr_b16 a[16:17], v2 offset:0\n\t"
    "ds_read_b64_tr_b16 a[18:19], v2 offset:256\n\t"
    "s_add_u32 m0, s97, 131072\n\t"
    "s_mov_b32 s87, 256\n\t"
    "buffer_load_dwordx4 v8, s[60:63], s87 offen offset:0 lds\n\t"
    "ds_read_b64_tr_b16 a[20:21], v2 offset:512\n\t"
    "ds_read_b64_tr_b16 a[22:23], v2 offset:768\n\t"
    "ds_read_b64_tr_b16 a[24:25], v2 offset:1024\n\t"
    "ds_read_b64_tr_b16 a[26:27], v2 offset:1280\n\t"
    "s_mov_b32 s88, 7424\n\t"
    "buffer_load_dwordx4 v9, s[60:63], s88 offen offset:1024 lds\n\t"
    "ds_read_b64_tr_b16 a[28:29], v2 offset:1536\n\t"
    "ds_read_b64_tr_b16 a[30:31], v2 offset:1792\n\t"
    "ds_read_b128 a[108:111], v7 offset:0\n\t"
    "ds_read_b128 a[124:127], v7 offset:4096\n\t"
    "s_mov_b32 s87, 14592\n\t"
    "buffer_load_dwordx4 v8, s[60:63], s87 offen offset:2048 lds\n\t"
    "ds_read_b64_tr_b16 a[32:33], v3 offset:0\n\t"
    "ds_read_b64_tr_b16 a[34:35], v3 offset:256\n\t"
    "ds_read_b64_tr_b16 a[36:37], v3 offset:512\n\t"
    "ds_read_b64_tr_b16 a[38:39], v3 offset:768\n\t"
    "s_mov_b32 s88, 21760\n\t"
    "buffer_load_dwordx4 v9, s[60:63], s88 offen offset:3072 lds\n\t"
    "ds_read_b64_tr_b16 a[40:41], v3 offset:1024\n\t"
    "ds_read_b64_tr_b16 a[42:43], v3 offset:1280\n\t"
    "ds_read_b64_tr_b16 a[44:45], v3 offset:1536\n\t"
    "ds_read_b64_tr_b16 a[46:47], v3 offset:1792\n\t"
    "s_add_u32 m0, s97, 135168\n\t"
    "s_mov_b32 s87, 33024\n\t"
    "buffer_load_dwordx4 v8, s[60:63], s87 offen offset:0 lds\n\t"
    "s_mov_b32 s88, 40192\n\t"
    "buffer_load_dwordx4 v9, s[60:63], s88 offen offset:1024 lds\n\t"
    "s_mov_b32 s87, 47360\n\t"
    "buffer_load_dwordx4 v8, s[60:63], s87 offen offset:2048 lds\n\t"
    "s_mov_b32 s88, 54528\n\t"
    "buffer_load_dwordx4 v9, s[60:63], s88 offen offset:3072 lds\n\t"
    "s_waitcnt vmcnt(12) lgkmcnt(0)\n\t"
    "s_barrier\n\t"
    "v_mfma_f32_32x32x16_bf16 v[64:79], v[192:195], a[96:99], 0\n\t"
    "v_add_u32_e32 v0, 0xc000, v0\n\t"
    "v_add_u32_e32 v1, 0xc000, v1\n\t"
    "v_add_u32_e32 v2, 0xc000, v2\n\t"
    "v_add_u32_e32 v3, 0xc000, v3\n\t"
    "v_lshlrev_b32_e32 v240, 16, v24\n\t"
    "v_and_b32_e32 v241, 0xffff0000, v24\n\t"
    "v_mfma_f32_32x32x16_bf16 v[80:95], v[192:195], a[112:115], 0\n\t"
    "v_add_u32_e32 v4, 0xc000, v4\n\t"
    "v_add_u32_e32 v5, 0xc000, v5\n\t"
    "v_add_u32_e32 v6, 0xc000, v6\n\t"
    "v_add_u32_e32 v7, 0xc000, v7\n\t"
    "v_lshlrev_b32_e32 v242, 16, v25\n\t"
    "v_and_b32_e32 v243, 0xffff0000, v25\n\t"
    "s_add_u32 m0, s96, 0\n\t"
    "v_mfma_f32_32x32x16_bf16 v[96:111], v[196:199], a[96:99], 0\n\t"
    "ds_read_b128 a[128:131], v4 offset:0\n\t"
    "ds_read_b128 a[144:147], v4 offset:4096\n\t"
    "v_lshlrev_b32_e32 v244, 16, v26\n\t"
    "v_and_b32_e32 v245, 0xffff0000, v26\n\t"
    "v_mfma_f32_32x32x16_bf16 v[112:127], v[196:199], a[112:115], 0\n\t"
    "ds_read_b64_tr_b16 v[208:209], v0 offset:0\n\t"
    "ds_read_b64_tr_b16 v[210:211], v0 offset:256\n\t"
    "v_lshlrev_b32_e32 v246, 16, v27\n\t"
    "v_and_b32_e32 v247, 0xffff0000, v27\n\t"
    "s_mov_b32 s87, 1576960\n\t"
    "buffer_load_dwordx4 v10, s[64:67], s87 offen offset:0 lds\n\t"
    "v_mfma_f32_32x32x16_bf16 v[128:143], v[200:203], a[96:99], 0\n\t"
    "ds_read_b64_tr_b16 v[212:213], v0 offset:512\n\t"
    "v_lshlrev_b32_e32 v248, 16, v28\n\t"
    "v_and_b32_e32 v249, 0xffff0000, v28\n\t"
    "v_mfma_f32_32x32x16_bf16 v[144:159], v[200:203], a[112:115], 0\n\t"
    "ds_read_b64_tr_b16 v[214:215], v0 offset:768\n\t"
    "ds_read_b64_tr_b16 v[216:217], v0 offset:1024\n\t"
    "v_lshlrev_b32_e32 v250, 16, v29\n\t"
    "v_and_b32_e32 v251, 0xffff0000, v29\n\t"
    "s_mov_b32 s88, 1576064\n\t"
    "buffer_load_dwordx4 v10, s[64:67], s88 offen offset:1024 lds\n\t"
    "v_mfma_f32_32x32x16_bf16 v[160:175], v[204:207], a[96:99], 0\n\t"
    "ds_read_b64_tr_b16 v[218:219], v0 offset:1280\n\t"
    "v_lshlrev_b32_e32 v16, 16, v30\n\t"
    "v_and_b32_e32 v17, 0xffff0000, v30\n\t"
    "v_mfma_f32_32x32x16_bf16 v[176:191], v[204:207], a[112:115], 0\n\t"
    "ds_read_b64_tr_b16 v[220:221], v0 offset:1536\n\t"
    "ds_read_b64_tr_b16 v[222:223], v0 offset:1792\n\t"
    "v_lshlrev_b32_e32 v18, 16, v31\n\t"
    "v_and_b32_e32 v19, 0xffff0000, v31\n\t"
    "v_mfma_f32_32x32x16_bf16 v[64:79], a[0:3], a[100:103], v[64:79]\n\t"
    "ds_read_b128 a[132:135], v5 offset:0\n\t"
    "v_add_f32_e32 v41, v240, v241\n\t"
    "v_add_f32_e32 v41, v41, v242\n\t"
    "s_mov_b32 s87, 1640448\n\t"
    "buffer_load_dwordx4 v10, s[64:67], s87 offen offset:2048 lds\n\t"
    "v_mfma_f32_32x32x16_bf16 v[80:95], a[0:3], a[116:119], v[80:95]\n\t"
    "ds_read_b128 a[148:151], v5 offset:4096\n\t"
    "ds_read_b64_tr_b16 a[48:49], v1 offset:0\n\t"
    "v_mfma_f32_16x16x32_bf16 v[48:51], v[44:47], v[192:195], 0\n\t"
    "v_add_f32_e32 v41, v41, v243\n\t"
    "v_add_f32_e32 v41, v41, v244\n\t"
    "v_mfma_f32_32x32x16_bf16 v[96:111], a[4:7], a[100:103], v[96:111]\n\t"
    "ds_read_b64_tr_b16 a[50:51], v1 offset:256\n\t"
    "v_add_f32_e32 v41, v41, v245\n\t"
    "s_mov_b32 s88, 1639552\n\t"
    "buffer_load_dwordx4 v10, s[64:67], s88 offen offset:3072 lds\n\t"
    "v_mfma_f32_32x32x16_bf16 v[112:127], a[4:7], a[116:119], v[112:127]\n\t"
    "ds_read_b64_tr_b16 a[52:53], v1 offset:512\n\t"
    "ds_read_b64_tr_b16 a[54:55], v1 offset:768\n\t"
    "v_mfma_f32_16x16x32_bf16 v[52:55], v[44:47], v[196:199], 0\n\t"
    "v_add_f32_e32 v41, v41, v246\n\t"
    "v_add_f32_e32 v41, v41, v247\n\t"
    "v_mfma_f32_32x32x16_bf16 v[128:143], a[8:11], a[100:103], v[128:143]\n\t"
    "ds_read_b64_tr_b16 a[56:57], v1 offset:1024\n\t"
    "v_add_f32_e32 v41, v41, v248\n\t"
    "v_add_f32_e32 v41, v41, v249\n\t"
    "s_add_u32 m0, s97, 16384\n\t"
    "s_mov_b32 s87, 384\n\t"
    "buffer_load_dwordx4 v8, s[60:63], s87 offen offset:0 lds\n\t"
    "v_mfma_f32_32x32x16_bf16 v[144:159], a[8:11], a[116:119], v[144:159]\n\t"
    "ds_read_b64_tr_b16 a[58:59], v1 offset:1280\n\t"
    "ds_read_b64_tr_b16 a[60:61], v1 offset:1536\n\t"
    "v_mfma_f32_16x16x32_bf16 v[56:59], v[44:47], v[200:203], 0\n\t"
    "v_add_f32_e32 v41, v41, v250\n\t"
    "v_add_f32_e32 v41, v41, v251\n\t"
    "v_mfma_f32_32x32x16_bf16 v[160:175], a[12:15], a[100:103], v[160:175]\n\t"
    "ds_read_b64_tr_b16 a[62:63], v1 offset:1792\n\t"
    "v_add_f32_e32 v41, v41, v16\n\t"
    "v_add_f32_e32 v41, v41, v17\n\t"
    "v_mfma_f32_32x32x16_bf16 v[176:191], a[12:15], a[116:119], v[176:191]\n\t"
    "ds_read_b128 a[136:139], v6 offset:0\n\t"
    "ds_read_b128 a[152:155], v6 offset:4096\n\t"
    "v_mfma_f32_16x16x32_bf16 v[60:63], v[44:47], v[204:207], 0\n\t"
    "v_add_f32_e32 v41, v41, v18\n\t"
    "v_add_f32_e32 v41, v41, v19\n\t"
    "s_mov_b32 s88, 7552\n\t"
    "buffer_load_dwordx4 v9, s[60:63], s88 offen offset:1024 lds\n\t"
    "v_mfma_f32_32x32x16_bf16 v[64:79], a[16:19], a[104:107], v[64:79]\n\t"
    "ds_read_b64_tr_b16 a[64:65], v2 offset:0\n\t"
    "ds_read_b64_tr_b16 a[66:67], v2 offset:256\n\t"
    "s_nop 1\n\t"
    "v_add_f32_dpp v41, v41, v41 row_ror:8 row_mask:0xf bank_mask:0xf\n\t"
    "s_nop 1\n\t"
    "v_add_f32_dpp v41, v41, v41 row_ror:4 row_mask:0xf bank_mask:0xf\n\t"
    "v_mfma_f32_32x32x16_bf16 v[80:95], a[16:19], a[120:123], v[80:95]\n\t"
    "ds_read_b64_tr_b16 a[68:69], v2 offset:512\n\t"
    "s_nop 1\n\t"
    "v_add_f32_dpp v41, v41, v41 row_ror:2 row_mask:0xf bank_mask:0xf\n\t"
    "s_nop 1\n\t"
    "v_add_f32_dpp v41, v41, v41 row_ror:1 row_mask:0xf bank_mask:0xf\n\t"
    "s_mov_b32 s87, 14720\n\t"
    "buffer_load_dwordx4 v8, s[60:63], s87 offen offset:2048 lds\n\t"
    "v_mfma_f32_32x32x16_bf16 v[96:111], a[20:23], a[104:107], v[96:111]\n\t"
    "ds_read_b64_tr_b16 a[70:71], v2 offset:768\n\t"
    "ds_read_b64_tr_b16 a[72:73], v2 offset:1024\n\t"
    "v_mov_b32_e32 v42, v41\n\t"
    "s_nop 1\n\t"
    "v_permlane16_swap_b32_e32 v41, v42\n\t"
    "v_mfma_f32_32x32x16_bf16 v[112:127], a[20:23], a[120:123], v[112:127]\n\t"
    "ds_read_b64_tr_b16 a[74:75], v2 offset:1280\n\t"
    "v_add_f32_e32 v41, v41, v42\n\t"
    "v_mul_f32_e32 v13, 0x3b000000, v41\n\t"
    "v_mfma_f32_32x32x16_bf16 v[128:143], a[24:27], a[104:107], v[128:143]\n\t"
    "ds_read_b64_tr_b16 a[76:77], v2 offset:1536\n\t"
    "ds_read_b64_tr_b16 a[78:79], v2 offset:1792\n\t"
    "v_mov_b32_e32 v41, 0\n\t"
    "v_sub_f32_e32 v43, v240, v13\n\t"
    "s_mov_b32 s88, 21888\n\t"
    "buffer_load_dwordx4 v9, s[60:63], s88 offen offset:3072 lds\n\t"
    "v_mfma_f32_32x32x16_bf16 v[144:159], a[24:27], a[120:123], v[144:159]\n\t"
    "ds_read_b128 a[140:143], v7 offset:0\n\t"
    "v_fmac_f32_e32 v41, v43, v43\n\t"
    "v_mfma_f32_32x32x16_bf16 v[160:175], a[28:31], a[104:107], v[160:175]\n\t"
    "ds_read_b128 a[156:159], v7 offset:4096\n\t"
    "ds_read_b64_tr_b16 a[80:81], v3 offset:0\n\t"
    "v_sub_f32_e32 v42, v241, v13\n\t"
    "v_fmac_f32_e32 v41, v42, v42\n\t"
    "s_add_u32 m0, s97, 20480\n\t"
    "s_mov_b32 s87, 33152\n\t"
    "buffer_load_dwordx4 v8, s[60:63], s87 offen offset:0 lds\n\t"
    "v_mfma_f32_32x32x16_bf16 v[176:191], a[28:31], a[120:123], v[176:191]\n\t"
    "ds_read_b64_tr_b16 a[82:83], v3 offset:256\n\t"
    "v_sub_f32_e32 v43, v242, v13\n\t"
    "v_fmac_f32_e32 v41, v43, v43\n\t"
    "v_mfma_f32_32x32x16_bf16 v[64:79], a[32:35], a[108:111], v[64:79]\n\t"
    "ds_read_b64_tr_b16 a[84:85], v3 offset:512\n\t"
    "ds_read_b64_tr_b16 a[86:87], v3 offset:768\n\t"
    "v_sub_f32_e32 v42, v243, v13\n\t"
    "v_fmac_f32_e32 v41, v42, v42\n\t"
    "s_mov_b32 s88, 40320\n\t"
    "buffer_load_dwordx4 v9, s[60:63], s88 offen offset:1024 lds\n\t"
    "v_mfma_f32_32x32x16_bf16 v[80:95], a[32:35], a[124:127], v[80:95]\n\t"
    "ds_read_b64_tr_b16 a[88:89], v3 offset:1024\n\t"
    "v_sub_f32_e32 v43, v244, v13\n\t"
    "v_fmac_f32_e32 v41, v43, v43\n\t"
    "v_mfma_f32_32x32x16_bf16 v[96:111], a[36:39], a[108:111], v[96:111]\n\t"
    "ds_read_b64_tr_b16 a[90:91], v3 offset:1280\n\t"
    "ds_read_b64_tr_b16 a[92:93], v3 offset:1536\n\t"
    "v_sub_f32_e32 v42, v245, v13\n\t"
    "v_fmac_f32_e32 v41, v42, v42\n\t"
    "v_mfma_f32_32x32x16_bf16 v[112:127], a[36:39], a[124:127], v[112:127]\n\t"
    "ds_read_b64_tr_b16 a[94:95], v3 offset:1792\n\t"
    "v_sub_f32_e32 v43, v246, v13\n\t"
    "v_fmac_f32_e32 v41, v43, v43\n\t"
    "s_mov_b32 s87, 47488\n\t"
    "buffer_load_dwordx4 v8, s[60:63], s87 offen offset:2048 lds\n\t"
    "v_mfma_f32_32x32x16_bf16 v[128:143], a[40:43], a[108:111], v[128:143]\n\t"
    "v_sub_f32_e32 v42, v247, v13\n\t"
    "v_fmac_f32_e32 v41, v42, v42\n\t"
    "v_mfma_f32_32x32x16_bf16 v[144:159], a[40:43], a[124:127], v[144:159]\n\t"
    "v_sub_f32_e32 v43, v248, v13\n\t"
    "v_fmac_f32_e32 v41, v43, v43\n\t"
    "s_mov_b32 s88, 54656\n\t"
    "buffer_load_dwordx4 v9, s[60:63], s88 offen offset:3072 lds\n\t"
    "v_mfma_f32_32x32x16_bf16 v[160:175], a[44:47], a[108:111], v[160:175]\n\t"
    "v_sub_f32_e32 v42, v249, v13\n\t"
    "v_fmac_f32_e32 v41, v42, v42\n\t"
    "v_mfma_f32_32x32x16_bf16 v[176:191], a[44:47], a[124:127], v[176:191]\n\t"
    "v_sub_f32_e32 v43, v250, v13\n\t"
    "s_waitcnt vmcnt(12) lgkmcnt(0)\n\t"
    "s_barrier\n\t"
    "v_mfma_f32_32x32x16_bf16 v[64:79], v[208:211], a[128:131], v[64:79]\n\t"
    "v_add_u32_e32 v0, 0x10000, v0\n\t"
    "v_add_u32_e32 v1, 0x10000, v1\n\t"
    "v_add_u32_e32 v2, 0x10000, v2\n\t"
    "v_add_u32_e32 v3, 0x10000, v3\n\t"
    "v_fmac_f32_e32 v41, v43, v43\n\t"
    "v_sub_f32_e32 v42, v251, v13\n\t"
    "v_mfma_f32_32x32x16_bf16 v[80:95], v[208:211], a[144:147], v[80:95]\n\t"
    "v_add_u32_e32 v4, 0x10000, v4\n\t"
    "v_add_u32_e32 v5, 0x10000, v5\n\t"
    "v_add_u32_e32 v6, 0x10000, v6\n\t"
    "v_add_u32_e32 v7, 0x10000, v7\n\t"
    "v_fmac_f32_e32 v41, v42, v42\n\t"
    "v_sub_f32_e32 v43, v16, v13\n\t"
    "s_add_u32 m0, s96, 49152\n\t"
    "v_mfma_f32_32x32x16_bf16 v[96:111], v[212:215], a[128:131], v[96:111]\n\t"
    "ds_read_b128 a[96:99], v4 offset:0\n\t"
    "ds_read_b128 a[112:115], v4 offset:4096\n\t"
    "v_fmac_f32_e32 v41, v43, v43\n\t"
    "v_sub_f32_e32 v42, v17, v13\n\t"
    "v_mfma_f32_32x32x16_bf16 v[112:127], v[212:215], a[144:147], v[112:127]\n\t"
    "ds_read_b64_tr_b16 v[192:193], v0 offset:0\n\t"
    "ds_read_b64_tr_b16 v[194:195], v0 offset:256\n\t"
    "v_fmac_f32_e32 v41, v42, v42\n\t"
    "v_sub_f32_e32 v43, v18, v13\n\t"
    "s_mov_b32 s87, 2101248\n\t"
    "buffer_load_dwordx4 v10, s[64:67], s87 offen offset:0 lds\n\t"
    "v_mfma_f32_32x32x16_bf16 v[128:143], v[216:219], a[128:131], v[128:143]\n\t"
    "ds_read_b64_tr_b16 v[196:197], v0 offset:512\n\t"
    "v_fmac_f32_e32 v41, v43, v43\n\t"
    "v_sub_f32_e32 v42, v19, v13\n\t"
    "v_mfma_f32_32x32x16_bf16 v[144:159], v[216:219], a[144:147], v[144:159]\n\t"
    "ds_read_b64_tr_b16 v[198:199], v0 offset:768\n\t"
    "ds_read_b64_tr_b16 v[200:201], v0 offset:1024\n\t"
    "v_fmac_f32_e32 v41, v42, v42\n\t"
    "s_nop 1\n\t"
    "v_add_f32_dpp v41, v41, v41 row_ror:8 row_mask:0xf bank_mask:0xf\n\t"
    "s_mov_b32 s88, 2100352\n\t"
    "buffer_load_dwordx4 v10, s[64:67], s88 offen offset:1024 lds\n\t"
    "v_mfma_f32_32x32x16_bf16 v[160:175], v[220:223], a[128:131], v[160:175]\n\t"
    "ds_read_b64_tr_b16 v[202:203], v0 offset:1280\n\t"
    "s_nop 1\n\t"
    "v_add_f32_dpp v41, v41, v41 row_ror:4 row_mask:0xf bank_mask:0xf\n\t"
    "s_nop 1\n\t"
    "v_add_f32_dpp v41, v41, v41 row_ror:2 row_mask:0xf bank_mask:0xf\n\t"
    "v_mfma_f32_32x32x16_bf16 v[176:191], v[220:223], a[144:147], v[176:191]\n\t"
    "ds_read_b64_tr_b16 v[204:205], v0 offset:1536\n\t"
    "ds_read_b64_tr_b16 v[206:207], v0 offset:1792\n\t"
    "s_nop 1\n\t"
    "v_add_f32_dpp v41, v41, v41 row_ror:1 row_mask:0xf bank_mask:0xf\n\t"
    "v_mov_b32_e32 v42, v41\n\t"
    "v_mfma_f32_32x32x16_bf16 v[64:79], a[48:51], a[132:135], v[64:79]\n\t"
    "ds_read_b128 a[100:103], v5 offset:0\n\t"
    "s_nop 1\n\t"
    "v_permlane16_swap_b32_e32 v41, v42\n\t"
    "v_add_f32_e32 v41, v41, v42\n\t"
    "s_mov_b32 s87, 2164736\n\t"
    "buffer_load_dwordx4 v10, s[64:67], s87 offen offset:2048 lds\n\t"
    "v_mfma_f32_32x32x16_bf16 v[80:95], a[48:51], a[148:151], v[80:95]\n\t"
    "ds_read_b128 a[116:119], v5 offset:4096\n\t"
    "ds_read_b64_tr_b16 a[0:1], v1 offset:0\n\t"
    "v_mfma_f32_16x16x32_bf16 v[48:51], v[44:47], v[208:211], v[48:51]\n\t"
    "v_mul_f32_e32 v41, 0x3b000000, v41\n\t"
    "v_cvt_pk_bf16_f32 v41, v13, v41\n\t"
    "v_mfma_f32_32x32x16_bf16 v[96:111], a[52:55], a[132:135], v[96:111]\n\t"
    "ds_read_b64_tr_b16 a[2:3], v1 offset:256\n\t"
    "v_lshlrev_b32_e32 v13, 16, v41\n\t"
    "s_mov_b32 s88, 2163840\n\t"
    "buffer_load_dwordx4 v10, s[64:67], s88 offen offset:3072 lds\n\t"
    "v_mfma_f32_32x32x16_bf16 v[112:127], a[52:55], a[148:151], v[112:127]\n\t"
    "ds_read_b64_tr_b16 a[4:5], v1 offset:512\n\t"
    "ds_read_b64_tr_b16 a[6:7], v1 offset:768\n\t"
    "v_mfma_f32_16x16x32_bf16 v[52:55], v[44:47], v[212:215], v[52:55]\n\t"
    "v_and_b32_e32 v41, 0xffff0000, v41\n\t"
    "v_add_f32_e32 v41, 0x3727c5ac, v41\n\t"
    "v_mfma_f32_32x32x16_bf16 v[128:143], a[56:59], a[132:135], v[128:143]\n\t"
    "ds_read_b64_tr_b16 a[8:9], v1 offset:1024\n\t"
    "v_cvt_pk_bf16_f32 v41, v41, v41\n\t"
    "v_and_b32_e32 v41, 0xffff0000, v41\n\t"
    "s_add_u32 m0, s97, 65536\n\t"
    "s_mov_b32 s87, 512\n\t"
    "buffer_load_dwordx4 v8, s[60:63], s87 offen offset:0 lds\n\t"
    "v_mfma_f32_32x32x16_bf16 v[144:159], a[56:59], a[148:151], v[144:159]\n\t"
    "ds_read_b64_tr_b16 a[10:11], v1 offset:1280\n\t"
    "ds_read_b64_tr_b16 a[12:13], v1 offset:1536\n\t"
    "v_mfma_f32_16x16x32_bf16 v[56:59], v[44:47], v[216:219], v[56:59]\n\t"
    "v_sqrt_f32_e32 v41, v41\n\t"
    "s_nop 0\n\t"
    "v_cvt_pk_bf16_f32 v14, v13, v41\n\t"
    "v_mfma_f32_32x32x16_bf16 v[160:175], a[60:63], a[132:135], v[160:175]\n\t"
    "ds_read_b64_tr_b16 a[14:15], v1 offset:1792\n\t"
    "v_and_b32_e32 v41, 0xffff0000, v14\n\t"
    "v_rcp_f32_e32 v41, v41\n\t"
    "v_mfma_f32_32x32x16_bf16 v[176:191], a[60:63], a[148:151], v[176:191]\n\t"
    "ds_read_b128 a[104:107], v6 offset:0\n\t"
    "ds_read_b128 a[120:123], v6 offset:4096\n\t"
    "v_mfma_f32_16x16x32_bf16 v[60:63], v[44:47], v[220:223], v[60:63]\n\t"
    "v_mov_b32_e32 v15, %[tag]\n\t"
    "s_mov_b64 exec, s[34:35]\n\t"
    "buffer_store_dwordx2 v[14:15], v40, s[52:55], s84 offen sc1\n\t"
    "s_mov_b64 exec, -1\n\t"
    "s_mov_b32 s88, 7680\n\t"
    "buffer_load_dwordx4 v9, s[60:63], s88 offen offset:1024 lds\n\t"
    "v_mfma_f32_32x32x16_bf16 v[64:79], a[64:67], a[136:139], v[64:79]\n\t"
    "ds_read_b64_tr_b16 a[16:17], v2 offset:0\n\t"
    "ds_read_b64_tr_b16 a[18:19], v2 offset:256\n\t"
    "v_sub_f32_e32 v240, v240, v13\n\t"
    "v_sub_f32_e32 v241, v241, v13\n\t"
    "v_mfma_f32_32x32x16_bf16 v[80:95], a[64:67], a[152:155], v[80:95]\n\t"
    "ds_read_b64_tr_b16 a[20:21], v2 offset:512\n\t"
    "v_sub_f32_e32 v242, v242, v13\n\t"
    "v_sub_f32_e32 v243, v243, v13\n\t"
    "s_mov_b32 s87, 14848\n\t"
    "buffer_load_dwordx4 v8, s[60:63], s87 offen offset:2048 lds\n\t"
    "v_mfma_f32_32x32x16_bf16 v[96:111], a[68:71], a[136:139], v[96:111]\n\t"
    "ds_read_b64_tr_b16 a[22:23], v2 offset:768\n\t"
    "ds_read_b64_tr_b16 a[24:25], v2 offset:1024\n\t"
    "v_sub_f32_e32 v244, v244, v13\n\t"
    "v_sub_f32_e32 v245, v245, v13\n\t"
    "v_mfma_f32_32x32x16_bf16 v[112:127], a[68:71], a[152:155], v[112:127]\n\t"
    "ds_read_b64_tr_b16 a[26:27], v2 offset:1280\n\t"
    "v_sub_f32_e32 v246, v246, v13\n\t"
    "v_sub_f32_e32 v247, v247, v13\n\t"
    "v_mfma_f32_32x32x16_bf16 v[128:143], a[72:75], a[136:139], v[128:143]\n\t"
    "ds_read_b64_tr_b16 a[28:29], v2 offset:1536\n\t"
    "ds_read_b64_tr_b16 a[30:31], v2 offset:1792\n\t"
    "v_sub_f32_e32 v248, v248, v13\n\t"
    "v_sub_f32_e32 v249, v249, v13\n\t"
    "s_mov_b32 s88, 22016\n\t"
    "buffer_load_dwordx4 v9, s[60:63], s88 offen offset:3072 lds\n\t"
    "v_mfma_f32_32x32x16_bf16 v[144:159], a[72:75], a[152:155], v[144:159]\n\t"
    "ds_read_b128 a[108:111], v7 offset:0\n\t"
    "v_sub_f32_e32 v250, v250, v13\n\t"
    "v_mfma_f32_32x32x16_bf16 v[160:175], a[76:79], a[136:139], v[160:175]\n\t"
    "ds_read_b128 a[124:127], v7 offset:4096\n\t"
    "ds_read_b64_tr_b16 a[32:33], v3 offset:0\n\t"
    "v_sub_f32_e32 v251, v251, v13\n\t"
    "v_sub_f32_e32 v16, v16, v13\n\t"
    "s_add_u32 m0, s97, 69632\n\t"
    "s_mov_b32 s87, 33280\n\t"
    "buffer_load_dwordx4 v8, s[60:63], s87 offen offset:0 lds\n\t"
    "v_mfma_f32_32x32x16_bf16 v[176:191], a[76:79], a[152:155], v[176:191]\n\t"
    "ds_read_b64_tr_b16 a[34:35], v3 offset:256\n\t"
    "v_sub_f32_e32 v17, v17, v13\n\t"
    "v_sub_f32_e32 v18, v18, v13\n\t"
    "v_mfma_f32_32x32x16_bf16 v[64:79], a[80:83], a[140:143], v[64:79]\n\t"
    "ds_read_b64_tr_b16 a[36:37], v3 offset:512\n\t"
    "ds_read_b64_tr_b16 a[38:39], v3 offset:768\n\t"
    "v_sub_f32_e32 v19, v19, v13\n\t"
    "v_cvt_pk_bf16_f32 v24, v240, v241\n\t"
    "s_mov_b32 s88, 40448\n\t"
    "buffer_load_dwordx4 v9, s[60:63], s88 offen offset:1024 lds\n\t"
    "v_mfma_f32_32x32x16_bf16 v[80:95], a[80:83], a[156:159], v[80:95]\n\t"
    "ds_read_b64_tr_b16 a[40:41], v3 offset:1024\n\t"
    "v_cvt_pk_bf16_f32 v25, v242, v243\n\t"
    "v_cvt_pk_bf16_f32 v26, v244, v245\n\t"
    "v_mfma_f32_32x32x16_bf16 v[96:111], a[84:87], a[140:143], v[96:111]\n\t"
    "ds_read_b64_tr_b16 a[42:43], v3 offset:1280\n\t"
    "ds_read_b64_tr_b16 a[44:45], v3 offset:1536\n\t"
    "v_cvt_pk_bf16_f32 v27, v246, v247\n\t"
    "v_cvt_pk_bf16_f32 v28, v248, v249\n\t"
    "v_mfma_f32_32x32x16_bf16 v[112:127], a[84:87], a[156:159], v[112:127]\n\t"
    "ds_read_b64_tr_b16 a[46:47], v3 offset:1792\n\t"
    "v_cvt_pk_bf16_f32 v29, v250, v251\n\t"
    "v_cvt_pk_bf16_f32 v30, v16, v17\n\t"
    "s_mov_b32 s87, 47616\n\t"
    "buffer_load_dwordx4 v8, s[60:63], s87 offen offset:2048 lds\n\t"
    "v_mfma_f32_32x32x16_bf16 v[128:143], a[88:91], a[140:143], v[128:143]\n\t"
    "v_cvt_pk_bf16_f32 v31, v18, v19\n\t"
    "v_lshlrev_b32_e32 v240, 16, v24\n\t"
    "v_mfma_f32_32x32x16_bf16 v[144:159], a[88:91], a[156:159], v[144:159]\n\t"
    "v_and_b32_e32 v241, 0xffff0000, v24\n\t"
    "v_lshlrev_b32_e32 v242, 16, v25\n\t"
    "s_mov_b32 s88, 54784\n\t"
    "buffer_load_dwordx4 v9, s[60:63], s88 offen offset:3072 lds\n\t"
    "v_mfma_f32_32x32x16_bf16 v[160:175], a[92:95], a[140:143], v[160:175]\n\t"
    "v_and_b32_e32 v243, 0xffff0000, v25\n\t"
    "v_lshlrev_b32_e32 v244, 16, v26\n\t"
    "v_mfma_f32_32x32x16_bf16 v[176:191], a[92:95], a[156:159], v[176:191]\n\t"
    "v_and_b32_e32 v245, 0xffff0000, v26\n\t"
    "s_waitcnt vmcnt(13) lgkmcnt(0)\n\t"
    "s_barrier\n\t"
    "v_mfma_f32_32x32x16_bf16 v[64:79], v[192:195], a[96:99], v[64:79]\n\t"
    "v_add_u32_e32 v0, 0xfffe4000, v0\n\t"
    "v_add_u32_e32 v1, 0xfffe4000, v1\n\t"
    "v_add_u32_e32 v2, 0xfffe4000, v2\n\t"
    "v_add_u32_e32 v3, 0xfffe4000, v3\n\t"
    "v_lshlrev_b32_e32 v246, 16, v27\n\t"
    "v_and_b32_e32 v247, 0xffff0000, v27\n\t"
    "v_mfma_f32_32x32x16_bf16 v[80:95], v[192:195], a[112:115], v[80:95]\n\t"
    "v_add_u32_e32 v4, 0xfffe4000, v4\n\t"
    "v_add_u32_e32 v5, 0xfffe4000, v5\n\t"
    "v_add_u32_e32 v6, 0xfffe4000, v6\n\t"
    "v_add_u32_e32 v7, 0xfffe4000, v7\n\t"
    "v_lshlrev_b32_e32 v248, 16, v28\n\t"
    "v_and_b32_e32 v249, 0xffff0000, v28\n\t"
    "s_add_u32 m0, s96, 114688\n\t"
    "v_mfma_f32_32x32x16_bf16 v[96:111], v[196:199], a[96:99], v[96:111]\n\t"
    "ds_read_b128 a[128:131], v4 offset:0\n\t"
    "ds_read_b128 a[144:147], v4 offset:4096\n\t"
    "v_lshlrev_b32_e32 v250, 16, v29\n\t"
    "v_and_b32_e32 v251, 0xffff0000, v29\n\t"
    "v_mfma_f32_32x32x16_bf16 v[112:127], v[196:199], a[112:115], v[112:127]\n\t"
    "ds_read_b64_tr_b16 v[208:209], v0 offset:0\n\t"
    "ds_read_b64_tr_b16 v[210:211], v0 offset:256\n\t"
    "v_lshlrev_b32_e32 v16, 16, v30\n\t"
    "v_and_b32_e32 v17, 0xffff0000, v30\n\t"
    "s_mov_b32 s87, 2625536\n\t"
    "buffer_load_dwordx4 v10, s[64:67], s87 offen offset:0 lds\n\t"
    "v_mfma_f32_32x32x16_bf16 v[128:143], v[200:203], a[96:99], v[128:143]\n\t"
    "ds_read_b64_tr_b16 v[212:213], v0 offset:512\n\t"
    "v_lshlrev_b32_e32 v18, 16, v31\n\t"
    "v_and_b32_e32 v19, 0xffff0000, v31\n\t"
    "v_mfma_f32_32x32x16_bf16 v[144:159], v[200:203], a[112:115], v[144:159]\n\t"
    "ds_read_b64_tr_b16 v[214:215], v0 offset:768\n\t"
    "ds_read_b64_tr_b16 v[216:217], v0 offset:1024\n\t"
    "v_mul_f32_e32 v240, v240, v41\n\t"
    "v_mul_f32_e32 v241, v241, v41\n\t"
    "s_mov_b32 s88, 2624640\n\t"
    "buffer_load_dwordx4 v10, s[64:67], s88 offen offset:1024 lds\n\t"
    "v_mfma_f32_32x32x16_bf16 v[160:175], v[204:207], a[96:99], v[160:175]\n\t"
    "ds_read_b64_tr_b16 v[218:219], v0 offset:1280\n\t"
    "v_mul_f32_e32 v242, v242, v41\n\t"
    "v_mul_f32_e32 v243, v243, v41\n\t"
    "v_mfma_f32_32x32x16_bf16 v[176:191], v[204:207], a[112:115], v[176:191]\n\t"
    "ds_read_b64_tr_b16 v[220:221], v0 offset:1536\n\t"
    "ds_read_b64_tr_b16 v[222:223], v0 offset:1792\n\t"
    "v_mul_f32_e32 v244, v244, v41\n\t"
    "v_mul_f32_e32 v245, v245, v41\n\t"
    "v_mfma_f32_32x32x16_bf16 v[64:79], a[0:3], a[100:103], v[64:79]\n\t"
    "ds_read_b128 a[132:135], v5 offset:0\n\t"
    "v_mul_f32_e32 v246, v246, v41\n\t"
    "v_mul_f32_e32 v247, v247, v41\n\t"
    "s_mov_b32 s87, 2689024\n\t"
    "buffer_load_dwordx4 v10, s[64:67], s87 offen offset:2048 lds\n\t"
    "v_mfma_f32_32x32x16_bf16 v[80:95], a[0:3], a[116:119], v[80:95]\n\t"
    "ds_read_b128 a[148:151], v5 offset:4096\n\t"
    "ds_read_b64_tr_b16 a[48:49], v1 offset:0\n\t"
    "v_mfma_f32_16x16x32_bf16 v[48:51], v[44:47], v[192:195], v[48:51]\n\t"
    "v_mul_f32_e32 v248, v248, v41\n\t"
    "v_mul_f32_e32 v249, v249, v41\n\t"
    "v_mfma_f32_32x32x16_bf16 v[96:111], a[4:7], a[100:103], v[96:111]\n\t"
    "ds_read_b64_tr_b16 a[50:51], v1 offset:256\n\t"
    "v_mul_f32_e32 v250, v250, v41\n\t"
    "s_mov_b32 s88, 2688128\n\t"
    "buffer_load_dwordx4 v10, s[64:67], s88 offen offset:3072 lds\n\t"
    "v_mfma_f32_32x32x16_bf16 v[112:127], a[4:7], a[116:119], v[112:127]\n\t"
    "ds_read_b64_tr_b16 a[52:53], v1 offset:512\n\t"
    "ds_read_b64_tr_b16 a[54:55], v1 offset:768\n\t"
    "v_mfma_f32_16x16x32_bf16 v[52:55], v[44:47], v[196:199], v[52:55]\n\t"
    "v_mul_f32_e32 v251, v251, v41\n\t"
    "v_mul_f32_e32 v16, v16, v41\n\t"
    "v_mfma_f32_32x32x16_bf16 v[128:143], a[8:11], a[100:103], v[128:143]\n\t"
    "ds_read_b64_tr_b16 a[56:57], v1 offset:1024\n\t"
    "v_mul_f32_e32 v17, v17, v41\n\t"
    "v_mul_f32_e32 v18, v18, v41\n\t"
    "s_add_u32 m0, s97, 131072\n\t"
    "s_mov_b32 s87, 640\n\t"
    "buffer_load_dwordx4 v8, s[60:63], s87 offen offset:0 lds\n\t"
    "v_mfma_f32_32x32x16_bf16 v[144:159], a[8:11], a[116:119], v[144:159]\n\t"
    "ds_read_b64_tr_b16 a[58:59], v1 offset:1280\n\t"
    "ds_read_b64_tr_b16 a[60:61], v1 offset:1536\n\t"
    "v_mfma_f32_16x16x32_bf16 v[56:59], v[44:47], v[200:203], v[56:59]\n\t"
    "v_mul_f32_e32 v19, v19, v41\n\t"
    "v_cvt_pk_bf16_f32 v24, v240, v241\n\t"
    "v_mfma_f32_32x32x16_bf16 v[160:175], a[12:15], a[100:103], v[160:175]\n\t"
    "ds_read_b64_tr_b16 a[62:63], v1 offset:1792\n\t"
    "v_cvt_pk_bf16_f32 v25, v242, v243\n\t"
    "v_cvt_pk_bf16_f32 v26, v244, v245\n\t"
    "v_mfma_f32_32x32x16_bf16 v[176:191], a[12:15], a[116:119], v[176:191]\n\t"
    "ds_read_b128 a[136:139], v6 offset:0\n\t"
    "ds_read_b128 a[152:155], v6 offset:4096\n\t"
    "v_mfma_f32_16x16x32_bf16 v[60:63], v[44:47], v[204:207], v[60:63]\n\t"
    "v_cvt_pk_bf16_f32 v27, v246, v247\n\t"
    "v_cvt_pk_bf16_f32 v28, v248, v249\n\t"
    "s_mov_b32 s88, 7808\n\t"
    "buffer_load_dwordx4 v9, s[60:63], s88 offen offset:1024 lds\n\t"
    "v_mfma_f32_32x32x16_bf16 v[64:79], a[16:19], a[104:107], v[64:79]\n\t"
    "ds_read_b64_tr_b16 a[64:65], v2 offset:0\n\t"
    "ds_read_b64_tr_b16 a[66:67], v2 offset:256\n\t"
    "v_cvt_pk_bf16_f32 v29, v250, v251\n\t"
    "v_cvt_pk_bf16_f32 v30, v16, v17\n\t"
    "v_mfma_f32_32x32x16_bf16 v[80:95], a[16:19], a[120:123], v[80:95]\n\t"
    "ds_read_b64_tr_b16 a[68:69], v2 offset:512\n\t"
    "v_cvt_pk_bf16_f32 v31, v18, v19\n\t"
    "v_mov_b32_e32 v41, 0\n\t"
    "s_mov_b32 s87, 14976\n\t"
    "buffer_load_dwordx4 v8, s[60:63], s87 offen offset:2048 lds\n\t"
    "v_mfma_f32_32x32x16_bf16 v[96:111], a[20:23], a[104:107], v[96:111]\n\t"
    "ds_read_b64_tr_b16 a[70:71], v2 offset:768\n\t"
    "ds_read_b64_tr_b16 a[72:73], v2 offset:1024\n\t"
    "v_dot2_f32_bf16 v41, v24, v32, v41\n\t"
    "v_dot2_f32_bf16 v41, v25, v33, v41\n\t"
    "v_mfma_f32_32x32x16_bf16 v[112:127], a[20:23], a[120:123], v[112:127]\n\t"
    "ds_read_b64_tr_b16 a[74:75], v2 offset:1280\n\t"
    "v_dot2_f32_bf16 v41, v26, v34, v41\n\t"
    "v_dot2_f32_bf16 v41, v27, v35, v41\n\t"
    "v_mfma_f32_32x32x16_bf16 v[128:143], a[24:27], a[104:107], v[128:143]\n\t"
    "ds_read_b64_tr_b16 a[76:77], v2 offset:1536\n\t"
    "ds_read_b64_tr_b16 a[78:79], v2 offset:1792\n\t"
    "v_dot2_f32_bf16 v41, v28, v36, v41\n\t"
    "v_dot2_f32_bf16 v41, v29, v37, v41\n\t"
    "s_mov_b32 s88, 22144\n\t"
    "buffer_load_dwordx4 v9, s[60:63], s88 offen offset:3072 lds\n\t"
    "v_mfma_f32_32x32x16_bf16 v[144:159], a[24:27], a[120:123], v[144:159]\n\t"
    "ds_read_b128 a[140:143], v7 offset:0\n\t"
    "v_dot2_f32_bf16 v41, v30, v38, v41\n\t"
    "v_mfma_f32_32x32x16_bf16 v[160:175], a[28:31], a[104:107], v[160:175]\n\t"
    "ds_read_b128 a[156:159], v7 offset:4096\n\t"
    "ds_read_b64_tr_b16 a[80:81], v3 offset:0\n\t"
    "v_dot2_f32_bf16 v41, v31, v39, v41\n\t"
    "s_nop 1\n\t"
    "s_add_u32 m0, s97, 135168\n\t"
    "s_mov_b32 s87, 33408\n\t"
    "buffer_load_dwordx4 v8, s[60:63], s87 offen offset:0 lds\n\t"
    "v_mfma_f32_32x32x16_bf16 v[176:191], a[28:31], a[120:123], v[176:191]\n\t"
    "ds_read_b64_tr_b16 a[82:83], v3 offset:256\n\t"
    "s_nop 1\n\t"
    "v_add_f32_dpp v41, v41, v41 row_ror:8 row_mask:0xf bank_mask:0xf\n\t"
    "s_nop 1\n\t"
    "v_add_f32_dpp v41, v41, v41 row_ror:4 row_mask:0xf bank_mask:0xf\n\t"
    "v_mfma_f32_32x32x16_bf16 v[64:79], a[32:35], a[108:111], v[64:79]\n\t"
    "ds_read_b64_tr_b16 a[84:85], v3 offset:512\n\t"
    "ds_read_b64_tr_b16 a[86:87], v3 offset:768\n\t"
    "s_nop 1\n\t"
    "v_add_f32_dpp v41, v41, v41 row_ror:2 row_mask:0xf bank_mask:0xf\n\t"
    "s_nop 1\n\t"
    "v_add_f32_dpp v41, v41, v41 row_ror:1 row_mask:0xf bank_mask:0xf\n\t"
    "s_mov_b32 s88, 40576\n\t"
    "buffer_load_dwordx4 v9, s[60:63], s88 offen offset:1024 lds\n\t"
    "v_mfma_f32_32x32x16_bf16 v[80:95], a[32:35], a[124:127], v[80:95]\n\t"
    "ds_read_b64_tr_b16 a[88:89], v3 offset:1024\n\t"
    "v_mov_b32_e32 v42, v41\n\t"
    "s_nop 1\n\t"
    "v_permlane16_swap_b32_e32 v41, v42\n\t"
    "v_mfma_f32_32x32x16_bf16 v[96:111], a[36:39], a[108:111], v[96:111]\n\t"
    "ds_read_b64_tr_b16 a[90:91], v3 offset:1280\n\t"
    "ds_read_b64_tr_b16 a[92:93], v3 offset:1536\n\t"
    "v_add_f32_e32 v41, v41, v42\n\t"
    "v_cvt_pk_bf16_f32 v41, v41, v41\n\t"
    "v_mfma_f32_32x32x16_bf16 v[112:127], a[36:39], a[124:127], v[112:127]\n\t"
    "ds_read_b64_tr_b16 a[94:95], v3 offset:1792\n\t"
    "v_ashrrev_i32_e32 v42, 31, v41\n\t"
    "v_or_b32_e32 v42, 0x80000000, v42\n\t"
    "s_mov_b32 s87, 47744\n\t"
    "buffer_load_dwordx4 v8, s[60:63], s87 offen offset:2048 lds\n\t"
    "v_mfma_f32_32x32x16_bf16 v[128:143], a[40:43], a[108:111], v[128:143]\n\t"
    "v_xor_b32_e32 v41, v41, v42\n\t"
    "v_and_b32_e32 v41, 0xffff0000, v41\n\t"
    "v_mfma_f32_32x32x16_bf16 v[144:159], a[40:43], a[124:127], v[144:159]\n\t"
    "v_lshrrev_b32_e32 v42, 3, v40\n\t"
    "v_sub_u32_e32 v42, 0x7ff, v42\n\t"
    "s_mov_b32 s88, 54912\n\t"
    "buffer_load_dwordx4 v9, s[60:63], s88 offen offset:3072 lds\n\t"
    "v_mfma_f32_32x32x16_bf16 v[160:175], a[44:47], a[108:111], v[160:175]\n\t"
    "v_or_b32_e32 v14, v41, v42\n\t"
    "s_mov_b64 exec, s[34:35]\n\t"
    "buffer_store_dwordx2 v[14:15], v40, s[52:55], s85 offen sc1\n\t"
    "s_mov_b64 exec, -1\n\t"
    "v_mfma_f32_32x32x16_bf16 v[176:191], a[44:47], a[124:127], v[176:191]\n\t"
    "s_waitcnt vmcnt(13) lgkmcnt(0)\n\t"
    "s_barrier\n\t"
    "v_mfma_f32_32x32x16_bf16 v[64:79], v[208:211], a[128:131], v[64:79]\n\t"
    "v_add_u32_e32 v0, 0xc000, v0\n\t"
    "v_add_u32_e32 v1, 0xc000, v1\n\t"
    "v_add_u32_e32 v2, 0xc000, v2\n\t"
    "v_add_u32_e32 v3, 0xc000, v3\n\t"
    "v_mfma_f32_32x32x16_bf16 v[80:95], v[208:211], a[144:147], v[80:95]\n\t"
    "v_add_u32_e32 v4, 0xc000, v4\n\t"
    "v_add_u32_e32 v5, 0xc000, v5\n\t"
    "v_add_u32_e32 v6, 0xc000, v6\n\t"
    "v_add_u32_e32 v7, 0xc000, v7\n\t"
    "s_add_u32 m0, s96, 0\n\t"
    "v_mfma_f32_32x32x16_bf16 v[96:111], v[212:215], a[128:131], v[96:111]\n\t"
    "ds_read_b128 a[96:99], v4 offset:0\n\t"
    "ds_read_b128 a[112:115], v4 offset:4096\n\t"
    "v_mfma_f32_32x32x16_bf16 v[112:127], v[212:215], a[144:147], v[112:127]\n\t"
    "ds_read_b64_tr_b16 v[192:193], v0 offset:0\n\t"
    "ds_read_b64_tr_b16 v[194:195], v0 offset:256\n\t"
    "s_mov_b32 s87, 3149824\n\t"
    "buffer_load_dwordx4 v10, s[64:67], s87 offen offset:0 lds\n\t"
    "v_mfma_f32_32x32x16_bf16 v[128:143], v[216:219], a[128:131], v[128:143]\n\t"
    "ds_read_b64_tr_b16 v[196:197], v0 offset:512\n\t"
    "v_mfma_f32_32x32x16_bf16 v[144:159], v[216:219], a[144:147], v[144:159]\n\t"
    "ds_read_b64_tr_b16 v[198:199], v0 offset:768\n\t"
    "ds_read_b64_tr_b16 v[200:201], v0 offset:1024\n\t"
    "s_mov_b32 s88, 3148928\n\t"
    "buffer_load_dwordx4 v10, s[64:67], s88 offen offset:1024 lds\n\t"
    "v_mfma_f32_32x32x16_bf16 v[160:175], v[220:223], a[128:131], v[160:175]\n\t"
    "ds_read_b64_tr_b16 v[202:203], v0 offset:1280\n\t"
    "v_mfma_f32_32x32x16_bf16 v[176:191], v[220:223], a[144:147], v[176:191]\n\t"
    "ds_read_b64_tr_b16 v[204:205], v0 offset:1536\n\t"
    "ds_read_b64_tr_b16 v[206:207], v0 offset:1792\n\t"
    "v_mfma_f32_32x32x16_bf16 v[64:79], a[48:51], a[132:135], v[64:79]\n\t"
    "ds_read_b128 a[100:103], v5 offset:0\n\t"
    "s_mov_b32 s87, 3213312\n\t"
    "buffer_load_dwordx4 v10, s[64:67], s87 offen offset:2048 lds\n\t"
    "v_mfma_f32_32x32x16_bf16 v[80:95], a[48:51], a[148:151], v[80:95]\n\t"
    "ds_read_b128 a[116:119], v5 offset:4096\n\t"
    "ds_read_b64_tr_b16 a[0:1], v1 offset:0\n\t"
    "v_mfma_f32_16x16x32_bf16 v[48:51], v[44:47], v[208:211], v[48:51]\n\t"
    "v_mfma_f32_32x32x16_bf16 v[96:111], a[52:55], a[132:135], v[96:111]\n\t"
    "ds_read_b64_tr_b16 a[2:3], v1 offset:256\n\t"
    "s_mov_b32 s88, 3212416\n\t"
    "buffer_load_dwordx4 v10, s[64:67], s88 offen offset:3072 lds\n\t"
    "v_mfma_f32_32x32x16_bf16 v[112:127], a[52:55], a[148:151], v[112:127]\n\t"
    "ds_read_b64_tr_b16 a[4:5], v1 offset:512\n\t"
    "ds_read_b64_tr_b16 a[6:7], v1 offset:768\n\t"
    "v_mfma_f32_16x16x32_bf16 v[52:55], v[44:47], v[212:215], v[52:55]\n\t"
    "v_mfma_f32_32x32x16_bf16 v[128:143], a[56:59], a[132:135], v[128:143]\n\t"
    "ds_read_b64_tr_b16 a[8:9], v1 offset:1024\n\t"
    "s_add_u32 m0, s97, 16384\n\t"
    "s_mov_b32 s87, 768\n\t"
    "buffer_load_dwordx4 v8, s[60:63], s87 offen offset:0 lds\n\t"
    "v_mfma_f32_32x32x16_bf16 v[144:159], a[56:59], a[148:151], v[144:159]\n\t"
    "ds_read_b64_tr_b16 a[10:11], v1 offset:1280\n\t"
    "ds_read_b64_tr_b16 a[12:13], v1 offset:1536\n\t"
    "v_mfma_f32_16x16x32_bf16 v[56:59], v[44:47], v[216:219], v[56:59]\n\t"
    "v_mfma_f32_32x32x16_bf16 v[160:175], a[60:63], a[132:135], v[160:175]\n\t"
    "ds_read_b64_tr_b16 a[14:15], v1 offset:1792\n\t"
    "v_mfma_f32_32x32x16_bf16 v[176:191], a[60:63], a[148:151], v[176:191]\n\t"
    "ds_read_b128 a[104:107], v6 offset:0\n\t"
    "ds_read_b128 a[120:123], v6 offset:4096\n\t"
    "v_mfma_f32_16x16x32_bf16 v[60:63], v[44:47], v[220:223], v[60:63]\n\t"
    "s_mov_b32 s88, 7936\n\t"
    "buffer_load_dwordx4 v9, s[60:63], s88 offen offset:1024 lds\n\t"
    "v_mfma_f32_32x32x16_bf16 v[64:79], a[64:67], a[136:139], v[64:79]\n\t"
    "ds_read_b64_tr_b16 a[16:17], v2 offset:0\n\t"
    "ds_read_b64_tr_b16 a[18:19], v2 offset:256\n\t"
    "v_mfma_f32_32x32x16_bf16 v[80:95], a[64:67], a[152:155], v[80:95]\n\t"
    "ds_read_b64_tr_b16 a[20:21], v2 offset:512\n\t"
    "s_mov_b32 s87, 15104\n\t"
    "buffer_load_dwordx4 v8, s[60:63], s87 offen offset:2048 lds\n\t"
    "v_mfma_f32_32x32x16_bf16 v[96:111], a[68:71], a[136:139], v[96:111]\n\t"
    "ds_read_b64_tr_b16 a[22:23], v2 offset:768\n\t"
    "ds_read_b64_tr_b16 a[24:25], v2 offset:1024\n\t"
    "v_mfma_f32_32x32x16_bf16 v[112:127], a[68:71], a[152:155], v[112:127]\n\t"
    "ds_read_b64_tr_b16 a[26:27], v2 offset:1280\n\t"
    "v_mfma_f32_32x32x16_bf16 v[128:143], a[72:75], a[136:139], v[128:143]\n\t"
    "ds_read_b64_tr_b16 a[28:29], v2 offset:1536\n\t"
    "ds_read_b64_tr_b16 a[30:31], v2 offset:1792\n\t"
    "s_mov_b32 s88, 22272\n\t"
    "buffer_load_dwordx4 v9, s[60:63], s88 offen offset:3072 lds\n\t"
    "v_mfma_f32_32x32x16_bf16 v[144:159], a[72:75], a[152:155], v[144:159]\n\t"
    "ds_read_b128 a[108:111], v7 offset:0\n\t"
    "v_mfma_f32_32x32x16_bf16 v[160:175], a[76:79], a[136:139], v[160:175]\n\t"
    "ds_read_b128 a[124:127], v7 offset:4096\n\t"
    "ds_read_b64_tr_b16 a[32:33], v3 offset:0\n\t"
    "s_add_u32 m0, s97, 20480\n\t"
    "s_mov_b32 s87, 33536\n\t"
    "buffer_load_dwordx4 v8, s[60:63], s87 offen offset:0 lds\n\t"
    "v_mfma_f32_32x32x16_bf16 v[176:191], a[76:79], a[152:155], v[176:191]\n\t"
    "ds_read_b64_tr_b16 a[34:35], v3 offset:256\n\t"
    "v_mfma_f32_32x32x16_bf16 v[64:79], a[80:83], a[140:143], v[64:79]\n\t"
    "ds_read_b64_tr_b16 a[36:37], v3 offset:512\n\t"
    "ds_read_b64_tr_b16 a[38:39], v3 offset:768\n\t"
    "s_mov_b32 s88, 40704\n\t"
    "buffer_load_dwordx4 v9, s[60:63], s88 offen offset:1024 lds\n\t"
    "v_mfma_f32_32x32x16_bf16 v[80:95], a[80:83], a[156:159], v[80:95]\n\t"
    "ds_read_b64_tr_b16 a[40:41], v3 offset:1024\n\t"
    "v_mfma_f32_32x32x16_bf16 v[96:111], a[84:87], a[140:143], v[96:111]\n\t"
    "ds_read_b64_tr_b16 a[42:43], v3 offset:1280\n\t"
    "ds_read_b64_tr_b16 a[44:45], v3 offset:1536\n\t"
    "v_mfma_f32_32x32x16_bf16 v[112:127], a[84:87], a[156:159], v[112:127]\n\t"
    "ds_read_b64_tr_b16 a[46:47], v3 offset:1792\n\t"
    "s_mov_b32 s87, 47872\n\t"
    "buffer_load_dwordx4 v8, s[60:63], s87 offen offset:2048 lds\n\t"
    "v_mfma_f32_32x32x16_bf16 v[128:143], a[88:91], a[140:143], v[128:143]\n\t"
    "v_mfma_f32_32x32x16_bf16 v[144:159], a[88:91], a[156:159], v[144:159]\n\t"
    "s_mov_b32 s88, 55040\n\t"
    "buffer_load_dwordx4 v9, s[60:63], s88 offen offset:3072 lds\n\t"
    "v_mfma_f32_32x32x16_bf16 v[160:175], a[92:95], a[140:143], v[160:175]\n\t"
    "v_mfma_f32_32x32x16_bf16 v[176:191], a[92:95], a[156:159], v[176:191]\n\t"
    "s_waitcnt vmcnt(13) lgkmcnt(0)\n\t"
    "s_barrier\n\t"
    "v_mfma_f32_32x32x16_bf16 v[64:79], v[192:195], a[96:99], v[64:79]\n\t"
    "v_add_u32_e32 v0, 0x10000, v0\n\t"
    "v_add_u32_e32 v1, 0x10000, v1\n\t"
    "v_add_u32_e32 v2, 0x10000, v2\n\t"
    "v_add_u32_e32 v3, 0x10000, v3\n\t"
    "v_mfma_f32_32x32x16_bf16 v[80:95], v[192:195], a[112:115], v[80:95]\n\t"
    "v_add_u32_e32 v4, 0x10000, v4\n\t"
    "v_add_u32_e32 v5, 0x10000, v5\n\t"
    "v_add_u32_e32 v6, 0x10000, v6\n\t"
    "v_add_u32_e32 v7, 0x10000, v7\n\t"
    "s_add_u32 m0, s96, 49152\n\t"
    "v_mfma_f32_32x32x16_bf16 v[96:111], v[196:199], a[96:99], v[96:111]\n\t"
    "ds_read_b128 a[128:131], v4 offset:0\n\t"
    "ds_read_b128 a[144:147], v4 offset:4096\n\t"
    "v_mfma_f32_32x32x16_bf16 v[112:127], v[196:199], a[112:115], v[112:127]\n\t"
    "ds_read_b64_tr_b16 v[208:209], v0 offset:0\n\t"
    "ds_read_b64_tr_b16 v[210:211], v0 offset:256\n\t"
    "s_mov_b32 s87, 3674112\n\t"
    "buffer_load_dwordx4 v10, s[64:67], s87 offen offset:0 lds\n\t"
    "v_mfma_f32_32x32x16_bf16 v[128:143], v[200:203], a[96:99], v[128:143]\n\t"
    "ds_read_b64_tr_b16 v[212:213], v0 offset:512\n\t"
    "v_mfma_f32_32x32x16_bf16 v[144:159], v[200:203], a[112:115], v[144:159]\n\t"
    "ds_read_b64_tr_b16 v[214:215], v0 offset:768\n\t"
    "ds_read_b64_tr_b16 v[216:217], v0 offset:1024\n\t"
    "s_mov_b32 s88, 3673216\n\t"
    "buffer_load_dwordx4 v10, s[64:67], s88 offen offset:1024 lds\n\t"
    "v_mfma_f32_32x32x16_bf16 v[160:175], v[204:207], a[96:99], v[160:175]\n\t"
    "ds_read_b64_tr_b16 v[218:219], v0 offset:1280\n\t"
    "v_mfma_f32_32x32x16_bf16 v[176:191], v[204:207], a[112:115], v[176:191]\n\t"
    "ds_read_b64_tr_b16 v[220:221], v0 offset:1536\n\t"
    "ds_read_b64_tr_b16 v[222:223], v0 offset:1792\n\t"
    "v_mfma_f32_32x32x16_bf16 v[64:79], a[0:3], a[100:103], v[64:79]\n\t"
    "ds_read_b128 a[132:135], v5 offset:0\n\t"
    "s_mov_b32 s87, 3737600\n\t"
    "buffer_load_dwordx4 v10, s[64:67], s87 offen offset:2048 lds\n\t"
    "v_mfma_f32_32x32x16_bf16 v[80:95], a[0:3], a[116:119], v[80:95]\n\t"
    "ds_read_b128 a[148:151], v5 offset:4096\n\t"
    "ds_read_b64_tr_b16 a[48:49], v1 offset:0\n\t"
    "v_mfma_f32_16x16x32_bf16 v[48:51], v[44:47], v[192:195], v[48:51]\n\t"
    "v_mfma_f32_32x32x16_bf16 v[96:111], a[4:7], a[100:103], v[96:111]\n\t"
    "ds_read_b64_tr_b16 a[50:51], v1 offset:256\n\t"
    "s_mov_b32 s88, 3736704\n\t"
    "buffer_load_dwordx4 v10, s[64:67], s88 offen offset:3072 lds\n\t"
    "v_mfma_f32_32x32x16_bf16 v[112:127], a[4:7], a[116:119], v[112:127]\n\t"
    "ds_read_b64_tr_b16 a[52:53], v1 offset:512\n\t"
    "ds_read_b64_tr_b16 a[54:55], v1 offset:768\n\t"
    "v_mfma_f32_16x16x32_bf16 v[52:55], v[44:47], v[196:199], v[52:55]\n\t"
    "v_mfma_f32_32x32x16_bf16 v[128:143], a[8:11], a[100:103], v[128:143]\n\t"
    "ds_read_b64_tr_b16 a[56:57], v1 offset:1024\n\t"
    "s_add_u32 m0, s97, 65536\n\t"
    "s_mov_b32 s87, 896\n\t"
    "buffer_load_dwordx4 v8, s[60:63], s87 offen offset:0 lds\n\t"
    "v_mfma_f32_32x32x16_bf16 v[144:159], a[8:11], a[116:119], v[144:159]\n\t"
    "ds_read_b64_tr_b16 a[58:59], v1 offset:1280\n\t"
    "ds_read_b64_tr_b16 a[60:61], v1 offset:1536\n\t"
    "v_mfma_f32_16x16x32_bf16 v[56:59], v[44:47], v[200:203], v[56:59]\n\t"
    "v_mfma_f32_32x32x16_bf16 v[160:175], a[12:15], a[100:103], v[160:175]\n\t"
    "ds_read_b64_tr_b16 a[62:63], v1 offset:1792\n\t"
    "v_mfma_f32_32x32x16_bf16 v[176:191], a[12:15], a[116:119], v[176:191]\n\t"
    "ds_read_b128 a[136:139], v6 offset:0\n\t"
    "ds_read_b128 a[152:155], v6 offset:4096\n\t"
    "v_mfma_f32_16x16x32_bf16 v[60:63], v[44:47], v[204:207], v[60:63]\n\t"
    "s_mov_b32 s88, 8064\n\t"
    "buffer_load_dwordx4 v9, s[60:63], s88 offen offset:1024 lds\n\t"
    "v_mfma_f32_32x32x16_bf16 v[64:79], a[16:19], a[104:107], v[64:79]\n\t"
    "ds_read_b64_tr_b16 a[64:65], v2 offset:0\n\t"
    "ds_read_b64_tr_b16 a[66:67], v2 offset:256\n\t"
    "v_mfma_f32_32x32x16_bf16 v[80:95], a[16:19], a[120:123], v[80:95]\n\t"
    "ds_read_b64_tr_b16 a[68:69], v2 offset:512\n\t"
    "s_mov_b32 s87, 15232\n\t"
    "buffer_load_dwordx4 v8, s[60:63], s87 offen offset:2048 lds\n\t"
    "v_mfma_f32_32x32x16_bf16 v[96:111], a[20:23], a[104:107], v[96:111]\n\t"
    "ds_read_b64_tr_b16 a[70:71], v2 offset:768\n\t"
    "ds_read_b64_tr_b16 a[72:73], v2 offset:1024\n\t"
    "v_mfma_f32_32x32x16_bf16 v[112:127], a[20:23], a[120:123], v[112:127]\n\t"
    "ds_read_b64_tr_b16 a[74:75], v2 offset:1280\n\t"
    "v_mfma_f32_32x32x16_bf16 v[128:143], a[24:27], a[104:107], v[128:143]\n\t"
    "ds_read_b64_tr_b16 a[76:77], v2 offset:1536\n\t"
    "ds_read_b64_tr_b16 a[78:79], v2 offset:1792\n\t"
    "s_mov_b32 s88, 22400\n\t"
    "buffer_load_dwordx4 v9, s[60:63], s88 offen offset:3072 lds\n\t"
    "v_mfma_f32_32x32x16_bf16 v[144:159], a[24:27], a[120:123], v[144:159]\n\t"
    "ds_read_b128 a[140:143], v7 offset:0\n\t"
    "v_mfma_f32_32x32x16_bf16 v[160:175], a[28:31], a[104:107], v[160:175]\n\t"
    "ds_read_b128 a[156:159], v7 offset:4096\n\t"
    "ds_read_b64_tr_b16 a[80:81], v3 offset:0\n\t"
    "s_add_u32 m0, s97, 69632\n\t"
    "s_mov_b32 s87, 33664\n\t"
    "buffer_load_dwordx4 v8, s[60:63], s87 offen offset:0 lds\n\t"
    "v_mfma_f32_32x32x16_bf16 v[176:191], a[28:31], a[120:123], v[176:191]\n\t"
    "ds_read_b64_tr_b16 a[82:83], v3 offset:256\n\t"
    "v_mfma_f32_32x32x16_bf16 v[64:79], a[32:35], a[108:111], v[64:79]\n\t"
    "ds_read_b64_tr_b16 a[84:85], v3 offset:512\n\t"
    "ds_read_b64_tr_b16 a[86:87], v3 offset:768\n\t"
    "s_mov_b32 s88, 40832\n\t"
    "buffer_load_dwordx4 v9, s[60:63], s88 offen offset:1024 lds\n\t"
    "v_mfma_f32_32x32x16_bf16 v[80:95], a[32:35], a[124:127], v[80:95]\n\t"
    "ds_read_b64_tr_b16 a[88:89], v3 offset:1024\n\t"
    "v_mfma_f32_32x32x16_bf16 v[96:111], a[36:39], a[108:111], v[96:111]\n\t"
    "ds_read_b64_tr_b16 a[90:91], v3 offset:1280\n\t"
    "ds_read_b64_tr_b16 a[92:93], v3 offset:1536\n\t"
    "v_mfma_f32_32x32x16_bf16 v[112:127], a[36:39], a[124:127], v[112:127]\n\t"
    "ds_read_b64_tr_b16 a[94:95], v3 offset:1792\n\t"
    "s_mov_b32 s87, 48000\n\t"
    "buffer_load_dwordx4 v8, s[60:63], s87 offen offset:2048 lds\n\t"
    "v_mfma_f32_32x32x16_bf16 v[128:143], a[40:43], a[108:111], v[128:143]\n\t"
    "v_mfma_f32_32x32x16_bf16 v[144:159], a[40:43], a[124:127], v[144:159]\n\t"
    "s_mov_b32 s88, 55168\n\t"
    "buffer_load_dwordx4 v9, s[60:63], s88 offen offset:3072 lds\n\t"
    "v_mfma_f32_32x32x16_bf16 v[160:175], a[44:47], a[108:111], v[160:175]\n\t"
    "v_mfma_f32_32x32x16_bf16 v[176:191], a[44:47], a[124:127], v[176:191]\n\t"
    "s_waitcnt vmcnt(12) lgkmcnt(0)\n\t"
    "s_barrier\n\t"
    "v_mfma_f32_32x32x16_bf16 v[64:79], v[208:211], a[128:131], v[64:79]\n\t"
    "v_add_u32_e32 v0, 0xfffe4000, v0\n\t"
    "v_add_u32_e32 v1, 0xfffe4000, v1\n\t"
    "v_add_u32_e32 v2, 0xfffe4000, v2\n\t"
    "v_add_u32_e32 v3, 0xfffe4000, v3\n\t"
    "s_load_dwordx4 s[52:55], %[ka], 0x30\n\t"
    "v_mfma_f32_32x32x16_bf16 v[80:95], v[208:211], a[144:147], v[80:95]\n\t"
    "v_add_u32_e32 v4, 0xfffe4000, v4\n\t"
    "v_add_u32_e32 v5, 0xfffe4000, v5\n\t"
    "v_add_u32_e32 v6, 0xfffe4000, v6\n\t"
    "v_add_u32_e32 v7, 0xfffe4000, v7\n\t"
    "s_mov_b32 m0, s90\n\t"
    "s_mov_b32 s87, 0\n\t"
    "v_mfma_f32_32x32x16_bf16 v[96:111], v[212:215], a[128:131], v[96:111]\n\t"
    "ds_read_b128 a[96:99], v4 offset:0\n\t"
    "ds_read_b128 a[112:115], v4 offset:4096\n\t"
    "v_mfma_f32_32x32x16_bf16 v[112:127], v[212:215], a[144:147], v[112:127]\n\t"
    "ds_read_b64_tr_b16 v[192:193], v0 offset:0\n\t"
    "ds_read_b64_tr_b16 v[194:195], v0 offset:256\n\t"
    "buffer_load_dwordx4 v233, s[68:71], s87 offen offset:0 lds\n\t"
    "v_mfma_f32_32x32x16_bf16 v[128:143], v[216:219], a[128:131], v[128:143]\n\t"
    "ds_read_b64_tr_b16 v[196:197], v0 offset:512\n\t"
    "v_mfma_f32_32x32x16_bf16 v[144:159], v[216:219], a[144:147], v[144:159]\n\t"
    "ds_read_b64_tr_b16 v[198:199], v0 offset:768\n\t"
    "ds_read_b64_tr_b16 v[200:201], v0 offset:1024\n\t"
    "buffer_load_dwordx4 v234, s[68:71], s87 offen offset:1024 lds\n\t"
    "v_mfma_f32_32x32x16_bf16 v[160:175], v[220:223], a[128:131], v[160:175]\n\t"
    "ds_read_b64_tr_b16 v[202:203], v0 offset:1280\n\t"
    "v_mfma_f32_32x32x16_bf16 v[176:191], v[220:223], a[144:147], v[176:191]\n\t"
    "ds_read_b64_tr_b16 v[204:205], v0 offset:1536\n\t"
    "ds_read_b64_tr_b16 v[206:207], v0 offset:1792\n\t"
    "buffer_load_dwordx4 v235, s[68:71], s87 offen offset:2048 lds\n\t"
    "v_mfma_f32_32x32x16_bf16 v[64:79], a[48:51], a[132:135], v[64:79]\n\t"
    "ds_read_b128 a[100:103], v5 offset:0\n\t"
    "v_mfma_f32_32x32x16_bf16 v[80:95], a[48:51], a[148:151], v[80:95]\n\t"
    "ds_read_b128 a[116:119], v5 offset:4096\n\t"
    "ds_read_b64_tr_b16 a[0:1], v1 offset:0\n\t"
    "v_mfma_f32_16x16x32_bf16 v[48:51], v[44:47], v[208:211], v[48:51]\n\t"
    "buffer_load_dwordx4 v236, s[68:71], s87 offen offset:3072 lds\n\t"
    "v_mfma_f32_32x32x16_bf16 v[96:111], a[52:55], a[132:135], v[96:111]\n\t"
    "ds_read_b64_tr_b16 a[2:3], v1 offset:256\n\t"
    "v_mfma_f32_32x32x16_bf16 v[112:127], a[52:55], a[148:151], v[112:127]\n\t"
    "ds_read_b64_tr_b16 a[4:5], v1 offset:512\n\t"
    "ds_read_b64_tr_b16 a[6:7], v1 offset:768\n\t"
    "v_mfma_f32_16x16x32_bf16 v[52:55], v[44:47], v[212:215], v[52:55]\n\t"
    "s_mov_b32 m0, s91\n\t"
    "s_mov_b32 s87, 16384\n\t"
    "v_mfma_f32_32x32x16_bf16 v[128:143], a[56:59], a[132:135], v[128:143]\n\t"
    "ds_read_b64_tr_b16 a[8:9], v1 offset:1024\n\t"
    "v_mfma_f32_32x32x16_bf16 v[144:159], a[56:59], a[148:151], v[144:159]\n\t"
    "ds_read_b64_tr_b16 a[10:11], v1 offset:1280\n\t"
    "ds_read_b64_tr_b16 a[12:13], v1 offset:1536\n\t"
    "v_mfma_f32_16x16x32_bf16 v[56:59], v[44:47], v[216:219], v[56:59]\n\t"
    "buffer_load_dwordx4 v233, s[68:71], s87 offen offset:0 lds\n\t"
    "v_mfma_f32_32x32x16_bf16 v[160:175], a[60:63], a[132:135], v[160:175]\n\t"
    "ds_read_b64_tr_b16 a[14:15], v1 offset:1792\n\t"
    "v_mfma_f32_32x32x16_bf16 v[176:191], a[60:63], a[148:151], v[176:191]\n\t"
    "ds_read_b128 a[104:107], v6 offset:0\n\t"
    "ds_read_b128 a[120:123], v6 offset:4096\n\t"
    "v_mfma_f32_16x16x32_bf16 v[60:63], v[44:47], v[220:223], v[60:63]\n\t"
    "buffer_load_dwordx4 v234, s[68:71], s87 offen offset:1024 lds\n\t"
    "v_mfma_f32_32x32x16_bf16 v[64:79], a[64:67], a[136:139], v[64:79]\n\t"
    "ds_read_b64_tr_b16 a[16:17], v2 offset:0\n\t"
    "ds_read_b64_tr_b16 a[18:19], v2 offset:256\n\t"
    "v_mfma_f32_32x32x16_bf16 v[80:95], a[64:67], a[152:155], v[80:95]\n\t"
    "ds_read_b64_tr_b16 a[20:21], v2 offset:512\n\t"
    "buffer_load_dwordx4 v235, s[68:71], s87 offen offset:2048 lds\n\t"
    "v_mfma_f32_32x32x16_bf16 v[96:111], a[68:71], a[136:139], v[96:111]\n\t"
    "ds_read_b64_tr_b16 a[22:23], v2 offset:768\n\t"
    "ds_read_b64_tr_b16 a[24:25], v2 offset:1024\n\t"
    "v_mfma_f32_32x32x16_bf16 v[112:127], a[68:71], a[152:155], v[112:127]\n\t"
    "ds_read_b64_tr_b16 a[26:27], v2 offset:1280\n\t"
    "buffer_load_dwordx4 v236, s[68:71], s87 offen offset:3072 lds\n\t"
    "v_mfma_f32_32x32x16_bf16 v[128:143], a[72:75], a[136:139], v[128:143]\n\t"
    "ds_read_b64_tr_b16 a[28:29], v2 offset:1536\n\t"
    "ds_read_b64_tr_b16 a[30:31], v2 offset:1792\n\t"
    "v_mfma_f32_32x32x16_bf16 v[144:159], a[72:75], a[152:155], v[144:159]\n\t"
    "ds_read_b128 a[108:111], v7 offset:0\n\t"
    "s_mov_b32 m0, s92\n\t"
    "s_mov_b32 s87, 32768\n\t"
    "v_mfma_f32_32x32x16_bf16 v[160:175], a[76:79], a[136:139], v[160:175]\n\t"
    "ds_read_b128 a[124:127], v7 offset:4096\n\t"
    "ds_read_b64_tr_b16 a[32:33], v3 offset:0\n\t"
    "v_mfma_f32_32x32x16_bf16 v[176:191], a[76:79], a[152:155], v[176:191]\n\t"
    "ds_read_b64_tr_b16 a[34:35], v3 offset:256\n\t"
    "buffer_load_dwordx4 v233, s[68:71], s87 offen offset:0 lds\n\t"
    "v_mfma_f32_32x32x16_bf16 v[64:79], a[80:83], a[140:143], v[64:79]\n\t"
    "ds_read_b64_tr_b16 a[36:37], v3 offset:512\n\t"
    "ds_read_b64_tr_b16 a[38:39], v3 offset:768\n\t"
    "v_mfma_f32_32x32x16_bf16 v[80:95], a[80:83], a[156:159], v[80:95]\n\t"
    "ds_read_b64_tr_b16 a[40:41], v3 offset:1024\n\t"
    "buffer_load_dwordx4 v234, s[68:71], s87 offen offset:1024 lds\n\t"
    "v_mfma_f32_32x32x16_bf16 v[96:111], a[84:87], a[140:143], v[96:111]\n\t"
    "ds_read_b64_tr_b16 a[42:43], v3 offset:1280\n\t"
    "ds_read_b64_tr_b16 a[44:45], v3 offset:1536\n\t"
    "v_mfma_f32_32x32x16_bf16 v[112:127], a[84:87], a[156:159], v[112:127]\n\t"
    "ds_read_b64_tr_b16 a[46:47], v3 offset:1792\n\t"
    "buffer_load_dwordx4 v235, s[68:71], s87 offen offset:2048 lds\n\t"
    "v_mfma_f32_32x32x16_bf16 v[128:143], a[88:91], a[140:143], v[128:143]\n\t"
    "v_mfma_f32_32x32x16_bf16 v[144:159], a[88:91], a[156:159], v[144:159]\n\t"
    "buffer_load_dwordx4 v236, s[68:71], s87 offen offset:3072 lds\n\t"
    "v_mfma_f32_32x32x16_bf16 v[160:175], a[92:95], a[140:143], v[160:175]\n\t"
    "v_mfma_f32_32x32x16_bf16 v[176:191], a[92:95], a[156:159], v[176:191]\n\t"
    "s_waitcnt vmcnt(12) lgkmcnt(0)\n\t"
    "s_barrier\n\t"
    "v_mfma_f32_32x32x16_bf16 v[64:79], v[192:195], a[96:99], v[64:79]\n\t"
    "v_add_u32_e32 v0, 0xc000, v0\n\t"
    "v_add_u32_e32 v1, 0xc000, v1\n\t"
    "v_add_u32_e32 v2, 0xc000, v2\n\t"
    "v_add_u32_e32 v3, 0xc000, v3\n\t"
    "v_mfma_f32_32x32x16_bf16 v[80:95], v[192:195], a[112:115], v[80:95]\n\t"
    "v_add_u32_e32 v4, 0xc000, v4\n\t"
    "v_add_u32_e32 v5, 0xc000, v5\n\t"
    "v_add_u32_e32 v6, 0xc000, v6\n\t"
    "v_add_u32_e32 v7, 0xc000, v7\n\t"
    "s_mov_b32 m0, s93\n\t"
    "s_nop 0\n\t"
    "v_mfma_f32_32x32x16_bf16 v[96:111], v[196:199], a[96:99], v[96:111]\n\t"
    "ds_read_b128 a[128:131], v4 offset:0\n\t"
    "ds_read_b128 a[144:147], v4 offset:4096\n\t"
    "v_mfma_f32_32x32x16_bf16 v[112:127], v[196:199], a[112:115], v[112:127]\n\t"
    "ds_read_b64_tr_b16 v[208:209], v0 offset:0\n\t"
    "ds_read_b64_tr_b16 v[210:211], v0 offset:256\n\t"
    "buffer_load_dwordx4 v237, s[72:75], 0 offen offset:0 lds\n\t"
    "v_mfma_f32_32x32x16_bf16 v[128:143], v[200:203], a[96:99], v[128:143]\n\t"
    "ds_read_b64_tr_b16 v[212:213], v0 offset:512\n\t"
    "v_mfma_f32_32x32x16_bf16 v[144:159], v[200:203], a[112:115], v[144:159]\n\t"
    "ds_read_b64_tr_b16 v[214:215], v0 offset:768\n\t"
    "ds_read_b64_tr_b16 v[216:217], v0 offset:1024\n\t"
    "buffer_load_dwordx4 v238, s[72:75], 0 offen offset:1024 lds\n\t"
    "v_mfma_f32_32x32x16_bf16 v[160:175], v[204:207], a[96:99], v[160:175]\n\t"
    "ds_read_b64_tr_b16 v[218:219], v0 offset:1280\n\t"
    "v_mfma_f32_32x32x16_bf16 v[176:191], v[204:207], a[112:115], v[176:191]\n\t"
    "ds_read_b64_tr_b16 v[220:221], v0 offset:1536\n\t"
    "ds_read_b64_tr_b16 v[222:223], v0 offset:1792\n\t"
    "v_mfma_f32_32x32x16_bf16 v[64:79], a[0:3], a[100:103], v[64:79]\n\t"
    "ds_read_b128 a[132:135], v5 offset:0\n\t"
    "buffer_load_dwordx4 v237, s[72:75], 0 offen offset:2048 lds\n\t"
    "v_mfma_f32_32x32x16_bf16 v[80:95], a[0:3], a[116:119], v[80:95]\n\t"
    "ds_read_b128 a[148:151], v5 offset:4096\n\t"
    "ds_read_b64_tr_b16 a[48:49], v1 offset:0\n\t"
    "v_mfma_f32_16x16x32_bf16 v[48:51], v[44:47], v[192:195], v[48:51]\n\t"
    "v_mfma_f32_32x32x16_bf16 v[96:111], a[4:7], a[100:103], v[96:111]\n\t"
    "ds_read_b64_tr_b16 a[50:51], v1 offset:256\n\t"
    "buffer_load_dwordx4 v238, s[72:75], 0 offen offset:3072 lds\n\t"
    "v_mfma_f32_32x32x16_bf16 v[112:127], a[4:7], a[116:119], v[112:127]\n\t"
    "ds_read_b64_tr_b16 a[52:53], v1 offset:512\n\t"
    "ds_read_b64_tr_b16 a[54:55], v1 offset:768\n\t"
    "v_mfma_f32_16x16x32_bf16 v[52:55], v[44:47], v[196:199], v[52:55]\n\t"
    "v_mfma_f32_32x32x16_bf16 v[128:143], a[8:11], a[100:103], v[128:143]\n\t"
    "ds_read_b64_tr_b16 a[56:57], v1 offset:1024\n\t"
    "s_add_u32 m0, s96, 0x1000\n\t"
    "v_mfma_f32_32x32x16_bf16 v[144:159], a[8:11], a[116:119], v[144:159]\n\t"
    "ds_read_b64_tr_b16 a[58:59], v1 offset:1280\n\t"
    "ds_read_b64_tr_b16 a[60:61], v1 offset:1536\n\t"
    "v_mfma_f32_16x16x32_bf16 v[56:59], v[44:47], v[200:203], v[56:59]\n\t"
    "v_mfma_f32_32x32x16_bf16 v[160:175], a[12:15], a[100:103], v[160:175]\n\t"
    "ds_read_b64_tr_b16 a[62:63], v1 offset:1792\n\t"
    "v_mfma_f32_32x32x16_bf16 v[176:191], a[12:15], a[116:119], v[176:191]\n\t"
    "ds_read_b128 a[136:139], v6 offset:0\n\t"
    "ds_read_b128 a[152:155], v6 offset:4096\n\t"
    "v_mfma_f32_16x16x32_bf16 v[60:63], v[44:47], v[204:207], v[60:63]\n\t"
    "s_mov_b32 s87, 0\n\t"
    "buffer_load_dwordx4 v11, s[76:79], s87 offen offset:0 lds\n\t"
    "v_mfma_f32_32x32x16_bf16 v[64:79], a[16:19], a[104:107], v[64:79]\n\t"
    "ds_read_b64_tr_b16 a[64:65], v2 offset:0\n\t"
    "ds_read_b64_tr_b16 a[66:67], v2 offset:256\n\t"
    "v_mfma_f32_32x32x16_bf16 v[80:95], a[16:19], a[120:123], v[80:95]\n\t"
    "ds_read_b64_tr_b16 a[68:69], v2 offset:512\n\t"
    "s_mov_b32 s88, 7168\n\t"
    "buffer_load_dwordx4 v11, s[76:79], s88 offen offset:1024 lds\n\t"
    "v_mfma_f32_32x32x16_bf16 v[96:111], a[20:23], a[104:107], v[96:111]\n\t"
    "ds_read_b64_tr_b16 a[70:71], v2 offset:768\n\t"
    "ds_read_b64_tr_b16 a[72:73], v2 offset:1024\n\t"
    "v_mfma_f32_32x32x16_bf16 v[112:127], a[20:23], a[120:123], v[112:127]\n\t"
    "ds_read_b64_tr_b16 a[74:75], v2 offset:1280\n\t"
    "v_mfma_f32_32x32x16_bf16 v[128:143], a[24:27], a[104:107], v[128:143]\n\t"
    "ds_read_b64_tr_b16 a[76:77], v2 offset:1536\n\t"
    "ds_read_b64_tr_b16 a[78:79], v2 offset:1792\n\t"
    "s_mov_b32 s87, 14336\n\t"
    "buffer_load_dwordx4 v11, s[76:79], s87 offen offset:2048 lds\n\t"
    "v_mfma_f32_32x32x16_bf16 v[144:159], a[24:27], a[120:123], v[144:159]\n\t"
    "ds_read_b128 a[140:143], v7 offset:0\n\t"
    "v_mfma_f32_32x32x16_bf16 v[160:175], a[28:31], a[104:107], v[160:175]\n\t"
    "ds_read_b128 a[156:159], v7 offset:4096\n\t"
    "ds_read_b64_tr_b16 a[80:81], v3 offset:0\n\t"
    "s_mov_b32 s88, 21504\n\t"
    "buffer_load_dwordx4 v11, s[76:79], s88 offen offset:3072 lds\n\t"
    "v_mfma_f32_32x32x16_bf16 v[176:191], a[28:31], a[120:123], v[176:191]\n\t"
    "ds_read_b64_tr_b16 a[82:83], v3 offset:256\n\t"
    "v_mfma_f32_32x32x16_bf16 v[64:79], a[32:35], a[108:111], v[64:79]\n\t"
    "ds_read_b64_tr_b16 a[84:85], v3 offset:512\n\t"
    "ds_read_b64_tr_b16 a[86:87], v3 offset:768\n\t"
    "buffer_load_dwordx2 v[16:17], v20, s[80:83], 0 offen offset:0 sc1\n\t"
    "v_mfma_f32_32x32x16_bf16 v[80:95], a[32:35], a[124:127], v[80:95]\n\t"
    "ds_read_b64_tr_b16 a[88:89], v3 offset:1024\n\t"
    "v_mfma_f32_32x32x16_bf16 v[96:111], a[36:39], a[108:111], v[96:111]\n\t"
    "ds_read_b64_tr_b16 a[90:91], v3 offset:1280\n\t"
    "ds_read_b64_tr_b16 a[92:93], v3 offset:1536\n\t"
    "v_mfma_f32_32x32x16_bf16 v[112:127], a[36:39], a[124:127], v[112:127]\n\t"
    "ds_read_b64_tr_b16 a[94:95], v3 offset:1792\n\t"
    "buffer_load_dwordx2 v[18:19], v20, s[80:83], 0 offen offset:256 sc1\n\t"
    "v_mfma_f32_32x32x16_bf16 v[128:143], a[40:43], a[108:111], v[128:143]\n\t"
    "v_mfma_f32_32x32x16_bf16 v[144:159], a[40:43], a[124:127], v[144:159]\n\t"
    "v_mfma_f32_32x32x16_bf16 v[160:175], a[44:47], a[108:111], v[160:175]\n\t"
    "v_mfma_f32_32x32x16_bf16 v[176:191], a[44:47], a[124:127], v[176:191]\n\t"
    "s_waitcnt lgkmcnt(0)\n\t"
    "s_barrier\n\t"
    "v_mfma_f32_32x32x16_bf16 v[64:79], v[208:211], a[128:131], v[64:79]\n\t"
    "v_mfma_f32_32x32x16_bf16 v[80:95], v[208:211], a[144:147], v[80:95]\n\t"
    "s_mov_b32 m0, s94\n\t"
    "s_mov_b32 s87, 0x4000\n\t"
    "v_mfma_f32_32x32x16_bf16 v[96:111], v[212:215], a[128:131], v[96:111]\n\t"
    "v_mfma_f32_32x32x16_bf16 v[112:127], v[212:215], a[144:147], v[112:127]\n\t"
    "buffer_load_dwordx4 v237, s[72:75], s87 offen offset:0 lds\n\t"
    "v_mfma_f32_32x32x16_bf16 v[128:143], v[216:219], a[128:131], v[128:143]\n\t"
    "v_mfma_f32_32x32x16_bf16 v[144:159], v[216:219], a[144:147], v[144:159]\n\t"
    "buffer_load_dwordx4 v238, s[72:75], s87 offen offset:1024 lds\n\t"
    "v_mfma_f32_32x32x16_bf16 v[160:175], v[220:223], a[128:131], v[160:175]\n\t"
    "v_mfma_f32_32x32x16_bf16 v[176:191], v[220:223], a[144:147], v[176:191]\n\t"
    "v_mfma_f32_32x32x16_bf16 v[64:79], a[48:51], a[132:135], v[64:79]\n\t"
    "buffer_load_dwordx4 v237, s[72:75], s87 offen offset:2048 lds\n\t"
    "v_mfma_f32_32x32x16_bf16 v[80:95], a[48:51], a[148:151], v[80:95]\n\t"
    "v_mfma_f32_16x16x32_bf16 v[48:51], v[44:47], v[208:211], v[48:51]\n\t"
    "v_mfma_f32_32x32x16_bf16 v[96:111], a[52:55], a[132:135], v[96:111]\n\t"
    "buffer_load_dwordx4 v238, s[72:75], s87 offen offset:3072 lds\n\t"
    "v_mfma_f32_32x32x16_bf16 v[112:127], a[52:55], a[148:151], v[112:127]\n\t"
    "v_mfma_f32_16x16x32_bf16 v[52:55], v[44:47], v[212:215], v[52:55]\n\t"
    "v_lshlrev_b32_e32 v250, 4, v255\n\t"
    "v_lshrrev_b32_e32 v249, 3, v255\n\t"
    "v_lshlrev_b32_e32 v249, 10, v249\n\t"
    "v_and_b32_e32 v248, 7, v255\n\t"
    "v_mfma_f32_32x32x16_bf16 v[128:143], a[56:59], a[132:135], v[128:143]\n\t"
    "v_lshl_add_u32 v249, v248, 4, v249\n\t"
    "s_mul_i32 s33, %[h], 512\n\t"
    "s_add_u32 s33, s33, 4096\n\t"
    "s_add_u32 s64, s58, s33\n\t"
    "v_mfma_f32_32x32x16_bf16 v[144:159], a[56:59], a[148:151], v[144:159]\n\t"
    "v_mfma_f32_16x16x32_bf16 v[56:59], v[44:47], v[216:219], v[56:59]\n\t"
    "s_addc_u32 s65, s59, 0\n\t"
    "s_and_b32 s65, s65, 0xffff\n\t"
    "s_mov_b32 s66, 512\n\t"
    "v_mfma_f32_32x32x16_bf16 v[160:175], a[60:63], a[132:135], v[160:175]\n\t"
    "s_mov_b32 s67, 0x20000\n\t"
    "s_mov_b32 m0, 53248\n\t"
    "s_nop 0\n\t"
    "buffer_load_dwordx4 v250, s[64:67], 0 offen sc1 lds\n\t"
    "v_mfma_f32_32x32x16_bf16 v[176:191], a[60:63], a[148:151], v[176:191]\n\t"
    "v_mfma_f32_16x16x32_bf16 v[60:63], v[44:47], v[220:223], v[60:63]\n\t"
    "s_mul_i32 s33, %[h], 65536\n\t"
    "s_mul_i32 s34, %[wid], 16384\n\t"
    "s_add_u32 s33, s33, s34\n\t"
    "v_mfma_f32_32x32x16_bf16 v[64:79], a[64:67], a[136:139], v[64:79]\n\t"
    "s_mul_i32 s34, %[b], 128\n\t"
    "s_add_u32 s33, s33, s34\n\t"
    "s_add_u32 s64, s40, s33\n\t"
    "s_addc_u32 s65, s41, 0\n\t"
    "v_mfma_f32_32x32x16_bf16 v[80:95], a[64:67], a[152:155], v[80:95]\n\t"
    "s_and_b32 s65, s65, 0xffff\n\t"
    "s_mov_b32 s66, 16384\n\t"
    "s_mov_b32 s67, 0x20000\n\t"
    "v_mfma_f32_32x32x16_bf16 v[96:111], a[68:71], a[136:139], v[96:111]\n\t"
    "s_lshr_b32 s89, s96, 1\n\t"
    "s_add_u32 m0, s89, 45056\n\t"
    "s_mov_b32 s99, 7168\n\t"
    "buffer_load_dwordx4 v249, s[64:67], 0 offen lds\n\t"
    "v_mfma_f32_32x32x16_bf16 v[112:127], a[68:71], a[152:155], v[112:127]\n\t"
    "buffer_load_dwordx4 v249, s[64:67], s99 offen offset:1024 lds\n\t"
    "s_mul_i32 s33, %[fj], 8192\n\t"
    "s_mul_i32 s34, %[wid], 2048\n\t"
    "v_mfma_f32_32x32x16_bf16 v[128:143], a[72:75], a[136:139], v[128:143]\n\t"
    "s_add_u32 s33, s33, s34\n\t"
    "s_add_u32 s64, s56, s33\n\t"
    "s_addc_u32 s65, s57, 0\n\t"
    "s_and_b32 s65, s65, 0xffff\n\t"
    "v_mfma_f32_32x32x16_bf16 v[144:159], a[72:75], a[152:155], v[144:159]\n\t"
    "s_mov_b32 s66, 0\n\t"
    "s_mov_b32 s67, 0x20000\n\t"
    "s_cmp_lt_u32 %[fj], 172\n\t"
    "v_mfma_f32_32x32x16_bf16 v[160:175], a[76:79], a[136:139], v[160:175]\n\t"
    "s_cselect_b32 s66, 2048, 0\n\t"
    "s_add_u32 m0, s89, 36864\n\t"
    "s_nop 0\n\t"
    "buffer_load_dwordx4 v250, s[64:67], 0 offen lds\n\t"
    "v_mfma_f32_32x32x16_bf16 v[176:191], a[76:79], a[152:155], v[176:191]\n\t"
    "buffer_load_dwordx4 v250, s[64:67], 0 offen offset:1024 lds\n\t"
    "v_lshrrev_b32_e32 v248, 4, v255\n\t"
    "v_mul_u32_u24_e32 v248, 2752, v248\n\t"
    "v_mfma_f32_32x32x16_bf16 v[64:79], a[80:83], a[140:143], v[64:79]\n\t"
    "v_and_b32_e32 v249, 15, v255\n\t"
    "v_lshl_add_u32 v248, v249, 5, v248\n\t"
    "s_lshr_b32 s86, %[h], 2\n\t"
    "s_mul_i32 s86, s86, 384\n\t"
    "v_mfma_f32_32x32x16_bf16 v[80:95], a[80:83], a[156:159], v[80:95]\n\t"
    "s_mul_i32 s33, %[pj], 44032\n\t"
    "s_mul_i32 s34, %[wid], 11008\n\t"
    "s_add_u32 s33, s33, s34\n\t"
    "v_mfma_f32_32x32x16_bf16 v[96:111], a[84:87], a[140:143], v[96:111]\n\t"
    "s_add_u32 s64, s52, s33\n\t"
    "s_addc_u32 s65, s53, 0\n\t"
    "s_and_b32 s65, s65, 0xffff\n\t"
    "s_mov_b32 s66, 0\n\t"
    "v_mfma_f32_32x32x16_bf16 v[112:127], a[84:87], a[156:159], v[112:127]\n\t"
    "s_mov_b32 s67, 0x20000\n\t"
    "s_sub_u32 s66, 1409024, s33\n\t"
    "s_mov_b32 m0, 61440\n\t"
    "v_mfma_f32_32x32x16_bf16 v[128:143], a[88:91], a[140:143], v[128:143]\n\t"
    "s_nop 0\n\t"
    "buffer_load_dwordx4 v248, s[64:67], s86 offen lds\n\t"
    "s_mul_i32 s33, %[pj], 44032\n\t"
    "s_mul_i32 s34, %[wid], 11008\n\t"
    "v_mfma_f32_32x32x16_bf16 v[144:159], a[88:91], a[156:159], v[144:159]\n\t"
    "s_add_u32 s33, s33, s34\n\t"
    "s_add_u32 s64, s54, s33\n\t"
    "s_addc_u32 s65, s55, 0\n\t"
    "v_mfma_f32_32x32x16_bf16 v[160:175], a[92:95], a[140:143], v[160:175]\n\t"
    "s_and_b32 s65, s65, 0xffff\n\t"
    "s_mov_b32 s66, 0\n\t"
    "s_mov_b32 s67, 0x20000\n\t"
    "s_sub_u32 s66, 1409024, s33\n\t"
    "v_mfma_f32_32x32x16_bf16 v[176:191], a[92:95], a[156:159], v[176:191]\n\t"
    "s_mov_b32 m0, 61440\n\t"
    "s_nop 0\n\t"
    "buffer_load_dwordx4 v248, s[64:67], s86 offen lds\n\t"
    "s_mov_b64 exec, 0xffff\n\t"
    "ds_write2_b32 v22, v48, v49 offset0:0 offset1:64\n\t"
    "ds_write2_b32 v22, v52, v53 offset0:1 offset1:65\n\t"
    "ds_write2_b32 v22, v56, v57 offset0:2 offset1:66\n\t"
    "ds_write2_b32 v22, v60, v61 offset0:3 offset1:67\n\t"
    "s_mov_b64 exec, -1\n\t"
    "s_waitcnt vmcnt(11) lgkmcnt(0)\n\t"
    "s_barrier\n\t"
    "ds_read_b128 v[48:51], v12 offset:0\n\t"
    "ds_read_b128 v[52:55], v12 offset:512\n\t"
    "ds_read_b64_tr_b16 v[192:193], v23 offset:4096\n\t"
    "ds_read_b64_tr_b16 v[194:195], v23 offset:6144\n\t"
    "ds_read_b64_tr_b16 v[196:197], v23 offset:4352\n\t"
    "ds_read_b64_tr_b16 v[198:199], v23 offset:6400\n\t"
    "ds_read_b64_tr_b16 v[200:201], v23 offset:4608\n\t"
    "ds_read_b64_tr_b16 v[202:203], v23 offset:6656\n\t"
    "ds_read_b64_tr_b16 v[204:205], v23 offset:4864\n\t"
    "ds_read_b64_tr_b16 v[206:207], v23 offset:6912\n\t"
    "ds_read_b128 a[192:195], v228 offset:0\n\t"
    "ds_read_b128 a[196:199], v229 offset:0\n\t"
    "ds_read_b128 a[200:203], v230 offset:0\n\t"
    "ds_read_b128 a[204:207], v231 offset:0\n\t"
    "ds_read_b128 a[208:211], v228 offset:4096\n\t"
    "ds_read_b128 a[212:215], v229 offset:4096\n\t"
    "ds_read_b128 a[216:219], v230 offset:4096\n\t"
    "ds_read_b128 a[220:223], v231 offset:4096\n\t"
    "ds_read_b128 a[224:227], v228 offset:8192\n\t"
    "ds_read_b128 a[228:231], v229 offset:8192\n\t"
    "ds_read_b128 a[232:235], v230 offset:8192\n\t"
    "ds_read_b128 a[236:239], v231 offset:8192\n\t"
    "ds_read_b128 a[240:243], v228 offset:12288\n\t"
    "ds_read_b128 a[244:247], v229 offset:12288\n\t"
    "ds_read_b128 a[248:251], v230 offset:12288\n\t"
    "ds_read_b128 a[252:255], v231 offset:12288\n\t"
    "s_mul_i32 s33, %[b], 524288\n\t"
    "s_mul_i32 s34, %[h], 16384\n\t"
    "s_add_u32 s33, s33, s34\n\t"
    "s_mul_i32 s34, %[wid], 4096\n\t"
    "s_add_u32 s33, s33, s34\n\t"
    "s_add_u32 s33, s33, 18874368\n\t"
    "s_add_u32 s76, s58, s33\n\t"
    "s_addc_u32 s77, s59, 0\n\t"
    "s_and_b32 s77, s77, 0xffff\n\t"
    "s_mov_b32 s78, 4096\n\t"
    "s_mov_b32 s79, 0x20000\n\t"
    "s_add_u32 s60, s58, 0\n\t"
    "s_addc_u32 s61, s59, 0\n\t"
    "s_and_b32 s61, s61, 0xffff\n\t"
    "s_mov_b32 s62, 65536\n\t"
    "s_mov_b32 s63, 0x20000\n\t"
    "s_mov_b32 s86, 200000\n\t"
    "L_poll_%=:\n\t"
    "v_cmp_ne_u32_e32 vcc, %[tag], v17\n\t"
    "v_cmp_ne_u32_e64 s[88:89], %[tag], v19\n\t"
    "s_or_b64 vcc, vcc, s[88:89]\n\t"
    "s_cbranch_vccz L_got_%=\n\t"
    "s_sub_u32 s86, s86, 1\n\t"
    "s_cbranch_scc1 L_got_%=\n\t"
    "s_sleep 2\n\t"
    "buffer_load_dwordx2 v[16:17], v20, s[80:83], 0 offen offset:0 sc1\n\t"
    "buffer_load_dwordx2 v[18:19], v20, s[80:83], 0 offen offset:256 sc1\n\t"
    "s_waitcnt vmcnt(0)\n\t"
    "s_branch L_poll_%=\n\t"
    "L_got_%=:\n\t"
    "v_and_b32_e32 v44, 0xffff0000, v16\n\t"
    "v_lshlrev_b32_e32 v40, 16, v16\n\t"
    "v_rcp_f32_e32 v44, v44\n\t"
    "v_xor_b32_e32 v40, 0x80000000, v40\n\t"
    "v_mul_f32_e32 v44, 0x3E0293EE, v44\n\t"
    "v_and_b32_e32 v46, 0xffff0000, v18\n\t"
    "v_lshlrev_b32_e32 v41, 16, v18\n\t"
    "v_rcp_f32_e32 v46, v46\n\t"
    "v_xor_b32_e32 v41, 0x80000000, v41\n\t"
    "v_mul_f32_e32 v46, 0x3E0293EE, v46\n\t"
    "s_waitcnt lgkmcnt(0)\n\t"
    "v_add_f32_e32 v48, v48, v52\n\t"
    "v_add_f32_e32 v49, v49, v53\n\t"
    "v_add_f32_e32 v50, v50, v54\n\t"
    "v_add_f32_e32 v51, v51, v55\n\t"
    "buffer_store_dwordx4 v[192:195], v21, s[76:79], 0 offen offset:0 sc1\n\t"
    "buffer_store_dwordx4 v[196:199], v21, s[76:79], 0 offen offset:1024 sc1\n\t"
    "buffer_store_dwordx4 v[200:203], v21, s[76:79], 0 offen offset:2048 sc1\n\t"
    "buffer_store_dwordx4 v[204:207], v21, s[76:79], 0 offen offset:3072 sc1\n\t"
    "v_mfma_f32_32x32x2_f32 v[64:79], v48, v40, v[64:79]\n\t"
    "v_mfma_f32_32x32x2_f32 v[80:95], v48, v41, v[80:95]\n\t"
    "v_mfma_f32_32x32x2_f32 v[96:111], v49, v40, v[96:111]\n\t"
    "v_mfma_f32_32x32x2_f32 v[112:127], v49, v41, v[112:127]\n\t"
    "v_and_b32_e32 v239, 7, v255\n\t"
    "v_lshlrev_b32_e32 v251, 2, v239\n\t"
    "s_mov_b32 s84, 49152\n\t"
    "s_mov_b32 s85, 32768\n\t"
    "s_lshl_b32 s97, %[wid], 10\n\t"
    "s_add_u32 m0, s96, 4096\n\t"
    "s_add_u32 s86, s96, 20480\n\t"
    "s_nop 0\n\t"
    "buffer_load_dwordx4 v250, s[60:63], s86 offen offset:0 sc1 lds\n\t"
    "buffer_load_dwordx4 v250, s[60:63], s86 offen offset:1024 sc1 lds\n\t"
    "buffer_load_dwordx4 v250, s[60:63], s86 offen offset:2048 sc1 lds\n\t"
    "buffer_load_dwordx4 v250, s[60:63], s86 offen offset:3072 sc1 lds\n\t"
    "s_nop 7\n\t"
    "s_add_u32 s80, s40, 0\n\t"
    "v_pk_mul_f32 v[64:65], v[64:65], v[44:45] op_sel_hi:[1,0]\n\t"
    "v_mfma_f32_32x32x2_f32 v[128:143], v50, v40, v[128:143]\n\t"
    "v_pk_mul_f32 v[66:67], v[66:67], v[44:45] op_sel_hi:[1,0]\n\t"
    "v_pk_mul_f32 v[68:69], v[68:69], v[44:45] op_sel_hi:[1,0]\n\t"
    "v_pk_mul_f32 v[70:71], v[70:71], v[44:45] op_sel_hi:[1,0]\n\t"
    "v_pk_mul_f32 v[72:73], v[72:73], v[44:45] op_sel_hi:[1,0]\n\t"
    "v_pk_mul_f32 v[74:75], v[74:75], v[44:45] op_sel_hi:[1,0]\n\t"
    "v_pk_mul_f32 v[76:77], v[76:77], v[44:45] op_sel_hi:[1,0]\n\t"
    "v_pk_mul_f32 v[78:79], v[78:79], v[44:45] op_sel_hi:[1,0]\n\t"
    "v_cvt_pk_bf16_f32 v64, v64, v65\n\t"
    "v_cvt_pk_bf16_f32 v65, v66, v67\n\t"
    "v_cvt_pk_bf16_f32 v66, v68, v69\n\t"
    "v_cvt_pk_bf16_f32 v67, v70, v71\n\t"
    "v_cvt_pk_bf16_f32 v68, v72, v73\n\t"
    "v_cvt_pk_bf16_f32 v69, v74, v75\n\t"
    "v_cvt_pk_bf16_f32 v70, v76, v77\n\t"
    "v_cvt_pk_bf16_f32 v71, v78, v79\n\t"
    "s_nop 1\n\t"
    "v_permlane16_swap_b32_e32 v64, v68\n\t"
    "v_permlane16_swap_b32_e32 v65, v69\n\t"
    "v_permlane16_swap_b32_e32 v66, v70\n\t"
    "v_permlane16_swap_b32_e32 v67, v71\n\t"
    "v_accvgpr_write_b32 a128, v64\n\t"
    "v_accvgpr_write_b32 a129, v65\n\t"
    "v_accvgpr_write_b32 a130, v66\n\t"
    "v_accvgpr_write_b32 a131, v67\n\t"
    "v_accvgpr_write_b32 a144, v68\n\t"
    "v_accvgpr_write_b32 a145, v69\n\t"
    "v_accvgpr_write_b32 a146, v70\n\t"
    "v_accvgpr_write_b32 a147, v71\n\t"
    "v_mfma_f32_32x32x2_f32 v[144:159], v50, v41, v[144:159]\n\t"
    "s_addc_u32 s81, s41, 0\n\t"
    "v_pk_mul_f32 v[80:81], v[80:81], v[46:47] op_sel_hi:[1,0]\n\t"
    "v_pk_mul_f32 v[82:83], v[82:83], v[46:47] op_sel_hi:[1,0]\n\t"
    "v_pk_mul_f32 v[84:85], v[84:85], v[46:47] op_sel_hi:[1,0]\n\t"
    "v_pk_mul_f32 v[86:87], v[86:87], v[46:47] op_sel_hi:[1,0]\n\t"
    "v_pk_mul_f32 v[88:89], v[88:89], v[46:47] op_sel_hi:[1,0]\n\t"
    "v_pk_mul_f32 v[90:91], v[90:91], v[46:47] op_sel_hi:[1,0]\n\t"
    "v_pk_mul_f32 v[92:93], v[92:93], v[46:47] op_sel_hi:[1,0]\n\t"
    "v_pk_mul_f32 v[94:95], v[94:95], v[46:47] op_sel_hi:[1,0]\n\t"
    "v_cvt_pk_bf16_f32 v80, v80, v81\n\t"
    "v_cvt_pk_bf16_f32 v81, v82, v83\n\t"
    "v_cvt_pk_bf16_f32 v82, v84, v85\n\t"
    "v_cvt_pk_bf16_f32 v83, v86, v87\n\t"
    "v_cvt_pk_bf16_f32 v84, v88, v89\n\t"
    "v_cvt_pk_bf16_f32 v85, v90, v91\n\t"
    "v_cvt_pk_bf16_f32 v86, v92, v93\n\t"
    "v_cvt_pk_bf16_f32 v87, v94, v95\n\t"
    "s_nop 1\n\t"
    "v_permlane16_swap_b32_e32 v80, v84\n\t"
    "v_permlane16_swap_b32_e32 v81, v85\n\t"
    "v_permlane16_swap_b32_e32 v82, v86\n\t"
    "v_permlane16_swap_b32_e32 v83, v87\n\t"
    "v_accvgpr_write_b32 a160, v80\n\t"
    "v_accvgpr_write_b32 a161, v81\n\t"
    "v_accvgpr_write_b32 a162, v82\n\t"
    "v_accvgpr_write_b32 a163, v83\n\t"
    "v_accvgpr_write_b32 a176, v84\n\t"
    "v_accvgpr_write_b32 a177, v85\n\t"
    "v_accvgpr_write_b32 a178, v86\n\t"
    "v_accvgpr_write_b32 a179, v87\n\t"
    "s_and_b32 s81, s81, 0xffff\n\t"
    "s_mov_b32 s82, 2097152\n\t"
    "v_mfma_f32_32x32x2_f32 v[160:175], v51, v40, v[160:175]\n\t"
    "v_pk_mul_f32 v[96:97], v[96:97], v[44:45] op_sel_hi:[1,0]\n\t"
    "v_pk_mul_f32 v[98:99], v[98:99], v[44:45] op_sel_hi:[1,0]\n\t"
    "v_pk_mul_f32 v[100:101], v[100:101], v[44:45] op_sel_hi:[1,0]\n\t"
    "v_pk_mul_f32 v[102:103], v[102:103], v[44:45] op_sel_hi:[1,0]\n\t"
    "v_pk_mul_f32 v[104:105], v[104:105], v[44:45] op_sel_hi:[1,0]\n\t"
    "v_mfma_f32_32x32x2_f32 v[176:191], v51, v41, v[176:191]\n\t"
    "v_pk_mul_f32 v[106:107], v[106:107], v[44:45] op_sel_hi:[1,0]\n\t"
    "v_pk_mul_f32 v[108:109], v[108:109], v[44:45] op_sel_hi:[1,0]\n\t"
    "v_pk_mul_f32 v[110:111], v[110:111], v[44:45] op_sel_hi:[1,0]\n\t"
    "v_cvt_pk_bf16_f32 v96, v96, v97\n\t"
    "v_cvt_pk_bf16_f32 v97, v98, v99\n\t"
    "v_cvt_pk_bf16_f32 v98, v100, v101\n\t"
    "v_mfma_f32_16x16x32_bf16 v[0:3], a[192:195], a[128:131], 0\n\t"
    "v_cvt_pk_bf16_f32 v99, v102, v103\n\t"
    "v_cvt_pk_bf16_f32 v100, v104, v105\n\t"
    "v_cvt_pk_bf16_f32 v101, v106, v107\n\t"
    "v_cvt_pk_bf16_f32 v102, v108, v109\n\t"
    "v_cvt_pk_bf16_f32 v103, v110, v111\n\t"
    "s_nop 1\n\t"
    "v_mfma_f32_16x16x32_bf16 v[8:11], a[192:195], a[144:147], 0\n\t"
    "v_permlane16_swap_b32_e32 v96, v100\n\t"
    "v_permlane16_swap_b32_e32 v97, v101\n\t"
    "v_permlane16_swap_b32_e32 v98, v102\n\t"
    "v_permlane16_swap_b32_e32 v99, v103\n\t"
    "v_accvgpr_write_b32 a132, v96\n\t"
    "v_mfma_f32_16x16x32_bf16 v[16:19], a[192:195], a[160:163], 0\n\t"
    "v_accvgpr_write_b32 a133, v97\n\t"
    "v_accvgpr_write_b32 a134, v98\n\t"
    "v_accvgpr_write_b32 a135, v99\n\t"
    "v_accvgpr_write_b32 a148, v100\n\t"
    "v_accvgpr_write_b32 a149, v101\n\t"
    "v_accvgpr_write_b32 a150, v102\n\t"
    "v_mfma_f32_16x16x32_bf16 v[24:27], a[192:195], a[176:179], 0\n\t"
    "v_accvgpr_write_b32 a151, v103\n\t"
    "s_mov_b32 s83, 0x20000\n\t"
    "v_pk_mul_f32 v[112:113], v[112:113], v[46:47] op_sel_hi:[1,0]\n\t"
    "v_pk_mul_f32 v[114:115], v[114:115], v[46:47] op_sel_hi:[1,0]\n\t"
    "v_pk_mul_f32 v[116:117], v[116:117], v[46:47] op_sel_hi:[1,0]\n\t"
    "v_pk_mul_f32 v[118:119], v[118:119], v[46:47] op_sel_hi:[1,0]\n\t"
    "v_mfma_f32_16x16x32_bf16 v[4:7], a[208:211], a[128:131], 0\n\t"
    "v_pk_mul_f32 v[120:121], v[120:121], v[46:47] op_sel_hi:[1,0]\n\t"
    "v_pk_mul_f32 v[122:123], v[122:123], v[46:47] op_sel_hi:[1,0]\n\t"
    "v_pk_mul_f32 v[124:125], v[124:125], v[46:47] op_sel_hi:[1,0]\n\t"
    "v_pk_mul_f32 v[126:127], v[126:127], v[46:47] op_sel_hi:[1,0]\n\t"
    "v_cvt_pk_bf16_f32 v112, v112, v113\n\t"
    "v_mfma_f32_16x16x32_bf16 v[12:15], a[208:211], a[144:147], 0\n\t"
    "v_cvt_pk_bf16_f32 v113, v114, v115\n\t"
    "v_cvt_pk_bf16_f32 v114, v116, v117\n\t"
    "v_cvt_pk_bf16_f32 v115, v118, v119\n\t"
    "v_cvt_pk_bf16_f32 v116, v120, v121\n\t"
    "v_cvt_pk_bf16_f32 v117, v122, v123\n\t"
    "v_cvt_pk_bf16_f32 v118, v124, v125\n\t"
    "v_mfma_f32_16x16x32_bf16 v[20:23], a[208:211], a[160:163], 0\n\t"
    "v_cvt_pk_bf16_f32 v119, v126, v127\n\t"
    "s_nop 1\n\t"
    "v_permlane16_swap_b32_e32 v112, v116\n\t"
    "v_permlane16_swap_b32_e32 v113, v117\n\t"
    "v_permlane16_swap_b32_e32 v114, v118\n\t"
    "v_permlane16_swap_b32_e32 v115, v119\n\t"
    "v_mfma_f32_16x16x32_bf16 v[28:31], a[208:211], a[176:179], 0\n\t"
    "v_accvgpr_write_b32 a164, v112\n\t"
    "v_accvgpr_write_b32 a165, v113\n\t"
    "v_accvgpr_write_b32 a166, v114\n\t"
    "v_accvgpr_write_b32 a167, v115\n\t"
    "v_accvgpr_write_b32 a180, v116\n\t"
    "v_accvgpr_write_b32 a181, v117\n\t"
    "v_accvgpr_write_b32 a182, v118\n\t"
    "v_accvgpr_write_b32 a183, v119\n\t"
    "v_add_u32_e32 v248, s97, v250\n\t"
    "v_pk_mul_f32 v[128:129], v[128:129], v[44:45] op_sel_hi:[1,0]\n\t"
    "v_mfma_f32_16x16x32_bf16 v[0:3], a[196:199], a[132:135], v[0:3]\n\t"
    "v_pk_mul_f32 v[130:131], v[130:131], v[44:45] op_sel_hi:[1,0]\n\t"
    "v_pk_mul_f32 v[132:133], v[132:133], v[44:45] op_sel_hi:[1,0]\n\t"
    "v_pk_mul_f32 v[134:135], v[134:135], v[44:45] op_sel_hi:[1,0]\n\t"
    "v_pk_mul_f32 v[136:137], v[136:137], v[44:45] op_sel_hi:[1,0]\n\t"
    "v_pk_mul_f32 v[138:139], v[138:139], v[44:45] op_sel_hi:[1,0]\n\t"
    "v_pk_mul_f32 v[140:141], v[140:141], v[44:45] op_sel_hi:[1,0]\n\t"
    "v_pk_mul_f32 v[142:143], v[142:143], v[44:45] op_sel_hi:[1,0]\n\t"
    "v_mfma_f32_16x16x32_bf16 v[8:11], a[196:199], a[148:151], v[8:11]\n\t"
    "v_cvt_pk_bf16_f32 v128, v128, v129\n\t"
    "v_cvt_pk_bf16_f32 v129, v130, v131\n\t"
    "v_cvt_pk_bf16_f32 v130, v132, v133\n\t"
    "v_cvt_pk_bf16_f32 v131, v134, v135\n\t"
    "v_cvt_pk_bf16_f32 v132, v136, v137\n\t"
    "v_cvt_pk_bf16_f32 v133, v138, v139\n\t"
    "v_cvt_pk_bf16_f32 v134, v140, v141\n\t"
    "v_mfma_f32_16x16x32_bf16 v[16:19], a[196:199], a[164:167], v[16:19]\n\t"
    "v_cvt_pk_bf16_f32 v135, v142, v143\n\t"
    "s_nop 1\n\t"
    "v_permlane16_swap_b32_e32 v128, v132\n\t"
    "v_permlane16_swap_b32_e32 v129, v133\n\t"
    "v_permlane16_swap_b32_e32 v130, v134\n\t"
    "v_permlane16_swap_b32_e32 v131, v135\n\t"
    "v_accvgpr_write_b32 a136, v128\n\t"
    "v_mfma_f32_16x16x32_bf16 v[24:27], a[196:199], a[180:183], v[24:27]\n\t"
    "v_accvgpr_write_b32 a137, v129\n\t"
    "v_accvgpr_write_b32 a138, v130\n\t"
    "v_accvgpr_write_b32 a139, v131\n\t"
    "v_accvgpr_write_b32 a152, v132\n\t"
    "v_accvgpr_write_b32 a153, v133\n\t"
    "v_accvgpr_write_b32 a154, v134\n\t"
    "v_accvgpr_write_b32 a155, v135\n\t"
    "v_mfma_f32_16x16x32_bf16 v[4:7], a[212:215], a[132:135], v[4:7]\n\t"
    "s_lshl_b32 s33, %[wid], 2\n\t"
    "s_add_u32 s33, s33, 59392\n\t"
    "v_pk_mul_f32 v[144:145], v[144:145], v[46:47] op_sel_hi:[1,0]\n\t"
    "v_pk_mul_f32 v[146:147], v[146:147], v[46:47] op_sel_hi:[1,0]\n\t"
    "v_pk_mul_f32 v[148:149], v[148:149], v[46:47] op_sel_hi:[1,0]\n\t"
    "v_pk_mul_f32 v[150:151], v[150:151], v[46:47] op_sel_hi:[1,0]\n\t"
    "v_pk_mul_f32 v[152:153], v[152:153], v[46:47] op_sel_hi:[1,0]\n\t"
    "v_mfma_f32_16x16x32_bf16 v[12:15], a[212:215], a[148:151], v[12:15]\n\t"
    "v_pk_mul_f32 v[154:155], v[154:155], v[46:47] op_sel_hi:[1,0]\n\t"
    "v_pk_mul_f32 v[156:157], v[156:157], v[46:47] op_sel_hi:[1,0]\n\t"
    "v_pk_mul_f32 v[158:159], v[158:159], v[46:47] op_sel_hi:[1,0]\n\t"
    "v_cvt_pk_bf16_f32 v144, v144, v145\n\t"
    "v_cvt_pk_bf16_f32 v145, v146, v147\n\t"
    "v_cvt_pk_bf16_f32 v146, v148, v149\n\t"
    "v_cvt_pk_bf16_f32 v147, v150, v151\n\t"
    "v_mfma_f32_16x16x32_bf16 v[20:23], a[212:215], a[164:167], v[20:23]\n\t"
    "v_cvt_pk_bf16_f32 v148, v152, v153\n\t"
    "v_cvt_pk_bf16_f32 v149, v154, v155\n\t"
    "v_cvt_pk_bf16_f32 v150, v156, v157\n\t"
    "v_cvt_pk_bf16_f32 v151, v158, v159\n\t"
    "s_nop 1\n\t"
    "v_permlane16_swap_b32_e32 v144, v148\n\t"
    "v_permlane16_swap_b32_e32 v145, v149\n\t"
    "v_mfma_f32_16x16x32_bf16 v[28:31], a[212:215], a[180:183], v[28:31]\n\t"
    "v_permlane16_swap_b32_e32 v146, v150\n\t"
    "v_permlane16_swap_b32_e32 v147, v151\n\t"
    "v_accvgpr_write_b32 a168, v144\n\t"
    "v_accvgpr_write_b32 a169, v145\n\t"
    "v_accvgpr_write_b32 a170, v146\n\t"
    "v_accvgpr_write_b32 a171, v147\n\t"
    "v_accvgpr_write_b32 a184, v148\n\t"
    "v_accvgpr_write_b32 a185, v149\n\t"
    "v_accvgpr_write_b32 a186, v150\n\t"
    "v_accvgpr_write_b32 a187, v151\n\t"
    "v_mov_b32_e32 v249, s33\n\t"
    "v_pk_mul_f32 v[160:161], v[160:161], v[44:45] op_sel_hi:[1,0]\n\t"
    "v_mfma_f32_16x16x32_bf16 v[0:3], a[200:203], a[136:139], v[0:3]\n\t"
    "v_pk_mul_f32 v[162:163], v[162:163], v[44:45] op_sel_hi:[1,0]\n\t"
    "v_pk_mul_f32 v[164:165], v[164:165], v[44:45] op_sel_hi:[1,0]\n\t"
    "v_pk_mul_f32 v[166:167], v[166:167], v[44:45] op_sel_hi:[1,0]\n\t"
    "v_pk_mul_f32 v[168:169], v[168:169], v[44:45] op_sel_hi:[1,0]\n\t"
    "v_pk_mul_f32 v[170:171], v[170:171], v[44:45] op_sel_hi:[1,0]\n\t"
    "v_pk_mul_f32 v[172:173], v[172:173], v[44:45] op_sel_hi:[1,0]\n\t"
    "v_pk_mul_f32 v[174:175], v[174:175], v[44:45] op_sel_hi:[1,0]\n\t"
    "v_mfma_f32_16x16x32_bf16 v[8:11], a[200:203], a[152:155], v[8:11]\n\t"
    "v_cvt_pk_bf16_f32 v160, v160, v161\n\t"
    "v_cvt_pk_bf16_f32 v161, v162, v163\n\t"
    "v_cvt_pk_bf16_f32 v162, v164, v165\n\t"
    "v_cvt_pk_bf16_f32 v163, v166, v167\n\t"
    "v_cvt_pk_bf16_f32 v164, v168, v169\n\t"
    "v_cvt_pk_bf16_f32 v165, v170, v171\n\t"
    "v_cvt_pk_bf16_f32 v166, v172, v173\n\t"
    "v_mfma_f32_16x16x32_bf16 v[16:19], a[200:203], a[168:171], v[16:19]\n\t"
    "v_cvt_pk_bf16_f32 v167, v174, v175\n\t"
    "s_nop 1\n\t"
    "v_permlane16_swap_b32_e32 v160, v164\n\t"
    "v_permlane16_swap_b32_e32 v161, v165\n\t"
    "v_permlane16_swap_b32_e32 v162, v166\n\t"
    "v_permlane16_swap_b32_e32 v163, v167\n\t"
    "v_accvgpr_write_b32 a140, v160\n\t"
    "v_mfma_f32_16x16x32_bf16 v[24:27], a[200:203], a[184:187], v[24:27]\n\t"
    "v_accvgpr_write_b32 a141, v161\n\t"
    "v_accvgpr_write_b32 a142, v162\n\t"
    "v_accvgpr_write_b32 a143, v163\n\t"
    "v_accvgpr_write_b32 a156, v164\n\t"
    "v_accvgpr_write_b32 a157, v165\n\t"
    "v_accvgpr_write_b32 a158, v166\n\t"
    "v_accvgpr_write_b32 a159, v167\n\t"
    "v_mfma_f32_16x16x32_bf16 v[4:7], a[216:219], a[136:139], v[4:7]\n\t"
    "s_mov_b32 s99, s97\n\t"
    "s_mov_b32 s97, 61440\n\t"
    "v_pk_mul_f32 v[176:177], v[176:177], v[46:47] op_sel_hi:[1,0]\n\t"
    "v_pk_mul_f32 v[178:179], v[178:179], v[46:47] op_sel_hi:[1,0]\n\t"
    "v_pk_mul_f32 v[180:181], v[180:181], v[46:47] op_sel_hi:[1,0]\n\t"
    "v_pk_mul_f32 v[182:183], v[182:183], v[46:47] op_sel_hi:[1,0]\n\t"
    "v_pk_mul_f32 v[184:185], v[184:185], v[46:47] op_sel_hi:[1,0]\n\t"
    "v_mfma_f32_16x16x32_bf16 v[12:15], a[216:219], a[152:155], v[12:15]\n\t"
    "v_pk_mul_f32 v[186:187], v[186:187], v[46:47] op_sel_hi:[1,0]\n\t"
    "v_pk_mul_f32 v[188:189], v[188:189], v[46:47] op_sel_hi:[1,0]\n\t"
    "v_pk_mul_f32 v[190:191], v[190:191], v[46:47] op_sel_hi:[1,0]\n\t"
    "v_cvt_pk_bf16_f32 v176, v176, v177\n\t"
    "v_cvt_pk_bf16_f32 v177, v178, v179\n\t"
    "v_cvt_pk_bf16_f32 v178, v180, v181\n\t"
    "v_cvt_pk_bf16_f32 v179, v182, v183\n\t"
    "v_mfma_f32_16x16x32_bf16 v[20:23], a[216:219], a[168:171], v[20:23]\n\t"
    "v_cvt_pk_bf16_f32 v180, v184, v185\n\t"
    "v_cvt_pk_bf16_f32 v181, v186, v187\n\t"
    "v_cvt_pk_bf16_f32 v182, v188, v189\n\t"
    "v_cvt_pk_bf16_f32 v183, v190, v191\n\t"
    "s_nop 1\n\t"
    "v_permlane16_swap_b32_e32 v176, v180\n\t"
    "v_permlane16_swap_b32_e32 v177, v181\n\t"
    "v_mfma_f32_16x16x32_bf16 v[28:31], a[216:219], a[184:187], v[28:31]\n\t"
    "v_permlane16_swap_b32_e32 v178, v182\n\t"
    "v_permlane16_swap_b32_e32 v179, v183\n\t"
    "v_accvgpr_write_b32 a172, v176\n\t"
    "v_accvgpr_write_b32 a173, v177\n\t"
    "v_accvgpr_write_b32 a174, v178\n\t"
    "v_accvgpr_write_b32 a175, v179\n\t"
    "v_accvgpr_write_b32 a188, v180\n\t"
    "v_accvgpr_write_b32 a189, v181\n\t"
    "v_accvgpr_write_b32 a190, v182\n\t"
    "v_accvgpr_write_b32 a191, v183\n\t"
    "s_nop 1\n\t"
    "v_mfma_f32_16x16x32_bf16 v[0:3], a[204:207], a[140:143], v[0:3]\n\t"
    "v_mfma_f32_16x16x32_bf16 v[8:11], a[204:207], a[156:159], v[8:11]\n\t"
    "v_mfma_f32_16x16x32_bf16 v[16:19], a[204:207], a[172:175], v[16:19]\n\t"
    "v_mfma_f32_16x16x32_bf16 v[24:27], a[204:207], a[188:191], v[24:27]\n\t"
    "v_mfma_f32_16x16x32_bf16 v[4:7], a[220:223], a[140:143], v[4:7]\n\t"
    "v_mfma_f32_16x16x32_bf16 v[12:15], a[220:223], a[156:159], v[12:15]\n\t"
    "v_mfma_f32_16x16x32_bf16 v[20:23], a[220:223], a[172:175], v[20:23]\n\t"
    "v_mfma_f32_16x16x32_bf16 v[28:31], a[220:223], a[188:191], v[28:31]\n\t"
    "s_mov_b32 m0, s90\n\t"
    "s_barrier\n\t"
    "buffer_load_dwordx4 v233, s[68:71], s84 offen offset:0 lds\n\t"
    "buffer_load_dwordx4 v234, s[68:71], s84 offen offset:1024 lds\n\t"
    "buffer_load_dwordx4 v235, s[68:71], s84 offen offset:2048 lds\n\t"
    "buffer_load_dwordx4 v236, s[68:71], s84 offen offset:3072 lds\n\t"
    "s_add_u32 s84, s84, 0x4000\n\t"
    "ds_read_b128 a[192:195], v228 offset:16384\n\t"
    "ds_read_b128 a[196:199], v229 offset:16384\n\t"
    "ds_read_b128 a[200:203], v230 offset:16384\n\t"
    "ds_read_b128 a[204:207], v231 offset:16384\n\t"
    "ds_read_b128 a[208:211], v228 offset:20480\n\t"
    "ds_read_b128 a[212:215], v229 offset:20480\n\t"
    "ds_read_b128 a[216:219], v230 offset:20480\n\t"
    "ds_read_b128 a[220:223], v231 offset:20480\n\t"
    "v_max3_f32 v240, v0, v1, v2\n\t"
    "v_max3_f32 v241, v3, v4, v5\n\t"
    "v_max3_f32 v240, v240, v6, v7\n\t"
    "v_max_f32_e32 v240, v240, v241\n\t"
    "v_mov_b32_e32 v241, v240\n\t"
    "v_max3_f32 v242, v8, v9, v10\n\t"
    "v_max3_f32 v243, v11, v12, v13\n\t"
    "v_max3_f32 v242, v242, v14, v15\n\t"
    "v_max_f32_e32 v242, v242, v243\n\t"
    "v_mov_b32_e32 v243, v242\n\t"
    "v_max3_f32 v244, v16, v17, v18\n\t"
    "v_max3_f32 v245, v19, v20, v21\n\t"
    "v_max3_f32 v244, v244, v22, v23\n\t"
    "v_max_f32_e32 v244, v244, v245\n\t"
    "v_mov_b32_e32 v245, v244\n\t"
    "v_max3_f32 v246, v24, v25, v26\n\t"
    "v_max3_f32 v247, v27, v28, v29\n\t"
    "v_max3_f32 v246, v246, v30, v31\n\t"
    "v_max_f32_e32 v246, v246, v247\n\t"
    "v_mov_b32_e32 v247, v246\n\t"
    "s_nop 1\n\t"
    "v_permlane16_swap_b32_e32 v240, v241\n\t"
    "v_permlane16_swap_b32_e32 v242, v243\n\t"
    "v_permlane16_swap_b32_e32 v244, v245\n\t"
    "v_permlane16_swap_b32_e32 v246, v247\n\t"
    "v_max_f32_e32 v240, v240, v241\n\t"
    "v_mov_b32_e32 v241, v240\n\t"
    "v_max_f32_e32 v242, v242, v243\n\t"
    "v_mov_b32_e32 v243, v242\n\t"
    "v_max_f32_e32 v244, v244, v245\n\t"
    "v_mov_b32_e32 v245, v244\n\t"
    "v_max_f32_e32 v246, v246, v247\n\t"
    "v_mov_b32_e32 v247, v246\n\t"
    "s_nop 1\n\t"
    "v_permlane32_swap_b32_e32 v240, v241\n\t"
    "v_permlane32_swap_b32_e32 v242, v243\n\t"
    "v_permlane32_swap_b32_e32 v244, v245\n\t"
    "v_permlane32_swap_b32_e32 v246, v247\n\t"
    "v_max_f32_e32 v240, v240, v241\n\t"
    "v_xor_b32_e32 v240, 0x80000000, v240\n\t"
    "v_max_f32_e32 v242, v242, v243\n\t"
    "v_xor_b32_e32 v242, 0x80000000, v242\n\t"
    "v_max_f32_e32 v244, v244, v245\n\t"
    "v_xor_b32_e32 v244, 0x80000000, v244\n\t"
    "v_max_f32_e32 v246, v246, v247\n\t"
    "v_xor_b32_e32 v246, 0x80000000, v246\n\t"
    "v_mov_b32_e32 v128, v240\n\t"
    "v_mov_b32_e32 v129, v240\n\t"
    "v_mov_b32_e32 v130, v240\n\t"
    "v_mov_b32_e32 v131, v240\n\t"
    "v_mov_b32_e32 v132, v242\n\t"
    "v_mov_b32_e32 v133, v242\n\t"
    "v_mov_b32_e32 v134, v242\n\t"
    "v_mov_b32_e32 v135, v242\n\t"
    "v_mov_b32_e32 v136, v244\n\t"
    "v_mov_b32_e32 v137, v244\n\t"
    "v_mov_b32_e32 v138, v244\n\t"
    "v_mov_b32_e32 v139, v244\n\t"
    "v_mov_b32_e32 v140, v246\n\t"
    "v_mov_b32_e32 v141, v246\n\t"
    "v_mov_b32_e32 v142, v246\n\t"
    "v_mov_b32_e32 v143, v246\n\t"
    "v_add_f32_e32 v0, v240, v0\n\t"
    "s_nop 0\n\t"
    "v_mfma_f32_16x16x32_bf16 v[32:35], a[224:227], a[128:131], v[128:131]\n\t"
    "v_add_f32_e32 v1, v240, v1\n\t"
    "v_exp_f32_e32 v0, v0\n\t"
    "v_mfma_f32_16x16x32_bf16 v[40:43], a[224:227], a[144:147], v[132:135]\n\t"
    "v_add_f32_e32 v2, v240, v2\n\t"
    "v_exp_f32_e32 v1, v1\n\t"
    "v_mfma_f32_16x16x32_bf16 v[48:51], a[224:227], a[160:163], v[136:139]\n\t"
    "v_add_f32_e32 v3, v240, v3\n\t"
    "v_exp_f32_e32 v2, v2\n\t"
    "v_mfma_f32_16x16x32_bf16 v[56:59], a[224:227], a[176:179], v[140:143]\n\t"
    "v_add_f32_e32 v4, v240, v4\n\t"
    "v_cvt_pk_bf16_f32 v0, v0, v1\n\t"
    "v_exp_f32_e32 v3, v3\n\t"
    "v_mfma_f32_16x16x32_bf16 v[32:35], a[228:231], a[132:135], v[32:35]\n\t"
    "v_add_f32_e32 v5, v240, v5\n\t"
    "v_exp_f32_e32 v4, v4\n\t"
    "v_mfma_f32_16x16x32_bf16 v[40:43], a[228:231], a[148:151], v[40:43]\n\t"
    "v_add_f32_e32 v6, v240, v6\n\t"
    "v_cvt_pk_bf16_f32 v1, v2, v3\n\t"
    "v_exp_f32_e32 v5, v5\n\t"
    "v_mfma_f32_16x16x32_bf16 v[48:51], a[228:231], a[164:167], v[48:51]\n\t"
    "v_add_f32_e32 v7, v240, v7\n\t"
    "v_exp_f32_e32 v6, v6\n\t"
    "v_mfma_f32_16x16x32_bf16 v[56:59], a[228:231], a[180:183], v[56:59]\n\t"
    "v_add_f32_e32 v8, v242, v8\n\t"
    "v_cvt_pk_bf16_f32 v2, v4, v5\n\t"
    "v_exp_f32_e32 v7, v7\n\t"
    "v_mfma_f32_16x16x32_bf16 v[32:35], a[232:235], a[136:139], v[32:35]\n\t"
    "v_add_f32_e32 v9, v242, v9\n\t"
    "v_exp_f32_e32 v8, v8\n\t"
    "v_mfma_f32_16x16x32_bf16 v[40:43], a[232:235], a[152:155], v[40:43]\n\t"
    "v_add_f32_e32 v10, v242, v10\n\t"
    "v_cvt_pk_bf16_f32 v3, v6, v7\n\t"
    "v_exp_f32_e32 v9, v9\n\t"
    "v_mfma_f32_16x16x32_bf16 v[48:51], a[232:235], a[168:171], v[48:51]\n\t"
    "v_add_f32_e32 v11, v242, v11\n\t"
    "v_exp_f32_e32 v10, v10\n\t"
    "v_mfma_f32_16x16x32_bf16 v[56:59], a[232:235], a[184:187], v[56:59]\n\t"
    "v_add_f32_e32 v12, v242, v12\n\t"
    "v_cvt_pk_bf16_f32 v8, v8, v9\n\t"
    "v_exp_f32_e32 v11, v11\n\t"
    "v_mfma_f32_16x16x32_bf16 v[32:35], a[236:239], a[140:143], v[32:35]\n\t"
    "v_add_f32_e32 v13, v242, v13\n\t"
    "v_exp_f32_e32 v12, v12\n\t"
    "v_mfma_f32_16x16x32_bf16 v[40:43], a[236:239], a[156:159], v[40:43]\n\t"
    "v_add_f32_e32 v14, v242, v14\n\t"
    "v_cvt_pk_bf16_f32 v9, v10, v11\n\t"
    "v_exp_f32_e32 v13, v13\n\t"
    "v_mfma_f32_16x16x32_bf16 v[48:51], a[236:239], a[172:175], v[48:51]\n\t"
    "v_add_f32_e32 v15, v242, v15\n\t"
    "v_exp_f32_e32 v14, v14\n\t"
    "v_mfma_f32_16x16x32_bf16 v[56:59], a[236:239], a[188:191], v[56:59]\n\t"
    "v_add_f32_e32 v16, v244, v16\n\t"
    "v_cvt_pk_bf16_f32 v10, v12, v13\n\t"
    "v_exp_f32_e32 v15, v15\n\t"
    "v_mfma_f32_16x16x32_bf16 v[36:39], a[240:243], a[128:131], v[128:131]\n\t"
    "v_add_f32_e32 v17, v244, v17\n\t"
    "v_exp_f32_e32 v16, v16\n\t"
    "v_mfma_f32_16x16x32_bf16 v[44:47], a[240:243], a[144:147], v[132:135]\n\t"
    "v_add_f32_e32 v18, v244, v18\n\t"
    "v_cvt_pk_bf16_f32 v11, v14, v15\n\t"
    "v_exp_f32_e32 v17, v17\n\t"
    "v_mfma_f32_16x16x32_bf16 v[52:55], a[240:243], a[160:163], v[136:139]\n\t"
    "v_add_f32_e32 v19, v244, v19\n\t"
    "v_exp_f32_e32 v18, v18\n\t"
    "v_mfma_f32_16x16x32_bf16 v[60:63], a[240:243], a[176:179], v[140:143]\n\t"
    "v_add_f32_e32 v20, v244, v20\n\t"
    "v_cvt_pk_bf16_f32 v16, v16, v17\n\t"
    "v_exp_f32_e32 v19, v19\n\t"
    "v_mfma_f32_16x16x32_bf16 v[36:39], a[244:247], a[132:135], v[36:39]\n\t"
    "v_add_f32_e32 v21, v244, v21\n\t"
    "v_exp_f32_e32 v20, v20\n\t"
    "v_mfma_f32_16x16x32_bf16 v[44:47], a[244:247], a[148:151], v[44:47]\n\t"
    "v_add_f32_e32 v22, v244, v22\n\t"
    "v_cvt_pk_bf16_f32 v17, v18, v19\n\t"
    "v_exp_f32_e32 v21, v21\n\t"
    "v_mfma_f32_16x16x32_bf16 v[52:55], a[244:247], a[164:167], v[52:55]\n\t"
    "v_add_f32_e32 v23, v244, v23\n\t"
    "v_exp_f32_e32 v22, v22\n\t"
    "v_mfma_f32_16x16x32_bf16 v[60:63], a[244:247], a[180:183], v[60:63]\n\t"
    "v_add_f32_e32 v24, v246, v24\n\t"
    "v_cvt_pk_bf16_f32 v18, v20, v21\n\t"
    "v_exp_f32_e32 v23, v23\n\t"
    "v_mfma_f32_16x16x32_bf16 v[36:39], a[248:251], a[136:139], v[36:39]\n\t"
    "v_add_f32_e32 v25, v246, v25\n\t"
    "v_exp_f32_e32 v24, v24\n\t"
    "v_mfma_f32_16x16x32_bf16 v[44:47], a[248:251], a[152:155], v[44:47]\n\t"
    "v_add_f32_e32 v26, v246, v26\n\t"
    "v_cvt_pk_bf16_f32 v19, v22, v23\n\t"
    "v_exp_f32_e32 v25, v25\n\t"
    "v_mfma_f32_16x16x32_bf16 v[52:55], a[248:251], a[168:171], v[52:55]\n\t"
    "v_add_f32_e32 v27, v246, v27\n\t"
    "v_exp_f32_e32 v26, v26\n\t"
    "v_mfma_f32_16x16x32_bf16 v[60:63], a[248:251], a[184:187], v[60:63]\n\t"
    "v_add_f32_e32 v28, v246, v28\n\t"
    "v_cvt_pk_bf16_f32 v24, v24, v25\n\t"
    "v_exp_f32_e32 v27, v27\n\t"
    "v_mfma_f32_16x16x32_bf16 v[36:39], a[252:255], a[140:143], v[36:39]\n\t"
    "v_add_f32_e32 v29, v246, v29\n\t"
    "v_exp_f32_e32 v28, v28\n\t"
    "v_mfma_f32_16x16x32_bf16 v[44:47], a[252:255], a[156:159], v[44:47]\n\t"
    "v_add_f32_e32 v30, v246, v30\n\t"
    "v_cvt_pk_bf16_f32 v25, v26, v27\n\t"
    "v_exp_f32_e32 v29, v29\n\t"
    "v_mfma_f32_16x16x32_bf16 v[52:55], a[252:255], a[172:175], v[52:55]\n\t"
    "v_add_f32_e32 v31, v246, v31\n\t"
    "v_exp_f32_e32 v30, v30\n\t"
    "v_mfma_f32_16x16x32_bf16 v[60:63], a[252:255], a[188:191], v[60:63]\n\t"
    "v_cvt_pk_bf16_f32 v26, v28, v29\n\t"
    "v_exp_f32_e32 v31, v31\n\t"
    "s_waitcnt lgkmcnt(0)\n\t"
    "v_cvt_pk_bf16_f32 v27, v30, v31\n\t"
    "v_mfma_f32_16x16x32_bf16 v[64:67], a[192:195], a[128:131], v[128:131]\n\t"
    "v_exp_f32_e32 v32, v32\n\t"
    "v_mfma_f32_16x16x32_bf16 v[72:75], a[192:195], a[144:147], v[132:135]\n\t"
    "ds_read_b128 a[224:227], v228 offset:24576\n\t"
    "ds_read_b64_tr_b16 v[160:161], v232 offset:32768\n\t"
    "s_cmp_lt_u32 s85, 0x80000\n\t"
    "s_cselect_b32 m0, s95, s97\n\t"
    "v_mfma_f32_16x16x32_bf16 v[80:83], a[192:195], a[160:163], v[136:139]\n\t"
    "v_exp_f32_e32 v33, v33\n\t"
    "v_mfma_f32_16x16x32_bf16 v[88:91], a[192:195], a[176:179], v[140:143]\n\t"
    "ds_read_b128 a[228:231], v229 offset:24576\n\t"
    "ds_read_b64_tr_b16 v[162:163], v232 offset:36864\n\t"
    "v_mfma_f32_16x16x32_bf16 v[64:67], a[196:199], a[132:135], v[64:67]\n\t"
    "v_exp_f32_e32 v34, v34\n\t"
    "v_mfma_f32_16x16x32_bf16 v[72:75], a[196:199], a[148:151], v[72:75]\n\t"
    "v_cvt_pk_bf16_f32 v32, v32, v33\n\t"
    "ds_read_b128 a[232:235], v230 offset:24576\n\t"
    "ds_read_b64_tr_b16 v[164:165], v232 offset:33024\n\t"
    "v_mfma_f32_16x16x32_bf16 v[80:83], a[196:199], a[164:167], v[80:83]\n\t"
    "v_exp_f32_e32 v35, v35\n\t"
    "v_mfma_f32_16x16x32_bf16 v[88:91], a[196:199], a[180:183], v[88:91]\n\t"
    "ds_read_b128 a[236:239], v231 offset:24576\n\t"
    "ds_read_b64_tr_b16 v[166:167], v232 offset:37120\n\t"
    "v_mfma_f32_16x16x32_bf16 v[64:67], a[200:203], a[136:139], v[64:67]\n\t"
    "v_exp_f32_e32 v36, v36\n\t"
    "v_mfma_f32_16x16x32_bf16 v[72:75], a[200:203], a[152:155], v[72:75]\n\t"
    "v_cvt_pk_bf16_f32 v33, v34, v35\n\t"
    "ds_read_b128 a[240:243], v228 offset:28672\n\t"
    "ds_read_b64_tr_b16 v[168:169], v232 offset:33280\n\t"
    "v_mfma_f32_16x16x32_bf16 v[80:83], a[200:203], a[168:171], v[80:83]\n\t"
    "v_exp_f32_e32 v37, v37\n\t"
    "v_mfma_f32_16x16x32_bf16 v[88:91], a[200:203], a[184:187], v[88:91]\n\t"
    "ds_read_b128 a[244:247], v229 offset:28672\n\t"
    "ds_read_b64_tr_b16 v[170:171], v232 offset:37376\n\t"
    "v_mfma_f32_16x16x32_bf16 v[64:67], a[204:207], a[140:143], v[64:67]\n\t"
    "v_exp_f32_e32 v38, v38\n\t"
    "v_mfma_f32_16x16x32_bf16 v[72:75], a[204:207], a[156:159], v[72:75]\n\t"
    "v_cvt_pk_bf16_f32 v34, v36, v37\n\t"
    "ds_read_b128 a[248:251], v230 offset:28672\n\t"
    "ds_read_b64_tr_b16 v[172:173], v232 offset:33536\n\t"
    "v_mfma_f32_16x16x32_bf16 v[80:83], a[204:207], a[172:175], v[80:83]\n\t"
    "v_exp_f32_e32 v39, v39\n\t"
    "v_mfma_f32_16x16x32_bf16 v[88:91], a[204:207], a[188:191], v[88:91]\n\t"
    "ds_read_b128 a[252:255], v231 offset:28672\n\t"
    "ds_read_b64_tr_b16 v[174:175], v232 offset:37632\n\t"
    "v_mfma_f32_16x16x32_bf16 v[68:71], a[208:211], a[128:131], v[128:131]\n\t"
    "v_exp_f32_e32 v40, v40\n\t"
    "v_mfma_f32_16x16x32_bf16 v[76:79], a[208:211], a[144:147], v[132:135]\n\t"
    "v_cvt_pk_bf16_f32 v35, v38, v39\n\t"
    "ds_read_b64_tr_b16 v[176:177], v232 offset:33792\n\t"
    "v_mfma_f32_16x16x32_bf16 v[84:87], a[208:211], a[160:163], v[136:139]\n\t"
    "v_exp_f32_e32 v41, v41\n\t"
    "v_mfma_f32_16x16x32_bf16 v[92:95], a[208:211], a[176:179], v[140:143]\n\t"
    "ds_read_b64_tr_b16 v[178:179], v232 offset:37888\n\t"
    "ds_read_b64_tr_b16 v[180:181], v232 offset:34048\n\t"
    "v_mfma_f32_16x16x32_bf16 v[68:71], a[212:215], a[132:135], v[68:71]\n\t"
    "v_exp_f32_e32 v42, v42\n\t"
    "v_mfma_f32_16x16x32_bf16 v[76:79], a[212:215], a[148:151], v[76:79]\n\t"
    "v_cvt_pk_bf16_f32 v40, v40, v41\n\t"
    "ds_read_b64_tr_b16 v[182:183], v232 offset:38144\n\t"
    "v_mfma_f32_16x16x32_bf16 v[84:87], a[212:215], a[164:167], v[84:87]\n\t"
    "v_exp_f32_e32 v43, v43\n\t"
    "v_mfma_f32_16x16x32_bf16 v[92:95], a[212:215], a[180:183], v[92:95]\n\t"
    "ds_read_b64_tr_b16 v[184:185], v232 offset:34304\n\t"
    "ds_read_b64_tr_b16 v[186:187], v232 offset:38400\n\t"
    "v_mfma_f32_16x16x32_bf16 v[68:71], a[216:219], a[136:139], v[68:71]\n\t"
    "v_exp_f32_e32 v44, v44\n\t"
    "v_mfma_f32_16x16x32_bf16 v[76:79], a[216:219], a[152:155], v[76:79]\n\t"
    "v_cvt_pk_bf16_f32 v41, v42, v43\n\t"
    "ds_read_b64_tr_b16 v[188:189], v232 offset:34560\n\t"
    "v_mfma_f32_16x16x32_bf16 v[84:87], a[216:219], a[168:171], v[84:87]\n\t"
    "v_exp_f32_e32 v45, v45\n\t"
    "v_mfma_f32_16x16x32_bf16 v[92:95], a[216:219], a[184:187], v[92:95]\n\t"
    "ds_read_b64_tr_b16 v[190:191], v232 offset:38656\n\t"
    "ds_read_b64_tr_b16 v[192:193], v232 offset:40960\n\t"
    "v_mfma_f32_16x16x32_bf16 v[68:71], a[220:223], a[140:143], v[68:71]\n\t"
    "v_exp_f32_e32 v46, v46\n\t"
    "v_mfma_f32_16x16x32_bf16 v[76:79], a[220:223], a[156:159], v[76:79]\n\t"
    "v_cvt_pk_bf16_f32 v42, v44, v45\n\t"
    "ds_read_b64_tr_b16 v[194:195], v232 offset:45056\n\t"
    "v_mfma_f32_16x16x32_bf16 v[84:87], a[220:223], a[172:175], v[84:87]\n\t"
    "v_exp_f32_e32 v47, v47\n\t"
    "v_mfma_f32_16x16x32_bf16 v[92:95], a[220:223], a[188:191], v[92:95]\n\t"
    "s_waitcnt lgkmcnt(8)\n\t"
    "ds_read_b64_tr_b16 v[196:197], v232 offset:41216\n\t"
    "ds_read_b64_tr_b16 v[198:199], v232 offset:45312\n\t"
    "v_mfma_f32_16x16x32_bf16 v[96:99], a[224:227], a[128:131], v[128:131]\n\t"
    "v_exp_f32_e32 v48, v48\n\t"
    "v_mfma_f32_16x16x32_bf16 v[104:107], a[224:227], a[144:147], v[132:135]\n\t"
    "v_cvt_pk_bf16_f32 v43, v46, v47\n\t"
    "ds_read_b64_tr_b16 v[200:201], v232 offset:41472\n\t"
    "v_mfma_f32_16x16x32_bf16 v[112:115], a[224:227], a[160:163], v[136:139]\n\t"
    "v_exp_f32_e32 v49, v49\n\t"
    "v_mfma_f32_16x16x32_bf16 v[120:123], a[224:227], a[176:179], v[140:143]\n\t"
    "ds_read_b64_tr_b16 v[202:203], v232 offset:45568\n\t"
    "ds_read_b64_tr_b16 v[204:205], v232 offset:41728\n\t"
    "v_mfma_f32_16x16x32_bf16 v[96:99], a[228:231], a[132:135], v[96:99]\n\t"
    "v_exp_f32_e32 v50, v50\n\t"
    "v_mfma_f32_16x16x32_bf16 v[104:107], a[228:231], a[148:151], v[104:107]\n\t"
    "v_cvt_pk_bf16_f32 v48, v48, v49\n\t"
    "ds_read_b64_tr_b16 v[206:207], v232 offset:45824\n\t"
    "v_mfma_f32_16x16x32_bf16 v[112:115], a[228:231], a[164:167], v[112:115]\n\t"
    "v_exp_f32_e32 v51, v51\n\t"
    "v_mfma_f32_16x16x32_bf16 v[120:123], a[228:231], a[180:183], v[120:123]\n\t"
    "ds_read_b64_tr_b16 v[208:209], v232 offset:41984\n\t"
    "ds_read_b64_tr_b16 v[210:211], v232 offset:46080\n\t"
    "v_mfma_f32_16x16x32_bf16 v[96:99], a[232:235], a[136:139], v[96:99]\n\t"
    "v_exp_f32_e32 v52, v52\n\t"
    "v_mfma_f32_16x16x32_bf16 v[104:107], a[232:235], a[152:155], v[104:107]\n\t"
    "v_cvt_pk_bf16_f32 v49, v50, v51\n\t"
    "ds_read_b64_tr_b16 v[212:213], v232 offset:42240\n\t"
    "v_mfma_f32_16x16x32_bf16 v[112:115], a[232:235], a[168:171], v[112:115]\n\t"
    "v_exp_f32_e32 v53, v53\n\t"
    "v_mfma_f32_16x16x32_bf16 v[120:123], a[232:235], a[184:187], v[120:123]\n\t"
    "ds_read_b64_tr_b16 v[214:215], v232 offset:46336\n\t"
    "ds_read_b64_tr_b16 v[216:217], v232 offset:42496\n\t"
    "v_mfma_f32_16x16x32_bf16 v[96:99], a[236:239], a[140:143], v[96:99]\n\t"
    "v_exp_f32_e32 v54, v54\n\t"
    "v_mfma_f32_16x16x32_bf16 v[104:107], a[236:239], a[156:159], v[104:107]\n\t"
    "v_cvt_pk_bf16_f32 v50, v52, v53\n\t"
    "ds_read_b64_tr_b16 v[218:219], v232 offset:46592\n\t"
    "v_mfma_f32_16x16x32_bf16 v[112:115], a[236:239], a[172:175], v[112:115]\n\t"
    "v_exp_f32_e32 v55, v55\n\t"
    "v_mfma_f32_16x16x32_bf16 v[120:123], a[236:239], a[188:191], v[120:123]\n\t"
    "ds_read_b64_tr_b16 v[220:221], v232 offset:42752\n\t"
    "ds_read_b64_tr_b16 v[222:223], v232 offset:46848\n\t"
    "v_mfma_f32_16x16x32_bf16 v[100:103], a[240:243], a[128:131], v[128:131]\n\t"
    "v_exp_f32_e32 v56, v56\n\t"
    "v_mfma_f32_16x16x32_bf16 v[108:111], a[240:243], a[144:147], v[132:135]\n\t"
    "v_cvt_pk_bf16_f32 v51, v54, v55\n\t"
    "v_mfma_f32_16x16x32_bf16 v[116:119], a[240:243], a[160:163], v[136:139]\n\t"
    "v_exp_f32_e32 v57, v57\n\t"
    "v_mfma_f32_16x16x32_bf16 v[124:127], a[240:243], a[176:179], v[140:143]\n\t"
    "buffer_load_dwordx4 v237, s[72:75], s85 offen offset:0 lds\n\t"
    "v_mfma_f32_16x16x32_bf16 v[100:103], a[244:247], a[132:135], v[100:103]\n\t"
    "v_exp_f32_e32 v58, v58\n\t"
    "v_mfma_f32_16x16x32_bf16 v[108:111], a[244:247], a[148:151], v[108:111]\n\t"
    "v_cvt_pk_bf16_f32 v56, v56, v57\n\t"
    "v_mfma_f32_16x16x32_bf16 v[116:119], a[244:247], a[164:167], v[116:119]\n\t"
    "v_exp_f32_e32 v59, v59\n\t"
    "v_mfma_f32_16x16x32_bf16 v[124:127], a[244:247], a[180:183], v[124:127]\n\t"
    "buffer_load_dwordx4 v238, s[72:75], s85 offen offset:1024 lds\n\t"
    "v_mfma_f32_16x16x32_bf16 v[100:103], a[248:251], a[136:139], v[100:103]\n\t"
    "v_exp_f32_e32 v60, v60\n\t"
    "v_mfma_f32_16x16x32_bf16 v[108:111], a[248:251], a[152:155], v[108:111]\n\t"
    "v_cvt_pk_bf16_f32 v57, v58, v59\n\t"
    "v_mfma_f32_16x16x32_bf16 v[116:119], a[248:251], a[168:171], v[116:119]\n\t"
    "v_exp_f32_e32 v61, v61\n\t"
    "v_mfma_f32_16x16x32_bf16 v[124:127], a[248:251], a[184:187], v[124:127]\n\t"
    "buffer_load_dwordx4 v237, s[72:75], s85 offen offset:2048 lds\n\t"
    "v_mfma_f32_16x16x32_bf16 v[100:103], a[252:255], a[140:143], v[100:103]\n\t"
    "v_exp_f32_e32 v62, v62\n\t"
    "v_mfma_f32_16x16x32_bf16 v[108:111], a[252:255], a[156:159], v[108:111]\n\t"
    "v_cvt_pk_bf16_f32 v58, v60, v61\n\t"
    "v_mfma_f32_16x16x32_bf16 v[116:119], a[252:255], a[172:175], v[116:119]\n\t"
    "v_exp_f32_e32 v63, v63\n\t"
    "v_mfma_f32_16x16x32_bf16 v[124:127], a[252:255], a[188:191], v[124:127]\n\t"
    "buffer_load_dwordx4 v238, s[72:75], s85 offen offset:3072 lds\n\t"
    "s_waitcnt vmcnt(8) lgkmcnt(0)\n\t"
    "s_barrier\n\t"
    "v_mfma_f32_16x16x32_bf16 a[0:3], v[160:163], v[0:3], 0\n\t"
    "v_exp_f32_e32 v64, v64\n\t"
    "v_mfma_f32_16x16x32_bf16 a[8:11], v[160:163], v[8:11], 0\n\t"
    "v_cvt_pk_bf16_f32 v59, v62, v63\n\t"
    "v_mfma_f32_16x16x32_bf16 a[16:19], v[160:163], v[16:19], 0\n\t"
    "v_exp_f32_e32 v65, v65\n\t"
    "v_mfma_f32_16x16x32_bf16 a[24:27], v[160:163], v[24:27], 0\n\t"
    "ds_read_b128 a[192:195], v228 offset:32768\n\t"
    "ds_read_b128 a[196:199], v229 offset:32768\n\t"
    "v_mfma_f32_16x16x32_bf16 a[4:7], v[164:167], v[0:3], 0\n\t"
    "v_exp_f32_e32 v66, v66\n\t"
    "v_mfma_f32_16x16x32_bf16 a[12:15], v[164:167], v[8:11], 0\n\t"
    "v_cvt_pk_bf16_f32 v64, v64, v65\n\t"
    "v_mfma_f32_16x16x32_bf16 a[20:23], v[164:167], v[16:19], 0\n\t"
    "v_exp_f32_e32 v67, v67\n\t"
    "v_mfma_f32_16x16x32_bf16 a[28:31], v[164:167], v[24:27], 0\n\t"
    "ds_read_b128 a[200:203], v230 offset:32768\n\t"
    "ds_read_b128 a[204:207], v231 offset:32768\n\t"
    "v_mfma_f32_16x16x32_bf16 a[32:35], v[168:171], v[0:3], 0\n\t"
    "v_exp_f32_e32 v68, v68\n\t"
    "v_mfma_f32_16x16x32_bf16 a[40:43], v[168:171], v[8:11], 0\n\t"
    "v_cvt_pk_bf16_f32 v65, v66, v67\n\t"
    "v_mfma_f32_16x16x32_bf16 a[48:51], v[168:171], v[16:19], 0\n\t"
    "v_exp_f32_e32 v69, v69\n\t"
    "v_mfma_f32_16x16x32_bf16 a[56:59], v[168:171], v[24:27], 0\n\t"
    "ds_read_b128 a[208:211], v228 offset:36864\n\t"
    "ds_read_b128 a[212:215], v229 offset:36864\n\t"
    "v_mfma_f32_16x16x32_bf16 a[36:39], v[172:175], v[0:3], 0\n\t"
    "v_exp_f32_e32 v70, v70\n\t"
    "v_mfma_f32_16x16x32_bf16 a[44:47], v[172:175], v[8:11], 0\n\t"
    "v_cvt_pk_bf16_f32 v66, v68, v69\n\t"
    "v_mfma_f32_16x16x32_bf16 a[52:55], v[172:175], v[16:19], 0\n\t"
    "v_exp_f32_e32 v71, v71\n\t"
    "v_mfma_f32_16x16x32_bf16 a[60:63], v[172:175], v[24:27], 0\n\t"
    "ds_read_b128 a[216:219], v230 offset:36864\n\t"
    "ds_read_b128 a[220:223], v231 offset:36864\n\t"
    "v_mfma_f32_16x16x32_bf16 a[64:67], v[176:179], v[0:3], 0\n\t"
    "v_exp_f32_e32 v72, v72\n\t"
    "v_mfma_f32_16x16x32_bf16 a[72:75], v[176:179], v[8:11], 0\n\t"
    "v_cvt_pk_bf16_f32 v67, v70, v71\n\t"
    "v_mfma_f32_16x16x32_bf16 a[80:83], v[176:179], v[16:19], 0\n\t"
    "v_exp_f32_e32 v73, v73\n\t"
    "v_mfma_f32_16x16x32_bf16 a[88:91], v[176:179], v[24:27], 0\n\t"
    "ds_read_b128 a[224:227], v228 offset:40960\n\t"
    "ds_read_b128 a[228:231], v229 offset:40960\n\t"
    "v_mfma_f32_16x16x32_bf16 a[68:71], v[180:183], v[0:3], 0\n\t"
    "v_exp_f32_e32 v74, v74\n\t"
    "v_mfma_f32_16x16x32_bf16 a[76:79], v[180:183], v[8:11], 0\n\t"
    "v_cvt_pk_bf16_f32 v72, v72, v73\n\t"
    "v_mfma_f32_16x16x32_bf16 a[84:87], v[180:183], v[16:19], 0\n\t"
    "v_exp_f32_e32 v75, v75\n\t"
    "v_mfma_f32_16x16x32_bf16 a[92:95], v[180:183], v[24:27], 0\n\t"
    "ds_read_b128 a[232:235], v230 offset:40960\n\t"
    "ds_read_b128 a[236:239], v231 offset:40960\n\t"
    "v_mfma_f32_16x16x32_bf16 a[96:99], v[184:187], v[0:3], 0\n\t"
    "v_exp_f32_e32 v76, v76\n\t"
    "v_mfma_f32_16x16x32_bf16 a[104:107], v[184:187], v[8:11], 0\n\t"
    "v_cvt_pk_bf16_f32 v73, v74, v75\n\t"
    "v_mfma_f32_16x16x32_bf16 a[112:115], v[184:187], v[16:19], 0\n\t"
    "v_exp_f32_e32 v77, v77\n\t"
    "v_mfma_f32_16x16x32_bf16 a[120:123], v[184:187], v[24:27], 0\n\t"
    "ds_read_b128 a[240:243], v228 offset:45056\n\t"
    "ds_read_b128 a[244:247], v229 offset:45056\n\t"
    "v_mfma_f32_16x16x32_bf16 a[100:103], v[188:191], v[0:3], 0\n\t"
    "v_exp_f32_e32 v78, v78\n\t"
    "v_mfma_f32_16x16x32_bf16 a[108:111], v[188:191], v[8:11], 0\n\t"
    "v_cvt_pk_bf16_f32 v74, v76, v77\n\t"
    "v_mfma_f32_16x16x32_bf16 a[116:119], v[188:191], v[16:19], 0\n\t"
    "v_exp_f32_e32 v79, v79\n\t"
    "v_mfma_f32_16x16x32_bf16 a[124:127], v[188:191], v[24:27], 0\n\t"
    "ds_read_b128 a[248:251], v230 offset:45056\n\t"
    "ds_read_b128 a[252:255], v231 offset:45056\n\t"
    "v_mfma_f32_16x16x32_bf16 v[144:147], v[224:227], v[0:3], 0\n\t"
    "v_exp_f32_e32 v80, v80\n\t"
    "v_mfma_f32_16x16x32_bf16 v[148:151], v[224:227], v[8:11], 0\n\t"
    "v_cvt_pk_bf16_f32 v75, v78, v79\n\t"
    "v_mfma_f32_16x16x32_bf16 v[152:155], v[224:227], v[16:19], 0\n\t"
    "v_exp_f32_e32 v81, v81\n\t"
    "v_mfma_f32_16x16x32_bf16 v[156:159], v[224:227], v[24:27], 0\n\t"
    "s_cmp_lt_u32 s84, 0x80000\n\t"
    "s_cselect_b32 m0, s91, s97\n\t"
    "v_mfma_f32_16x16x32_bf16 a[0:3], v[192:195], v[32:35], a[0:3]\n\t"
    "v_exp_f32_e32 v82, v82\n\t"
    "v_mfma_f32_16x16x32_bf16 a[8:11], v[192:195], v[40:43], a[8:11]\n\t"
    "v_cvt_pk_bf16_f32 v80, v80, v81\n\t"
    "v_mfma_f32_16x16x32_bf16 a[16:19], v[192:195], v[48:51], a[16:19]\n\t"
    "v_exp_f32_e32 v83, v83\n\t"
    "v_mfma_f32_16x16x32_bf16 a[24:27], v[192:195], v[56:59], a[24:27]\n\t"
    "buffer_load_dwordx4 v233, s[68:71], s84 offen offset:0 lds\n\t"
    "s_add_u32 s85, s85, 0x4000\n\t"
    "v_mfma_f32_16x16x32_bf16 a[4:7], v[196:199], v[32:35], a[4:7]\n\t"
    "v_exp_f32_e32 v84, v84\n\t"
    "v_mfma_f32_16x16x32_bf16 a[12:15], v[196:199], v[40:43], a[12:15]\n\t"
    "v_cvt_pk_bf16_f32 v81, v82, v83\n\t"
    "v_mfma_f32_16x16x32_bf16 a[20:23], v[196:199], v[48:51], a[20:23]\n\t"
    "v_exp_f32_e32 v85, v85\n\t"
    "v_mfma_f32_16x16x32_bf16 a[28:31], v[196:199], v[56:59], a[28:31]\n\t"
    "buffer_load_dwordx4 v234, s[68:71], s84 offen offset:1024 lds\n\t"
    "v_mfma_f32_16x16x32_bf16 a[32:35], v[200:203], v[32:35], a[32:35]\n\t"
    "v_exp_f32_e32 v86, v86\n\t"
    "v_mfma_f32_16x16x32_bf16 a[40:43], v[200:203], v[40:43], a[40:43]\n\t"
    "v_cvt_pk_bf16_f32 v82, v84, v85\n\t"
    "v_mfma_f32_16x16x32_bf16 a[48:51], v[200:203], v[48:51], a[48:51]\n\t"
    "v_exp_f32_e32 v87, v87\n\t"
    "v_mfma_f32_16x16x32_bf16 a[56:59], v[200:203], v[56:59], a[56:59]\n\t"
    "buffer_load_dwordx4 v235, s[68:71], s84 offen offset:2048 lds\n\t"
    "v_mfma_f32_16x16x32_bf16 a[36:39], v[204:207], v[32:35], a[36:39]\n\t"
    "v_exp_f32_e32 v88, v88\n\t"
    "v_mfma_f32_16x16x32_bf16 a[44:47], v[204:207], v[40:43], a[44:47]\n\t"
    "v_cvt_pk_bf16_f32 v83, v86, v87\n\t"
    "v_mfma_f32_16x16x32_bf16 a[52:55], v[204:207], v[48:51], a[52:55]\n\t"
    "v_exp_f32_e32 v89, v89\n\t"
    "v_mfma_f32_16x16x32_bf16 a[60:63], v[204:207], v[56:59], a[60:63]\n\t"
    "buffer_load_dwordx4 v236, s[68:71], s84 offen offset:3072 lds\n\t"
    "v_mfma_f32_16x16x32_bf16 a[64:67], v[208:211], v[32:35], a[64:67]\n\t"
    "v_exp_f32_e32 v90, v90\n\t"
    "v_mfma_f32_16x16x32_bf16 a[72:75], v[208:211], v[40:43], a[72:75]\n\t"
    "v_cvt_pk_bf16_f32 v88, v88, v89\n\t"
    "v_mfma_f32_16x16x32_bf16 a[80:83], v[208:211], v[48:51], a[80:83]\n\t"
    "v_exp_f32_e32 v91, v91\n\t"
    "v_mfma_f32_16x16x32_bf16 a[88:91], v[208:211], v[56:59], a[88:91]\n\t"
    "s_waitcnt lgkmcnt(0)\n\t"
    "ds_read_b64_tr_b16 v[160:161], v232 offset:16384\n\t"
    "v_mfma_f32_16x16x32_bf16 a[68:71], v[212:215], v[32:35], a[68:71]\n\t"
    "v_exp_f32_e32 v92, v92\n\t"
    "v_mfma_f32_16x16x32_bf16 a[76:79], v[212:215], v[40:43], a[76:79]\n\t"
    "v_cvt_pk_bf16_f32 v89, v90, v91\n\t"
    "v_mfma_f32_16x16x32_bf16 a[84:87], v[212:215], v[48:51], a[84:87]\n\t"
    "v_exp_f32_e32 v93, v93\n\t"
    "v_mfma_f32_16x16x32_bf16 a[92:95], v[212:215], v[56:59], a[92:95]\n\t"
    "ds_read_b64_tr_b16 v[162:163], v232 offset:20480\n\t"
    "ds_read_b64_tr_b16 v[164:165], v232 offset:16640\n\t"
    "v_mfma_f32_16x16x32_bf16 a[96:99], v[216:219], v[32:35], a[96:99]\n\t"
    "v_exp_f32_e32 v94, v94\n\t"
    "v_mfma_f32_16x16x32_bf16 a[104:107], v[216:219], v[40:43], a[104:107]\n\t"
    "v_cvt_pk_bf16_f32 v90, v92, v93\n\t"
    "v_mfma_f32_16x16x32_bf16 a[112:115], v[216:219], v[48:51], a[112:115]\n\t"
    "v_exp_f32_e32 v95, v95\n\t"
    "v_mfma_f32_16x16x32_bf16 a[120:123], v[216:219], v[56:59], a[120:123]\n\t"
    "ds_read_b64_tr_b16 v[166:167], v232 offset:20736\n\t"
    "ds_read_b64_tr_b16 v[168:169], v232 offset:16896\n\t"
    "v_mfma_f32_16x16x32_bf16 a[100:103], v[220:223], v[32:35], a[100:103]\n\t"
    "ds_read_b64_tr_b16 v[170:171], v232 offset:20992\n\t"
    "ds_read_b64_tr_b16 v[172:173], v232 offset:17152\n\t"
    "v_mfma_f32_16x16x32_bf16 a[108:111], v[220:223], v[40:43], a[108:111]\n\t"
    "v_cvt_pk_bf16_f32 v91, v94, v95\n\t"
    "v_mfma_f32_16x16x32_bf16 a[116:119], v[220:223], v[48:51], a[116:119]\n\t"
    "ds_read_b64_tr_b16 v[174:175], v232 offset:21248\n\t"
    "ds_read_b64_tr_b16 v[176:177], v232 offset:17408\n\t"
    "v_mfma_f32_16x16x32_bf16 a[124:127], v[220:223], v[56:59], a[124:127]\n\t"
    "ds_read_b64_tr_b16 v[178:179], v232 offset:21504\n\t"
    "s_add_u32 s84, s84, 0x4000\n\t"
    "v_mul_u32_u24_e32 v247, 2752, v255\n\t"
    "s_lshr_b32 s86, s96, 1\n\t"
    "s_mul_i32 s33, %[wid], 352256\n\t"
    "s_mul_i32 s34, %[fj], 16\n\t"
    "s_add_u32 s33, s33, s34\n\t"
    "s_add_u32 s64, s52, s33\n\t"
    "s_addc_u32 s65, s53, 0\n\t"
    "s_and_b32 s65, s65, 0xffff\n\t"
    "s_mov_b32 s66, 0\n\t"
    "v_mfma_f32_16x16x32_bf16 v[144:147], v[224:227], v[32:35], v[144:147]\n\t"
    "s_mov_b32 s67, 0x20000\n\t"
    "s_sub_u32 s33, 1409024, s33\n\t"
    "s_cmp_lt_u32 %[fj], 172\n\t"
    "s_cselect_b32 s66, s33, 0\n\t"
    "s_add_u32 m0, s86, 20480\n\t"
    "s_mov_b32 s87, 175104\n\t"
    "buffer_load_dwordx4 v247, s[64:67], 0 offen lds\n\t"
    "buffer_load_dwordx4 v247, s[64:67], s87 offen offset:1024 lds\n\t"
    "s_mul_i32 s33, %[wid], 352256\n\t"
    "v_mfma_f32_16x16x32_bf16 v[148:151], v[224:227], v[40:43], v[148:151]\n\t"
    "s_mul_i32 s34, %[fj], 16\n\t"
    "s_add_u32 s33, s33, s34\n\t"
    "s_add_u32 s64, s54, s33\n\t"
    "s_addc_u32 s65, s55, 0\n\t"
    "s_and_b32 s65, s65, 0xffff\n\t"
    "s_mov_b32 s66, 0\n\t"
    "s_mov_b32 s67, 0x20000\n\t"
    "s_sub_u32 s33, 1409024, s33\n\t"
    "v_mfma_f32_16x16x32_bf16 v[152:155], v[224:227], v[48:51], v[152:155]\n\t"
    "s_cmp_lt_u32 %[fj], 172\n\t"
    "s_cselect_b32 s66, s33, 0\n\t"
    "s_add_u32 m0, s86, 28672\n\t"
    "s_mov_b32 s87, 175104\n\t"
    "buffer_load_dwordx4 v247, s[64:67], 0 offen lds\n\t"
    "buffer_load_dwordx4 v247, s[64:67], s87 offen offset:1024 lds\n\t"
    "s_mul_i32 s33, %[pj], 131072\n\t"
    "s_mul_i32 s34, %[wid], 32768\n\t"
    "s_add_u32 s33, s33, s34\n\t"
    "v_mfma_f32_16x16x32_bf16 v[156:159], v[224:227], v[56:59], v[156:159]\n\t"
    "s_add_u32 s33, s33, 18874368\n\t"
    "s_add_u32 s64, s58, s33\n\t"
    "s_addc_u32 s65, s59, 0\n\t"
    "s_and_b32 s65, s65, 0xffff\n\t"
    "s_mov_b32 s66, 0\n\t"
    "s_mov_b32 s67, 0x20000\n\t"
    "s_mov_b32 s46, 0\n\t"
    "s_mov_b32 s47, 0\n\t"
    "s_mul_i32 s33, %[b], 128\n\t"
    "s_mul_i32 s34, %[h], 4\n\t"
    "s_add_u32 s33, s33, s34\n\t"
    "s_add_u32 s33, s33, 1024\n\t"
    "s_cmp_lg_u32 %[wid], 0\n\t"
    "s_cbranch_scc1 L_fw_%=\n\t"
    "v_mov_b32_e32 v240, %[tag]\n\t"
    "s_mov_b64 exec, 1\n\t"
    "buffer_store_dword v240, off, s[60:63], s33 sc1\n\t"
    "s_mov_b64 exec, -1\n\t"
    "L_fw_%=:\n\t"
    "s_mov_b32 s86, 5\n\t"
    ".p2align 6\n\t"
    "L_loop_%=:\n\t"
    "v_mfma_f32_16x16x32_bf16 v[0:3], a[192:195], a[128:131], v[128:131]\n\t"
    "v_exp_f32_e32 v96, v96\n\t"
    "v_mfma_f32_16x16x32_bf16 v[8:11], a[192:195], a[144:147], v[132:135]\n\t"
    "s_cmp_lt_u32 s85, 0x80000\n\t"
    "s_cselect_b32 m0, s93, s97\n\t"
    "v_mfma_f32_16x16x32_bf16 v[16:19], a[192:195], a[160:163], v[136:139]\n\t"
    "v_exp_f32_e32 v97, v97\n\t"
    "v_mfma_f32_16x16x32_bf16 v[24:27], a[192:195], a[176:179], v[140:143]\n\t"
    "ds_read_b64_tr_b16 v[180:181], v232 offset:17664\n\t"
    "ds_read_b64_tr_b16 v[182:183], v232 offset:21760\n\t"
    "v_mfma_f32_16x16x32_bf16 v[0:3], a[196:199], a[132:135], v[0:3]\n\t"
    "v_exp_f32_e32 v98, v98\n\t"
    "v_mfma_f32_16x16x32_bf16 v[8:11], a[196:199], a[148:151], v[8:11]\n\t"
    "v_cvt_pk_bf16_f32 v96, v96, v97\n\t"
    "v_mfma_f32_16x16x32_bf16 v[16:19], a[196:199], a[164:167], v[16:19]\n\t"
    "v_exp_f32_e32 v99, v99\n\t"
    "v_mfma_f32_16x16x32_bf16 v[24:27], a[196:199], a[180:183], v[24:27]\n\t"
    "ds_read_b64_tr_b16 v[184:185], v232 offset:17920\n\t"
    "ds_read_b64_tr_b16 v[186:187], v232 offset:22016\n\t"
    "v_mfma_f32_16x16x32_bf16 v[0:3], a[200:203], a[136:139], v[0:3]\n\t"
    "v_exp_f32_e32 v100, v100\n\t"
    "v_mfma_f32_16x16x32_bf16 v[8:11], a[200:203], a[152:155], v[8:11]\n\t"
    "v_cvt_pk_bf16_f32 v97, v98, v99\n\t"
    "v_mfma_f32_16x16x32_bf16 v[16:19], a[200:203], a[168:171], v[16:19]\n\t"
    "v_exp_f32_e32 v101, v101\n\t"
    "v_mfma_f32_16x16x32_bf16 v[24:27], a[200:203], a[184:187], v[24:27]\n\t"
    "ds_read_b64_tr_b16 v[188:189], v232 offset:18176\n\t"
    "ds_read_b64_tr_b16 v[190:191], v232 offset:22272\n\t"
    "v_mfma_f32_16x16x32_bf16 v[0:3], a[204:207], a[140:143], v[0:3]\n\t"
    "v_exp_f32_e32 v102, v102\n\t"
    "v_mfma_f32_16x16x32_bf16 v[8:11], a[204:207], a[156:159], v[8:11]\n\t"
    "v_cvt_pk_bf16_f32 v98, v100, v101\n\t"
    "v_mfma_f32_16x16x32_bf16 v[16:19], a[204:207], a[172:175], v[16:19]\n\t"
    "v_exp_f32_e32 v103, v103\n\t"
    "v_mfma_f32_16x16x32_bf16 v[24:27], a[204:207], a[188:191], v[24:27]\n\t"
    "ds_read_b64_tr_b16 v[192:193], v232 offset:24576\n\t"
    "ds_read_b64_tr_b16 v[194:195], v232 offset:28672\n\t"
    "v_mfma_f32_16x16x32_bf16 v[4:7], a[208:211], a[128:131], v[128:131]\n\t"
    "v_exp_f32_e32 v104, v104\n\t"
    "v_mfma_f32_16x16x32_bf16 v[12:15], a[208:211], a[144:147], v[132:135]\n\t"
    "v_cvt_pk_bf16_f32 v99, v102, v103\n\t"
    "v_mfma_f32_16x16x32_bf16 v[20:23], a[208:211], a[160:163], v[136:139]\n\t"
    "v_exp_f32_e32 v105, v105\n\t"
    "v_mfma_f32_16x16x32_bf16 v[28:31], a[208:211], a[176:179], v[140:143]\n\t"
    "ds_read_b64_tr_b16 v[196:197], v232 offset:24832\n\t"
    "ds_read_b64_tr_b16 v[198:199], v232 offset:28928\n\t"
    "v_mfma_f32_16x16x32_bf16 v[4:7], a[212:215], a[132:135], v[4:7]\n\t"
    "v_exp_f32_e32 v106, v106\n\t"
    "v_mfma_f32_16x16x32_bf16 v[12:15], a[212:215], a[148:151], v[12:15]\n\t"
    "v_cvt_pk_bf16_f32 v104, v104, v105\n\t"
    "v_mfma_f32_16x16x32_bf16 v[20:23], a[212:215], a[164:167], v[20:23]\n\t"
    "v_exp_f32_e32 v107, v107\n\t"
    "v_mfma_f32_16x16x32_bf16 v[28:31], a[212:215], a[180:183], v[28:31]\n\t"
    "ds_read_b64_tr_b16 v[200:201], v232 offset:25088\n\t"
    "ds_read_b64_tr_b16 v[202:203], v232 offset:29184\n\t"
    "v_mfma_f32_16x16x32_bf16 v[4:7], a[216:219], a[136:139], v[4:7]\n\t"
    "v_exp_f32_e32 v108, v108\n\t"
    "v_mfma_f32_16x16x32_bf16 v[12:15], a[216:219], a[152:155], v[12:15]\n\t"
    "v_cvt_pk_bf16_f32 v105, v106, v107\n\t"
    "v_mfma_f32_16x16x32_bf16 v[20:23], a[216:219], a[168:171], v[20:23]\n\t"
    "v_exp_f32_e32 v109, v109\n\t"
    "v_mfma_f32_16x16x32_bf16 v[28:31], a[216:219], a[184:187], v[28:31]\n\t"
    "ds_read_b64_tr_b16 v[204:205], v232 offset:25344\n\t"
    "ds_read_b64_tr_b16 v[206:207], v232 offset:29440\n\t"
    "v_mfma_f32_16x16x32_bf16 v[4:7], a[220:223], a[140:143], v[4:7]\n\t"
    "v_exp_f32_e32 v110, v110\n\t"
    "v_mfma_f32_16x16x32_bf16 v[12:15], a[220:223], a[156:159], v[12:15]\n\t"
    "v_cvt_pk_bf16_f32 v106, v108, v109\n\t"
    "v_mfma_f32_16x16x32_bf16 v[20:23], a[220:223], a[172:175], v[20:23]\n\t"
    "v_exp_f32_e32 v111, v111\n\t"
    "v_mfma_f32_16x16x32_bf16 v[28:31], a[220:223], a[188:191], v[28:31]\n\t"
    "ds_read_b64_tr_b16 v[208:209], v232 offset:25600\n\t"
    "ds_read_b64_tr_b16 v[210:211], v232 offset:29696\n\t"
    "v_mfma_f32_16x16x32_bf16 v[32:35], a[224:227], a[128:131], v[128:131]\n\t"
    "v_exp_f32_e32 v112, v112\n\t"
    "v_mfma_f32_16x16x32_bf16 v[40:43], a[224:227], a[144:147], v[132:135]\n\t"
    "v_cvt_pk_bf16_f32 v107, v110, v111\n\t"
    "v_mfma_f32_16x16x32_bf16 v[48:51], a[224:227], a[160:163], v[136:139]\n\t"
    "v_exp_f32_e32 v113, v113\n\t"
    "v_mfma_f32_16x16x32_bf16 v[56:59], a[224:227], a[176:179], v[140:143]\n\t"
    "ds_read_b64_tr_b16 v[212:213], v232 offset:25856\n\t"
    "ds_read_b64_tr_b16 v[214:215], v232 offset:29952\n\t"
    "v_mfma_f32_16x16x32_bf16 v[32:35], a[228:231], a[132:135], v[32:35]\n\t"
    "v_exp_f32_e32 v114, v114\n\t"
    "v_mfma_f32_16x16x32_bf16 v[40:43], a[228:231], a[148:151], v[40:43]\n\t"
    "v_cvt_pk_bf16_f32 v112, v112, v113\n\t"
    "v_mfma_f32_16x16x32_bf16 v[48:51], a[228:231], a[164:167], v[48:51]\n\t"
    "v_exp_f32_e32 v115, v115\n\t"
    "v_mfma_f32_16x16x32_bf16 v[56:59], a[228:231], a[180:183], v[56:59]\n\t"
    "ds_read_b64_tr_b16 v[216:217], v232 offset:26112\n\t"
    "ds_read_b64_tr_b16 v[218:219], v232 offset:30208\n\t"
    "v_mfma_f32_16x16x32_bf16 v[32:35], a[232:235], a[136:139], v[32:35]\n\t"
    "v_exp_f32_e32 v116, v116\n\t"
    "v_mfma_f32_16x16x32_bf16 v[40:43], a[232:235], a[152:155], v[40:43]\n\t"
    "v_cvt_pk_bf16_f32 v113, v114, v115\n\t"
    "v_mfma_f32_16x16x32_bf16 v[48:51], a[232:235], a[168:171], v[48:51]\n\t"
    "v_exp_f32_e32 v117, v117\n\t"
    "v_mfma_f32_16x16x32_bf16 v[56:59], a[232:235], a[184:187], v[56:59]\n\t"
    "ds_read_b64_tr_b16 v[220:221], v232 offset:26368\n\t"
    "ds_read_b64_tr_b16 v[222:223], v232 offset:30464\n\t"
    "v_mfma_f32_16x16x32_bf16 v[32:35], a[236:239], a[140:143], v[32:35]\n\t"
    "v_exp_f32_e32 v118, v118\n\t"
    "v_mfma_f32_16x16x32_bf16 v[40:43], a[236:239], a[156:159], v[40:43]\n\t"
    "v_cvt_pk_bf16_f32 v114, v116, v117\n\t"
    "v_mfma_f32_16x16x32_bf16 v[48:51], a[236:239], a[172:175], v[48:51]\n\t"
    "v_exp_f32_e32 v119, v119\n\t"
    "v_mfma_f32_16x16x32_bf16 v[56:59], a[236:239], a[188:191], v[56:59]\n\t"
    "buffer_load_dwordx4 v237, s[72:75], s85 offen offset:0 lds\n\t"
    "v_mfma_f32_16x16x32_bf16 v[36:39], a[240:243], a[128:131], v[128:131]\n\t"
    "v_exp_f32_e32 v120, v120\n\t"
    "v_mfma_f32_16x16x32_bf16 v[44:47], a[240:243], a[144:147], v[132:135]\n\t"
    "v_cvt_pk_bf16_f32 v115, v118, v119\n\t"
    "v_mfma_f32_16x16x32_bf16 v[52:55], a[240:243], a[160:163], v[136:139]\n\t"
    "v_exp_f32_e32 v121, v121\n\t"
    "v_mfma_f32_16x16x32_bf16 v[60:63], a[240:243], a[176:179], v[140:143]\n\t"
    "buffer_load_dwordx4 v238, s[72:75], s85 offen offset:1024 lds\n\t"
    "v_mfma_f32_16x16x32_bf16 v[36:39], a[244:247], a[132:135], v[36:39]\n\t"
    "v_exp_f32_e32 v122, v122\n\t"
    "v_mfma_f32_16x16x32_bf16 v[44:47], a[244:247], a[148:151], v[44:47]\n\t"
    "v_cvt_pk_bf16_f32 v120, v120, v121\n\t"
    "v_mfma_f32_16x16x32_bf16 v[52:55], a[244:247], a[164:167], v[52:55]\n\t"
    "v_exp_f32_e32 v123, v123\n\t"
    "v_mfma_f32_16x16x32_bf16 v[60:63], a[244:247], a[180:183], v[60:63]\n\t"
    "buffer_load_dwordx4 v237, s[72:75], s85 offen offset:2048 lds\n\t"
    "v_mfma_f32_16x16x32_bf16 v[36:39], a[248:251], a[136:139], v[36:39]\n\t"
    "v_exp_f32_e32 v124, v124\n\t"
    "v_mfma_f32_16x16x32_bf16 v[44:47], a[248:251], a[152:155], v[44:47]\n\t"
    "v_cvt_pk_bf16_f32 v121, v122, v123\n\t"
    "v_mfma_f32_16x16x32_bf16 v[52:55], a[248:251], a[168:171], v[52:55]\n\t"
    "v_exp_f32_e32 v125, v125\n\t"
    "v_mfma_f32_16x16x32_bf16 v[60:63], a[248:251], a[184:187], v[60:63]\n\t"
    "buffer_load_dwordx4 v238, s[72:75], s85 offen offset:3072 lds\n\t"
    "v_mfma_f32_16x16x32_bf16 v[36:39], a[252:255], a[140:143], v[36:39]\n\t"
    "v_exp_f32_e32 v126, v126\n\t"
    "v_mfma_f32_16x16x32_bf16 v[44:47], a[252:255], a[156:159], v[44:47]\n\t"
    "v_cvt_pk_bf16_f32 v122, v124, v125\n\t"
    "v_mfma_f32_16x16x32_bf16 v[52:55], a[252:255], a[172:175], v[52:55]\n\t"
    "v_exp_f32_e32 v127, v127\n\t"
    "v_mfma_f32_16x16x32_bf16 v[60:63], a[252:255], a[188:191], v[60:63]\n\t"
    "s_waitcnt vmcnt(8) lgkmcnt(0)\n\t"
    "s_barrier\n\t"
    "v_mfma_f32_16x16x32_bf16 a[0:3], v[160:163], v[64:67], a[0:3]\n\t"
    "v_exp_f32_e32 v0, v0\n\t"
    "v_mfma_f32_16x16x32_bf16 a[8:11], v[160:163], v[72:75], a[8:11]\n\t"
    "v_cvt_pk_bf16_f32 v123, v126, v127\n\t"
    "v_mfma_f32_16x16x32_bf16 a[16:19], v[160:163], v[80:83], a[16:19]\n\t"
    "v_exp_f32_e32 v1, v1\n\t"
    "v_mfma_f32_16x16x32_bf16 a[24:27], v[160:163], v[88:91], a[24:27]\n\t"
    "ds_read_b128 a[192:195], v228 offset:0\n\t"
    "ds_read_b128 a[196:199], v229 offset:0\n\t"
    "v_mfma_f32_16x16x32_bf16 a[4:7], v[164:167], v[64:67], a[4:7]\n\t"
    "v_exp_f32_e32 v2, v2\n\t"
    "v_mfma_f32_16x16x32_bf16 a[12:15], v[164:167], v[72:75], a[12:15]\n\t"
    "v_cvt_pk_bf16_f32 v0, v0, v1\n\t"
    "v_mfma_f32_16x16x32_bf16 a[20:23], v[164:167], v[80:83], a[20:23]\n\t"
    "v_exp_f32_e32 v3, v3\n\t"
    "v_mfma_f32_16x16x32_bf16 a[28:31], v[164:167], v[88:91], a[28:31]\n\t"
    "ds_read_b128 a[200:203], v230 offset:0\n\t"
    "ds_read_b128 a[204:207], v231 offset:0\n\t"
    "v_mfma_f32_16x16x32_bf16 a[32:35], v[168:171], v[64:67], a[32:35]\n\t"
    "v_exp_f32_e32 v4, v4\n\t"
    "v_mfma_f32_16x16x32_bf16 a[40:43], v[168:171], v[72:75], a[40:43]\n\t"
    "v_cvt_pk_bf16_f32 v1, v2, v3\n\t"
    "v_mfma_f32_16x16x32_bf16 a[48:51], v[168:171], v[80:83], a[48:51]\n\t"
    "v_exp_f32_e32 v5, v5\n\t"
    "v_mfma_f32_16x16x32_bf16 a[56:59], v[168:171], v[88:91], a[56:59]\n\t"
    "ds_read_b128 a[208:211], v228 offset:4096\n\t"
    "ds_read_b128 a[212:215], v229 offset:4096\n\t"
    "v_mfma_f32_16x16x32_bf16 a[36:39], v[172:175], v[64:67], a[36:39]\n\t"
    "v_exp_f32_e32 v6, v6\n\t"
    "v_mfma_f32_16x16x32_bf16 a[44:47], v[172:175], v[72:75], a[44:47]\n\t"
    "v_cvt_pk_bf16_f32 v2, v4, v5\n\t"
    "v_mfma_f32_16x16x32_bf16 a[52:55], v[172:175], v[80:83], a[52:55]\n\t"
    "v_exp_f32_e32 v7, v7\n\t"
    "v_mfma_f32_16x16x32_bf16 a[60:63], v[172:175], v[88:91], a[60:63]\n\t"
    "ds_read_b128 a[216:219], v230 offset:4096\n\t"
    "ds_read_b128 a[220:223], v231 offset:4096\n\t"
    "v_mfma_f32_16x16x32_bf16 a[64:67], v[176:179], v[64:67], a[64:67]\n\t"
    "v_exp_f32_e32 v8, v8\n\t"
    "v_mfma_f32_16x16x32_bf16 a[72:75], v[176:179], v[72:75], a[72:75]\n\t"
    "v_cvt_pk_bf16_f32 v3, v6, v7\n\t"
    "v_mfma_f32_16x16x32_bf16 a[80:83], v[176:179], v[80:83], a[80:83]\n\t"
    "v_exp_f32_e32 v9, v9\n\t"
    "v_mfma_f32_16x16x32_bf16 a[88:91], v[176:179], v[88:91], a[88:91]\n\t"
    "ds_read_b128 a[224:227], v228 offset:8192\n\t"
    "ds_read_b128 a[228:231], v229 offset:8192\n\t"
    "v_mfma_f32_16x16x32_bf16 a[68:71], v[180:183], v[64:67], a[68:71]\n\t"
    "v_exp_f32_e32 v10, v10\n\t"
    "v_mfma_f32_16x16x32_bf16 a[76:79], v[180:183], v[72:75], a[76:79]\n\t"
    "v_cvt_pk_bf16_f32 v8, v8, v9\n\t"
    "v_mfma_f32_16x16x32_bf16 a[84:87], v[180:183], v[80:83], a[84:87]\n\t"
    "v_exp_f32_e32 v11, v11\n\t"
    "v_mfma_f32_16x16x32_bf16 a[92:95], v[180:183], v[88:91], a[92:95]\n\t"
    "ds_read_b128 a[232:235], v230 offset:8192\n\t"
    "ds_read_b128 a[236:239], v231 offset:8192\n\t"
    "v_mfma_f32_16x16x32_bf16 a[96:99], v[184:187], v[64:67], a[96:99]\n\t"
    "v_exp_f32_e32 v12, v12\n\t"
    "v_mfma_f32_16x16x32_bf16 a[104:107], v[184:187], v[72:75], a[104:107]\n\t"
    "v_cvt_pk_bf16_f32 v9, v10, v11\n\t"
    "v_mfma_f32_16x16x32_bf16 a[112:115], v[184:187], v[80:83], a[112:115]\n\t"
    "v_exp_f32_e32 v13, v13\n\t"
    "v_mfma_f32_16x16x32_bf16 a[120:123], v[184:187], v[88:91], a[120:123]\n\t"
    "ds_read_b128 a[240:243], v228 offset:12288\n\t"
    "ds_read_b128 a[244:247], v229 offset:12288\n\t"
    "v_mfma_f32_16x16x32_bf16 a[100:103], v[188:191], v[64:67], a[100:103]\n\t"
    "v_exp_f32_e32 v14, v14\n\t"
    "v_mfma_f32_16x16x32_bf16 a[108:111], v[188:191], v[72:75], a[108:111]\n\t"
    "v_cvt_pk_bf16_f32 v10, v12, v13\n\t"
    "v_mfma_f32_16x16x32_bf16 a[116:119], v[188:191], v[80:83], a[116:119]\n\t"
    "v_exp_f32_e32 v15, v15\n\t"
    "v_mfma_f32_16x16x32_bf16 a[124:127], v[188:191], v[88:91], a[124:127]\n\t"
    "ds_read_b128 a[248:251], v230 offset:12288\n\t"
    "ds_read_b128 a[252:255], v231 offset:12288\n\t"
    "v_mfma_f32_16x16x32_bf16 v[144:147], v[224:227], v[64:67], v[144:147]\n\t"
    "v_exp_f32_e32 v16, v16\n\t"
    "v_mfma_f32_16x16x32_bf16 v[148:151], v[224:227], v[72:75], v[148:151]\n\t"
    "v_cvt_pk_bf16_f32 v11, v14, v15\n\t"
    "v_mfma_f32_16x16x32_bf16 v[152:155], v[224:227], v[80:83], v[152:155]\n\t"
    "v_exp_f32_e32 v17, v17\n\t"
    "v_mfma_f32_16x16x32_bf16 v[156:159], v[224:227], v[88:91], v[156:159]\n\t"
    "s_cmp_lt_u32 s84, 0x80000\n\t"
    "s_cselect_b32 m0, s92, s97\n\t"
    "v_mfma_f32_16x16x32_bf16 a[0:3], v[192:195], v[96:99], a[0:3]\n\t"
    "v_exp_f32_e32 v18, v18\n\t"
    "v_mfma_f32_16x16x32_bf16 a[8:11], v[192:195], v[104:107], a[8:11]\n\t"
    "v_cvt_pk_bf16_f32 v16, v16, v17\n\t"
    "v_mfma_f32_16x16x32_bf16 a[16:19], v[192:195], v[112:115], a[16:19]\n\t"
    "v_exp_f32_e32 v19, v19\n\t"
    "v_mfma_f32_16x16x32_bf16 a[24:27], v[192:195], v[120:123], a[24:27]\n\t"
    "buffer_load_dwordx4 v233, s[68:71], s84 offen offset:0 lds\n\t"
    "s_add_u32 s85, s85, 0x4000\n\t"
    "v_mfma_f32_16x16x32_bf16 a[4:7], v[196:199], v[96:99], a[4:7]\n\t"
    "v_exp_f32_e32 v20, v20\n\t"
    "v_mfma_f32_16x16x32_bf16 a[12:15], v[196:199], v[104:107], a[12:15]\n\t"
    "v_cvt_pk_bf16_f32 v17, v18, v19\n\t"
    "v_mfma_f32_16x16x32_bf16 a[20:23], v[196:199], v[112:115], a[20:23]\n\t"
    "v_exp_f32_e32 v21, v21\n\t"
    "v_mfma_f32_16x16x32_bf16 a[28:31], v[196:199], v[120:123], a[28:31]\n\t"
    "buffer_load_dwordx4 v234, s[68:71], s84 offen offset:1024 lds\n\t"
    "v_mfma_f32_16x16x32_bf16 a[32:35], v[200:203], v[96:99], a[32:35]\n\t"
    "v_exp_f32_e32 v22, v22\n\t"
    "v_mfma_f32_16x16x32_bf16 a[40:43], v[200:203], v[104:107], a[40:43]\n\t"
    "v_cvt_pk_bf16_f32 v18, v20, v21\n\t"
    "v_mfma_f32_16x16x32_bf16 a[48:51], v[200:203], v[112:115], a[48:51]\n\t"
    "v_exp_f32_e32 v23, v23\n\t"
    "v_mfma_f32_16x16x32_bf16 a[56:59], v[200:203], v[120:123], a[56:59]\n\t"
    "buffer_load_dwordx4 v235, s[68:71], s84 offen offset:2048 lds\n\t"
    "v_mfma_f32_16x16x32_bf16 a[36:39], v[204:207], v[96:99], a[36:39]\n\t"
    "v_exp_f32_e32 v24, v24\n\t"
    "v_mfma_f32_16x16x32_bf16 a[44:47], v[204:207], v[104:107], a[44:47]\n\t"
    "v_cvt_pk_bf16_f32 v19, v22, v23\n\t"
    "v_mfma_f32_16x16x32_bf16 a[52:55], v[204:207], v[112:115], a[52:55]\n\t"
    "v_exp_f32_e32 v25, v25\n\t"
    "v_mfma_f32_16x16x32_bf16 a[60:63], v[204:207], v[120:123], a[60:63]\n\t"
    "buffer_load_dwordx4 v236, s[68:71], s84 offen offset:3072 lds\n\t"
    "v_mfma_f32_16x16x32_bf16 a[64:67], v[208:211], v[96:99], a[64:67]\n\t"
    "v_exp_f32_e32 v26, v26\n\t"
    "v_mfma_f32_16x16x32_bf16 a[72:75], v[208:211], v[104:107], a[72:75]\n\t"
    "v_cvt_pk_bf16_f32 v24, v24, v25\n\t"
    "v_mfma_f32_16x16x32_bf16 a[80:83], v[208:211], v[112:115], a[80:83]\n\t"
    "v_exp_f32_e32 v27, v27\n\t"
    "v_mfma_f32_16x16x32_bf16 a[88:91], v[208:211], v[120:123], a[88:91]\n\t"
    "s_waitcnt lgkmcnt(0)\n\t"
    "ds_read_b64_tr_b16 v[160:161], v232 offset:0\n\t"
    "v_mfma_f32_16x16x32_bf16 a[68:71], v[212:215], v[96:99], a[68:71]\n\t"
    "v_exp_f32_e32 v28, v28\n\t"
    "v_mfma_f32_16x16x32_bf16 a[76:79], v[212:215], v[104:107], a[76:79]\n\t"
    "v_cvt_pk_bf16_f32 v25, v26, v27\n\t"
    "v_mfma_f32_16x16x32_bf16 a[84:87], v[212:215], v[112:115], a[84:87]\n\t"
    "v_exp_f32_e32 v29, v29\n\t"
    "v_mfma_f32_16x16x32_bf16 a[92:95], v[212:215], v[120:123], a[92:95]\n\t"
    "ds_read_b64_tr_b16 v[162:163], v232 offset:4096\n\t"
    "ds_read_b64_tr_b16 v[164:165], v232 offset:256\n\t"
    "v_mfma_f32_16x16x32_bf16 a[96:99], v[216:219], v[96:99], a[96:99]\n\t"
    "v_exp_f32_e32 v30, v30\n\t"
    "v_mfma_f32_16x16x32_bf16 a[104:107], v[216:219], v[104:107], a[104:107]\n\t"
    "v_cvt_pk_bf16_f32 v26, v28, v29\n\t"
    "v_mfma_f32_16x16x32_bf16 a[112:115], v[216:219], v[112:115], a[112:115]\n\t"
    "v_exp_f32_e32 v31, v31\n\t"
    "v_mfma_f32_16x16x32_bf16 a[120:123], v[216:219], v[120:123], a[120:123]\n\t"
    "ds_read_b64_tr_b16 v[166:167], v232 offset:4352\n\t"
    "ds_read_b64_tr_b16 v[168:169], v232 offset:512\n\t"
    "v_mfma_f32_16x16x32_bf16 a[100:103], v[220:223], v[96:99], a[100:103]\n\t"
    "ds_read_b64_tr_b16 v[170:171], v232 offset:4608\n\t"
    "ds_read_b64_tr_b16 v[172:173], v232 offset:768\n\t"
    "v_mfma_f32_16x16x32_bf16 a[108:111], v[220:223], v[104:107], a[108:111]\n\t"
    "v_cvt_pk_bf16_f32 v27, v30, v31\n\t"
    "v_mfma_f32_16x16x32_bf16 a[116:119], v[220:223], v[112:115], a[116:119]\n\t"
    "ds_read_b64_tr_b16 v[174:175], v232 offset:4864\n\t"
    "ds_read_b64_tr_b16 v[176:177], v232 offset:1024\n\t"
    "v_mfma_f32_16x16x32_bf16 a[124:127], v[220:223], v[120:123], a[124:127]\n\t"
    "ds_read_b64_tr_b16 v[178:179], v232 offset:5120\n\t"
    "s_add_u32 s84, s84, 0x4000\n\t"
    "v_mfma_f32_16x16x32_bf16 v[144:147], v[224:227], v[96:99], v[144:147]\n\t"
    "ds_read_b128 v[240:243], v248 offset:4096\n\t"
    "v_mfma_f32_16x16x32_bf16 v[148:151], v[224:227], v[104:107], v[148:151]\n\t"
    "ds_read_b128 v[244:247], v248 offset:8192\n\t"
    "v_mfma_f32_16x16x32_bf16 v[152:155], v[224:227], v[112:115], v[152:155]\n\t"
    "v_mfma_f32_16x16x32_bf16 v[156:159], v[224:227], v[120:123], v[156:159]\n\t"
    "v_mfma_f32_16x16x32_bf16 v[64:67], a[192:195], a[128:131], v[128:131]\n\t"
    "v_exp_f32_e32 v32, v32\n\t"
    "v_mfma_f32_16x16x32_bf16 v[72:75], a[192:195], a[144:147], v[132:135]\n\t"
    "s_cmp_lt_u32 s85, 0x80000\n\t"
    "s_cselect_b32 m0, s94, s97\n\t"
    "v_mfma_f32_16x16x32_bf16 v[80:83], a[192:195], a[160:163], v[136:139]\n\t"
    "v_exp_f32_e32 v33, v33\n\t"
    "v_mfma_f32_16x16x32_bf16 v[88:91], a[192:195], a[176:179], v[140:143]\n\t"
    "ds_read_b64_tr_b16 v[180:181], v232 offset:1280\n\t"
    "ds_read_b64_tr_b16 v[182:183], v232 offset:5376\n\t"
    "v_mfma_f32_16x16x32_bf16 v[64:67], a[196:199], a[132:135], v[64:67]\n\t"
    "v_exp_f32_e32 v34, v34\n\t"
    "v_mfma_f32_16x16x32_bf16 v[72:75], a[196:199], a[148:151], v[72:75]\n\t"
    "v_cvt_pk_bf16_f32 v32, v32, v33\n\t"
    "v_mfma_f32_16x16x32_bf16 v[80:83], a[196:199], a[164:167], v[80:83]\n\t"
    "v_exp_f32_e32 v35, v35\n\t"
    "v_mfma_f32_16x16x32_bf16 v[88:91], a[196:199], a[180:183], v[88:91]\n\t"
    "ds_read_b64_tr_b16 v[184:185], v232 offset:1536\n\t"
    "ds_read_b64_tr_b16 v[186:187], v232 offset:5632\n\t"
    "v_mfma_f32_16x16x32_bf16 v[64:67], a[200:203], a[136:139], v[64:67]\n\t"
    "v_exp_f32_e32 v36, v36\n\t"
    "v_mfma_f32_16x16x32_bf16 v[72:75], a[200:203], a[152:155], v[72:75]\n\t"
    "v_cvt_pk_bf16_f32 v33, v34, v35\n\t"
    "v_mfma_f32_16x16x32_bf16 v[80:83], a[200:203], a[168:171], v[80:83]\n\t"
    "v_exp_f32_e32 v37, v37\n\t"
    "v_mfma_f32_16x16x32_bf16 v[88:91], a[200:203], a[184:187], v[88:91]\n\t"
    "ds_read_b64_tr_b16 v[188:189], v232 offset:1792\n\t"
    "ds_read_b64_tr_b16 v[190:191], v232 offset:5888\n\t"
    "v_mfma_f32_16x16x32_bf16 v[64:67], a[204:207], a[140:143], v[64:67]\n\t"
    "v_exp_f32_e32 v38, v38\n\t"
    "v_mfma_f32_16x16x32_bf16 v[72:75], a[204:207], a[156:159], v[72:75]\n\t"
    "v_cvt_pk_bf16_f32 v34, v36, v37\n\t"
    "v_mfma_f32_16x16x32_bf16 v[80:83], a[204:207], a[172:175], v[80:83]\n\t"
    "v_exp_f32_e32 v39, v39\n\t"
    "v_mfma_f32_16x16x32_bf16 v[88:91], a[204:207], a[188:191], v[88:91]\n\t"
    "ds_read_b64_tr_b16 v[192:193], v232 offset:8192\n\t"
    "ds_read_b64_tr_b16 v[194:195], v232 offset:12288\n\t"
    "v_mfma_f32_16x16x32_bf16 v[68:71], a[208:211], a[128:131], v[128:131]\n\t"
    "v_exp_f32_e32 v40, v40\n\t"
    "v_mfma_f32_16x16x32_bf16 v[76:79], a[208:211], a[144:147], v[132:135]\n\t"
    "v_cvt_pk_bf16_f32 v35, v38, v39\n\t"
    "v_mfma_f32_16x16x32_bf16 v[84:87], a[208:211], a[160:163], v[136:139]\n\t"
    "v_exp_f32_e32 v41, v41\n\t"
    "v_mfma_f32_16x16x32_bf16 v[92:95], a[208:211], a[176:179], v[140:143]\n\t"
    "ds_read_b64_tr_b16 v[196:197], v232 offset:8448\n\t"
    "ds_read_b64_tr_b16 v[198:199], v232 offset:12544\n\t"
    "v_mfma_f32_16x16x32_bf16 v[68:71], a[212:215], a[132:135], v[68:71]\n\t"
    "v_exp_f32_e32 v42, v42\n\t"
    "v_mfma_f32_16x16x32_bf16 v[76:79], a[212:215], a[148:151], v[76:79]\n\t"
    "v_cvt_pk_bf16_f32 v40, v40, v41\n\t"
    "v_mfma_f32_16x16x32_bf16 v[84:87], a[212:215], a[164:167], v[84:87]\n\t"
    "v_exp_f32_e32 v43, v43\n\t"
    "v_mfma_f32_16x16x32_bf16 v[92:95], a[212:215], a[180:183], v[92:95]\n\t"
    "ds_read_b64_tr_b16 v[200:201], v232 offset:8704\n\t"
    "ds_read_b64_tr_b16 v[202:203], v232 offset:12800\n\t"
    "v_mfma_f32_16x16x32_bf16 v[68:71], a[216:219], a[136:139], v[68:71]\n\t"
    "v_exp_f32_e32 v44, v44\n\t"
    "v_mfma_f32_16x16x32_bf16 v[76:79], a[216:219], a[152:155], v[76:79]\n\t"
    "v_cvt_pk_bf16_f32 v41, v42, v43\n\t"
    "v_mfma_f32_16x16x32_bf16 v[84:87], a[216:219], a[168:171], v[84:87]\n\t"
    "v_exp_f32_e32 v45, v45\n\t"
    "v_mfma_f32_16x16x32_bf16 v[92:95], a[216:219], a[184:187], v[92:95]\n\t"
    "ds_read_b64_tr_b16 v[204:205], v232 offset:8960\n\t"
    "ds_read_b64_tr_b16 v[206:207], v232 offset:13056\n\t"
    "v_mfma_f32_16x16x32_bf16 v[68:71], a[220:223], a[140:143], v[68:71]\n\t"
    "v_exp_f32_e32 v46, v46\n\t"
    "v_mfma_f32_16x16x32_bf16 v[76:79], a[220:223], a[156:159], v[76:79]\n\t"
    "v_cvt_pk_bf16_f32 v42, v44, v45\n\t"
    "v_mfma_f32_16x16x32_bf16 v[84:87], a[220:223], a[172:175], v[84:87]\n\t"
    "v_exp_f32_e32 v47, v47\n\t"
    "v_mfma_f32_16x16x32_bf16 v[92:95], a[220:223], a[188:191], v[92:95]\n\t"
    "ds_read_b64_tr_b16 v[208:209], v232 offset:9216\n\t"
    "ds_read_b64_tr_b16 v[210:211], v232 offset:13312\n\t"
    "v_mfma_f32_16x16x32_bf16 v[96:99], a[224:227], a[128:131], v[128:131]\n\t"
    "v_exp_f32_e32 v48, v48\n\t"
    "v_mfma_f32_16x16x32_bf16 v[104:107], a[224:227], a[144:147], v[132:135]\n\t"
    "v_cvt_pk_bf16_f32 v43, v46, v47\n\t"
    "v_mfma_f32_16x16x32_bf16 v[112:115], a[224:227], a[160:163], v[136:139]\n\t"
    "v_exp_f32_e32 v49, v49\n\t"
    "v_mfma_f32_16x16x32_bf16 v[120:123], a[224:227], a[176:179], v[140:143]\n\t"
    "ds_read_b64_tr_b16 v[212:213], v232 offset:9472\n\t"
    "ds_read_b64_tr_b16 v[214:215], v232 offset:13568\n\t"
    "v_mfma_f32_16x16x32_bf16 v[96:99], a[228:231], a[132:135], v[96:99]\n\t"
    "v_exp_f32_e32 v50, v50\n\t"
    "v_mfma_f32_16x16x32_bf16 v[104:107], a[228:231], a[148:151], v[104:107]\n\t"
    "v_cvt_pk_bf16_f32 v48, v48, v49\n\t"
    "v_mfma_f32_16x16x32_bf16 v[112:115], a[228:231], a[164:167], v[112:115]\n\t"
    "v_exp_f32_e32 v51, v51\n\t"
    "v_mfma_f32_16x16x32_bf16 v[120:123], a[228:231], a[180:183], v[120:123]\n\t"
    "ds_read_b64_tr_b16 v[216:217], v232 offset:9728\n\t"
    "ds_read_b64_tr_b16 v[218:219], v232 offset:13824\n\t"
    "v_mfma_f32_16x16x32_bf16 v[96:99], a[232:235], a[136:139], v[96:99]\n\t"
    "v_exp_f32_e32 v52, v52\n\t"
    "v_mfma_f32_16x16x32_bf16 v[104:107], a[232:235], a[152:155], v[104:107]\n\t"
    "v_cvt_pk_bf16_f32 v49, v50, v51\n\t"
    "v_mfma_f32_16x16x32_bf16 v[112:115], a[232:235], a[168:171], v[112:115]\n\t"
    "v_exp_f32_e32 v53, v53\n\t"
    "v_mfma_f32_16x16x32_bf16 v[120:123], a[232:235], a[184:187], v[120:123]\n\t"
    "ds_read_b64_tr_b16 v[220:221], v232 offset:9984\n\t"
    "ds_read_b64_tr_b16 v[222:223], v232 offset:14080\n\t"
    "v_mfma_f32_16x16x32_bf16 v[96:99], a[236:239], a[140:143], v[96:99]\n\t"
    "v_exp_f32_e32 v54, v54\n\t"
    "v_mfma_f32_16x16x32_bf16 v[104:107], a[236:239], a[156:159], v[104:107]\n\t"
    "v_cvt_pk_bf16_f32 v50, v52, v53\n\t"
    "v_mfma_f32_16x16x32_bf16 v[112:115], a[236:239], a[172:175], v[112:115]\n\t"
    "v_exp_f32_e32 v55, v55\n\t"
    "v_mfma_f32_16x16x32_bf16 v[120:123], a[236:239], a[188:191], v[120:123]\n\t"
    "buffer_load_dwordx4 v237, s[72:75], s85 offen offset:0 lds\n\t"
    "v_mfma_f32_16x16x32_bf16 v[100:103], a[240:243], a[128:131], v[128:131]\n\t"
    "v_exp_f32_e32 v56, v56\n\t"
    "v_mfma_f32_16x16x32_bf16 v[108:111], a[240:243], a[144:147], v[132:135]\n\t"
    "v_cvt_pk_bf16_f32 v51, v54, v55\n\t"
    "v_mfma_f32_16x16x32_bf16 v[116:119], a[240:243], a[160:163], v[136:139]\n\t"
    "v_exp_f32_e32 v57, v57\n\t"
    "v_mfma_f32_16x16x32_bf16 v[124:127], a[240:243], a[176:179], v[140:143]\n\t"
    "buffer_load_dwordx4 v238, s[72:75], s85 offen offset:1024 lds\n\t"
    "v_mfma_f32_16x16x32_bf16 v[100:103], a[244:247], a[132:135], v[100:103]\n\t"
    "v_exp_f32_e32 v58, v58\n\t"
    "v_mfma_f32_16x16x32_bf16 v[108:111], a[244:247], a[148:151], v[108:111]\n\t"
    "v_cvt_pk_bf16_f32 v56, v56, v57\n\t"
    "v_mfma_f32_16x16x32_bf16 v[116:119], a[244:247], a[164:167], v[116:119]\n\t"
    "v_exp_f32_e32 v59, v59\n\t"
    "v_mfma_f32_16x16x32_bf16 v[124:127], a[244:247], a[180:183], v[124:127]\n\t"
    "buffer_load_dwordx4 v237, s[72:75], s85 offen offset:2048 lds\n\t"
    "v_mfma_f32_16x16x32_bf16 v[100:103], a[248:251], a[136:139], v[100:103]\n\t"
    "v_exp_f32_e32 v60, v60\n\t"
    "v_mfma_f32_16x16x32_bf16 v[108:111], a[248:251], a[152:155], v[108:111]\n\t"
    "v_cvt_pk_bf16_f32 v57, v58, v59\n\t"
    "v_mfma_f32_16x16x32_bf16 v[116:119], a[248:251], a[168:171], v[116:119]\n\t"
    "v_exp_f32_e32 v61, v61\n\t"
    "v_mfma_f32_16x16x32_bf16 v[124:127], a[248:251], a[184:187], v[124:127]\n\t"
    "buffer_load_dwordx4 v238, s[72:75], s85 offen offset:3072 lds\n\t"
    "v_mfma_f32_16x16x32_bf16 v[100:103], a[252:255], a[140:143], v[100:103]\n\t"
    "v_exp_f32_e32 v62, v62\n\t"
    "v_mfma_f32_16x16x32_bf16 v[108:111], a[252:255], a[156:159], v[108:111]\n\t"
    "v_cvt_pk_bf16_f32 v58, v60, v61\n\t"
    "v_mfma_f32_16x16x32_bf16 v[116:119], a[252:255], a[172:175], v[116:119]\n\t"
    "v_exp_f32_e32 v63, v63\n\t"
    "v_mfma_f32_16x16x32_bf16 v[124:127], a[252:255], a[188:191], v[124:127]\n\t"
    "s_waitcnt vmcnt(8) lgkmcnt(0)\n\t"
    "s_barrier\n\t"
    "v_mfma_f32_16x16x32_bf16 a[0:3], v[160:163], v[0:3], a[0:3]\n\t"
    "v_exp_f32_e32 v64, v64\n\t"
    "v_mfma_f32_16x16x32_bf16 a[8:11], v[160:163], v[8:11], a[8:11]\n\t"
    "v_cvt_pk_bf16_f32 v59, v62, v63\n\t"
    "v_mfma_f32_16x16x32_bf16 a[16:19], v[160:163], v[16:19], a[16:19]\n\t"
    "v_exp_f32_e32 v65, v65\n\t"
    "v_mfma_f32_16x16x32_bf16 a[24:27], v[160:163], v[24:27], a[24:27]\n\t"
    "ds_read_b128 a[192:195], v228 offset:16384\n\t"
    "ds_read_b128 a[196:199], v229 offset:16384\n\t"
    "v_mfma_f32_16x16x32_bf16 a[4:7], v[164:167], v[0:3], a[4:7]\n\t"
    "v_exp_f32_e32 v66, v66\n\t"
    "v_mfma_f32_16x16x32_bf16 a[12:15], v[164:167], v[8:11], a[12:15]\n\t"
    "v_cvt_pk_bf16_f32 v64, v64, v65\n\t"
    "v_mfma_f32_16x16x32_bf16 a[20:23], v[164:167], v[16:19], a[20:23]\n\t"
    "v_exp_f32_e32 v67, v67\n\t"
    "v_mfma_f32_16x16x32_bf16 a[28:31], v[164:167], v[24:27], a[28:31]\n\t"
    "ds_read_b128 a[200:203], v230 offset:16384\n\t"
    "ds_read_b128 a[204:207], v231 offset:16384\n\t"
    "v_mfma_f32_16x16x32_bf16 a[32:35], v[168:171], v[0:3], a[32:35]\n\t"
    "v_exp_f32_e32 v68, v68\n\t"
    "v_mfma_f32_16x16x32_bf16 a[40:43], v[168:171], v[8:11], a[40:43]\n\t"
    "v_cvt_pk_bf16_f32 v65, v66, v67\n\t"
    "v_mfma_f32_16x16x32_bf16 a[48:51], v[168:171], v[16:19], a[48:51]\n\t"
    "v_exp_f32_e32 v69, v69\n\t"
    "v_mfma_f32_16x16x32_bf16 a[56:59], v[168:171], v[24:27], a[56:59]\n\t"
    "ds_read_b128 a[208:211], v228 offset:20480\n\t"
    "ds_read_b128 a[212:215], v229 offset:20480\n\t"
    "v_mfma_f32_16x16x32_bf16 a[36:39], v[172:175], v[0:3], a[36:39]\n\t"
    "v_exp_f32_e32 v70, v70\n\t"
    "v_mfma_f32_16x16x32_bf16 a[44:47], v[172:175], v[8:11], a[44:47]\n\t"
    "v_cvt_pk_bf16_f32 v66, v68, v69\n\t"
    "v_mfma_f32_16x16x32_bf16 a[52:55], v[172:175], v[16:19], a[52:55]\n\t"
    "v_exp_f32_e32 v71, v71\n\t"
    "v_mfma_f32_16x16x32_bf16 a[60:63], v[172:175], v[24:27], a[60:63]\n\t"
    "ds_read_b128 a[216:219], v230 offset:20480\n\t"
    "ds_read_b128 a[220:223], v231 offset:20480\n\t"
    "v_mfma_f32_16x16x32_bf16 a[64:67], v[176:179], v[0:3], a[64:67]\n\t"
    "v_exp_f32_e32 v72, v72\n\t"
    "v_mfma_f32_16x16x32_bf16 a[72:75], v[176:179], v[8:11], a[72:75]\n\t"
    "v_cvt_pk_bf16_f32 v67, v70, v71\n\t"
    "v_mfma_f32_16x16x32_bf16 a[80:83], v[176:179], v[16:19], a[80:83]\n\t"
    "v_exp_f32_e32 v73, v73\n\t"
    "v_mfma_f32_16x16x32_bf16 a[88:91], v[176:179], v[24:27], a[88:91]\n\t"
    "ds_read_b128 a[224:227], v228 offset:24576\n\t"
    "ds_read_b128 a[228:231], v229 offset:24576\n\t"
    "v_mfma_f32_16x16x32_bf16 a[68:71], v[180:183], v[0:3], a[68:71]\n\t"
    "v_exp_f32_e32 v74, v74\n\t"
    "v_mfma_f32_16x16x32_bf16 a[76:79], v[180:183], v[8:11], a[76:79]\n\t"
    "v_cvt_pk_bf16_f32 v72, v72, v73\n\t"
    "v_mfma_f32_16x16x32_bf16 a[84:87], v[180:183], v[16:19], a[84:87]\n\t"
    "v_exp_f32_e32 v75, v75\n\t"
    "v_mfma_f32_16x16x32_bf16 a[92:95], v[180:183], v[24:27], a[92:95]\n\t"
    "ds_read_b128 a[232:235], v230 offset:24576\n\t"
    "ds_read_b128 a[236:239], v231 offset:24576\n\t"
    "v_mfma_f32_16x16x32_bf16 a[96:99], v[184:187], v[0:3], a[96:99]\n\t"
    "v_exp_f32_e32 v76, v76\n\t"
    "v_mfma_f32_16x16x32_bf16 a[104:107], v[184:187], v[8:11], a[104:107]\n\t"
    "v_cvt_pk_bf16_f32 v73, v74, v75\n\t"
    "v_mfma_f32_16x16x32_bf16 a[112:115], v[184:187], v[16:19], a[112:115]\n\t"
    "v_exp_f32_e32 v77, v77\n\t"
    "v_mfma_f32_16x16x32_bf16 a[120:123], v[184:187], v[24:27], a[120:123]\n\t"
    "ds_read_b128 a[240:243], v228 offset:28672\n\t"
    "ds_read_b128 a[244:247], v229 offset:28672\n\t"
    "v_mfma_f32_16x16x32_bf16 a[100:103], v[188:191], v[0:3], a[100:103]\n\t"
    "v_exp_f32_e32 v78, v78\n\t"
    "v_mfma_f32_16x16x32_bf16 a[108:111], v[188:191], v[8:11], a[108:111]\n\t"
    "v_cvt_pk_bf16_f32 v74, v76, v77\n\t"
    "v_mfma_f32_16x16x32_bf16 a[116:119], v[188:191], v[16:19], a[116:119]\n\t"
    "v_exp_f32_e32 v79, v79\n\t"
    "v_mfma_f32_16x16x32_bf16 a[124:127], v[188:191], v[24:27], a[124:127]\n\t"
    "ds_read_b128 a[248:251], v230 offset:28672\n\t"
    "ds_read_b128 a[252:255], v231 offset:28672\n\t"
    "v_mfma_f32_16x16x32_bf16 v[144:147], v[224:227], v[0:3], v[144:147]\n\t"
    "v_exp_f32_e32 v80, v80\n\t"
    "v_mfma_f32_16x16x32_bf16 v[148:151], v[224:227], v[8:11], v[148:151]\n\t"
    "v_cvt_pk_bf16_f32 v75, v78, v79\n\t"
    "v_mfma_f32_16x16x32_bf16 v[152:155], v[224:227], v[16:19], v[152:155]\n\t"
    "v_exp_f32_e32 v81, v81\n\t"
    "v_mfma_f32_16x16x32_bf16 v[156:159], v[224:227], v[24:27], v[156:159]\n\t"
    "s_cmp_lt_u32 s84, 0x80000\n\t"
    "s_cselect_b32 m0, s90, s97\n\t"
    "v_mfma_f32_16x16x32_bf16 a[0:3], v[192:195], v[32:35], a[0:3]\n\t"
    "v_exp_f32_e32 v82, v82\n\t"
    "v_mfma_f32_16x16x32_bf16 a[8:11], v[192:195], v[40:43], a[8:11]\n\t"
    "v_cvt_pk_bf16_f32 v80, v80, v81\n\t"
    "v_mfma_f32_16x16x32_bf16 a[16:19], v[192:195], v[48:51], a[16:19]\n\t"
    "v_exp_f32_e32 v83, v83\n\t"
    "v_mfma_f32_16x16x32_bf16 a[24:27], v[192:195], v[56:59], a[24:27]\n\t"
    "buffer_load_dwordx4 v233, s[68:71], s84 offen offset:0 lds\n\t"
    "s_add_u32 s85, s85, 0x4000\n\t"
    "v_mfma_f32_16x16x32_bf16 a[4:7], v[196:199], v[32:35], a[4:7]\n\t"
    "v_exp_f32_e32 v84, v84\n\t"
    "v_mfma_f32_16x16x32_bf16 a[12:15], v[196:199], v[40:43], a[12:15]\n\t"
    "v_cvt_pk_bf16_f32 v81, v82, v83\n\t"
    "v_mfma_f32_16x16x32_bf16 a[20:23], v[196:199], v[48:51], a[20:23]\n\t"
    "v_exp_f32_e32 v85, v85\n\t"
    "v_mfma_f32_16x16x32_bf16 a[28:31], v[196:199], v[56:59], a[28:31]\n\t"
    "buffer_load_dwordx4 v234, s[68:71], s84 offen offset:1024 lds\n\t"
    "v_mfma_f32_16x16x32_bf16 a[32:35], v[200:203], v[32:35], a[32:35]\n\t"
    "v_exp_f32_e32 v86, v86\n\t"
    "v_mfma_f32_16x16x32_bf16 a[40:43], v[200:203], v[40:43], a[40:43]\n\t"
    "v_cvt_pk_bf16_f32 v82, v84, v85\n\t"
    "v_mfma_f32_16x16x32_bf16 a[48:51], v[200:203], v[48:51], a[48:51]\n\t"
    "v_exp_f32_e32 v87, v87\n\t"
    "v_mfma_f32_16x16x32_bf16 a[56:59], v[200:203], v[56:59], a[56:59]\n\t"
    "buffer_load_dwordx4 v235, s[68:71], s84 offen offset:2048 lds\n\t"
    "v_mfma_f32_16x16x32_bf16 a[36:39], v[204:207], v[32:35], a[36:39]\n\t"
    "v_exp_f32_e32 v88, v88\n\t"
    "v_mfma_f32_16x16x32_bf16 a[44:47], v[204:207], v[40:43], a[44:47]\n\t"
    "v_cvt_pk_bf16_f32 v83, v86, v87\n\t"
    "v_mfma_f32_16x16x32_bf16 a[52:55], v[204:207], v[48:51], a[52:55]\n\t"
    "v_exp_f32_e32 v89, v89\n\t"
    "v_mfma_f32_16x16x32_bf16 a[60:63], v[204:207], v[56:59], a[60:63]\n\t"
    "buffer_load_dwordx4 v236, s[68:71], s84 offen offset:3072 lds\n\t"
    "v_mfma_f32_16x16x32_bf16 a[64:67], v[208:211], v[32:35], a[64:67]\n\t"
    "v_exp_f32_e32 v90, v90\n\t"
    "v_mfma_f32_16x16x32_bf16 a[72:75], v[208:211], v[40:43], a[72:75]\n\t"
    "v_cvt_pk_bf16_f32 v88, v88, v89\n\t"
    "v_mfma_f32_16x16x32_bf16 a[80:83], v[208:211], v[48:51], a[80:83]\n\t"
    "v_exp_f32_e32 v91, v91\n\t"
    "v_mfma_f32_16x16x32_bf16 a[88:91], v[208:211], v[56:59], a[88:91]\n\t"
    "s_waitcnt lgkmcnt(0)\n\t"
    "ds_read_b64_tr_b16 v[160:161], v232 offset:32768\n\t"
    "v_mfma_f32_16x16x32_bf16 a[68:71], v[212:215], v[32:35], a[68:71]\n\t"
    "v_exp_f32_e32 v92, v92\n\t"
    "v_mfma_f32_16x16x32_bf16 a[76:79], v[212:215], v[40:43], a[76:79]\n\t"
    "v_cvt_pk_bf16_f32 v89, v90, v91\n\t"
    "v_mfma_f32_16x16x32_bf16 a[84:87], v[212:215], v[48:51], a[84:87]\n\t"
    "v_exp_f32_e32 v93, v93\n\t"
    "v_mfma_f32_16x16x32_bf16 a[92:95], v[212:215], v[56:59], a[92:95]\n\t"
    "ds_read_b64_tr_b16 v[162:163], v232 offset:36864\n\t"
    "ds_read_b64_tr_b16 v[164:165], v232 offset:33024\n\t"
    "v_mfma_f32_16x16x32_bf16 a[96:99], v[216:219], v[32:35], a[96:99]\n\t"
    "v_exp_f32_e32 v94, v94\n\t"
    "v_mfma_f32_16x16x32_bf16 a[104:107], v[216:219], v[40:43], a[104:107]\n\t"
    "v_cvt_pk_bf16_f32 v90, v92, v93\n\t"
    "v_mfma_f32_16x16x32_bf16 a[112:115], v[216:219], v[48:51], a[112:115]\n\t"
    "v_exp_f32_e32 v95, v95\n\t"
    "v_mfma_f32_16x16x32_bf16 a[120:123], v[216:219], v[56:59], a[120:123]\n\t"
    "ds_read_b64_tr_b16 v[166:167], v232 offset:37120\n\t"
    "ds_read_b64_tr_b16 v[168:169], v232 offset:33280\n\t"
    "v_mfma_f32_16x16x32_bf16 a[100:103], v[220:223], v[32:35], a[100:103]\n\t"
    "ds_read_b64_tr_b16 v[170:171], v232 offset:37376\n\t"
    "ds_read_b64_tr_b16 v[172:173], v232 offset:33536\n\t"
    "v_mfma_f32_16x16x32_bf16 a[108:111], v[220:223], v[40:43], a[108:111]\n\t"
    "v_cvt_pk_bf16_f32 v91, v94, v95\n\t"
    "v_mfma_f32_16x16x32_bf16 a[116:119], v[220:223], v[48:51], a[116:119]\n\t"
    "ds_read_b64_tr_b16 v[174:175], v232 offset:37632\n\t"
    "ds_read_b64_tr_b16 v[176:177], v232 offset:33792\n\t"
    "v_mfma_f32_16x16x32_bf16 a[124:127], v[220:223], v[56:59], a[124:127]\n\t"
    "ds_read_b64_tr_b16 v[178:179], v232 offset:37888\n\t"
    "s_add_u32 s84, s84, 0x4000\n\t"
    "v_mfma_f32_16x16x32_bf16 v[144:147], v[224:227], v[32:35], v[144:147]\n\t"
    "v_max3_u32 v252, v240, v242, v244\n\t"
    "v_min3_u32 v253, v241, v243, v245\n\t"
    "v_mfma_f32_16x16x32_bf16 v[148:151], v[224:227], v[40:43], v[148:151]\n\t"
    "v_max3_u32 v254, v241, v243, v245\n\t"
    "v_max_u32_e32 v252, v252, v246\n\t"
    "v_mfma_f32_16x16x32_bf16 v[152:155], v[224:227], v[48:51], v[152:155]\n\t"
    "v_min_u32_e32 v253, v253, v247\n\t"
    "v_max_u32_e32 v254, v254, v247\n\t"
    "v_mfma_f32_16x16x32_bf16 v[156:159], v[224:227], v[56:59], v[156:159]\n\t"
    "ds_read_b128 v[240:243], v248 offset:12288\n\t"
    "ds_read_b128 v[244:247], v248 offset:16384\n\t"
    "v_mfma_f32_16x16x32_bf16 v[0:3], a[192:195], a[128:131], v[128:131]\n\t"
    "v_exp_f32_e32 v96, v96\n\t"
    "v_mfma_f32_16x16x32_bf16 v[8:11], a[192:195], a[144:147], v[132:135]\n\t"
    "s_cmp_lt_u32 s85, 0x80000\n\t"
    "s_cselect_b32 m0, s95, s97\n\t"
    "v_mfma_f32_16x16x32_bf16 v[16:19], a[192:195], a[160:163], v[136:139]\n\t"
    "v_exp_f32_e32 v97, v97\n\t"
    "v_mfma_f32_16x16x32_bf16 v[24:27], a[192:195], a[176:179], v[140:143]\n\t"
    "ds_read_b64_tr_b16 v[180:181], v232 offset:34048\n\t"
    "ds_read_b64_tr_b16 v[182:183], v232 offset:38144\n\t"
    "v_mfma_f32_16x16x32_bf16 v[0:3], a[196:199], a[132:135], v[0:3]\n\t"
    "v_exp_f32_e32 v98, v98\n\t"
    "v_mfma_f32_16x16x32_bf16 v[8:11], a[196:199], a[148:151], v[8:11]\n\t"
    "v_cvt_pk_bf16_f32 v96, v96, v97\n\t"
    "v_mfma_f32_16x16x32_bf16 v[16:19], a[196:199], a[164:167], v[16:19]\n\t"
    "v_exp_f32_e32 v99, v99\n\t"
    "v_mfma_f32_16x16x32_bf16 v[24:27], a[196:199], a[180:183], v[24:27]\n\t"
    "ds_read_b64_tr_b16 v[184:185], v232 offset:34304\n\t"
    "ds_read_b64_tr_b16 v[186:187], v232 offset:38400\n\t"
    "v_mfma_f32_16x16x32_bf16 v[0:3], a[200:203], a[136:139], v[0:3]\n\t"
    "v_exp_f32_e32 v100, v100\n\t"
    "v_mfma_f32_16x16x32_bf16 v[8:11], a[200:203], a[152:155], v[8:11]\n\t"
    "v_cvt_pk_bf16_f32 v97, v98, v99\n\t"
    "v_mfma_f32_16x16x32_bf16 v[16:19], a[200:203], a[168:171], v[16:19]\n\t"
    "v_exp_f32_e32 v101, v101\n\t"
    "v_mfma_f32_16x16x32_bf16 v[24:27], a[200:203], a[184:187], v[24:27]\n\t"
    "ds_read_b64_tr_b16 v[188:189], v232 offset:34560\n\t"
    "ds_read_b64_tr_b16 v[190:191], v232 offset:38656\n\t"
    "v_mfma_f32_16x16x32_bf16 v[0:3], a[204:207], a[140:143], v[0:3]\n\t"
    "v_exp_f32_e32 v102, v102\n\t"
    "v_mfma_f32_16x16x32_bf16 v[8:11], a[204:207], a[156:159], v[8:11]\n\t"
    "v_cvt_pk_bf16_f32 v98, v100, v101\n\t"
    "v_mfma_f32_16x16x32_bf16 v[16:19], a[204:207], a[172:175], v[16:19]\n\t"
    "v_exp_f32_e32 v103, v103\n\t"
    "v_mfma_f32_16x16x32_bf16 v[24:27], a[204:207], a[188:191], v[24:27]\n\t"
    "ds_read_b64_tr_b16 v[192:193], v232 offset:40960\n\t"
    "ds_read_b64_tr_b16 v[194:195], v232 offset:45056\n\t"
    "v_mfma_f32_16x16x32_bf16 v[4:7], a[208:211], a[128:131], v[128:131]\n\t"
    "v_exp_f32_e32 v104, v104\n\t"
    "v_mfma_f32_16x16x32_bf16 v[12:15], a[208:211], a[144:147], v[132:135]\n\t"
    "v_cvt_pk_bf16_f32 v99, v102, v103\n\t"
    "v_mfma_f32_16x16x32_bf16 v[20:23], a[208:211], a[160:163], v[136:139]\n\t"
    "v_exp_f32_e32 v105, v105\n\t"
    "v_mfma_f32_16x16x32_bf16 v[28:31], a[208:211], a[176:179], v[140:143]\n\t"
    "ds_read_b64_tr_b16 v[196:197], v232 offset:41216\n\t"
    "ds_read_b64_tr_b16 v[198:199], v232 offset:45312\n\t"
    "v_mfma_f32_16x16x32_bf16 v[4:7], a[212:215], a[132:135], v[4:7]\n\t"
    "v_exp_f32_e32 v106, v106\n\t"
    "v_mfma_f32_16x16x32_bf16 v[12:15], a[212:215], a[148:151], v[12:15]\n\t"
    "v_cvt_pk_bf16_f32 v104, v104, v105\n\t"
    "v_mfma_f32_16x16x32_bf16 v[20:23], a[212:215], a[164:167], v[20:23]\n\t"
    "v_exp_f32_e32 v107, v107\n\t"
    "v_mfma_f32_16x16x32_bf16 v[28:31], a[212:215], a[180:183], v[28:31]\n\t"
    "ds_read_b64_tr_b16 v[200:201], v232 offset:41472\n\t"
    "ds_read_b64_tr_b16 v[202:203], v232 offset:45568\n\t"
    "v_mfma_f32_16x16x32_bf16 v[4:7], a[216:219], a[136:139], v[4:7]\n\t"
    "v_exp_f32_e32 v108, v108\n\t"
    "v_mfma_f32_16x16x32_bf16 v[12:15], a[216:219], a[152:155], v[12:15]\n\t"
    "v_cvt_pk_bf16_f32 v105, v106, v107\n\t"
    "v_mfma_f32_16x16x32_bf16 v[20:23], a[216:219], a[168:171], v[20:23]\n\t"
    "v_exp_f32_e32 v109, v109\n\t"
    "v_mfma_f32_16x16x32_bf16 v[28:31], a[216:219], a[184:187], v[28:31]\n\t"
    "ds_read_b64_tr_b16 v[204:205], v232 offset:41728\n\t"
    "ds_read_b64_tr_b16 v[206:207], v232 offset:45824\n\t"
    "v_mfma_f32_16x16x32_bf16 v[4:7], a[220:223], a[140:143], v[4:7]\n\t"
    "v_exp_f32_e32 v110, v110\n\t"
    "v_mfma_f32_16x16x32_bf16 v[12:15], a[220:223], a[156:159], v[12:15]\n\t"
    "v_cvt_pk_bf16_f32 v106, v108, v109\n\t"
    "v_mfma_f32_16x16x32_bf16 v[20:23], a[220:223], a[172:175], v[20:23]\n\t"
    "v_exp_f32_e32 v111, v111\n\t"
    "v_mfma_f32_16x16x32_bf16 v[28:31], a[220:223], a[188:191], v[28:31]\n\t"
    "ds_read_b64_tr_b16 v[208:209], v232 offset:41984\n\t"
    "ds_read_b64_tr_b16 v[210:211], v232 offset:46080\n\t"
    "v_mfma_f32_16x16x32_bf16 v[32:35], a[224:227], a[128:131], v[128:131]\n\t"
    "v_exp_f32_e32 v112, v112\n\t"
    "v_mfma_f32_16x16x32_bf16 v[40:43], a[224:227], a[144:147], v[132:135]\n\t"
    "v_cvt_pk_bf16_f32 v107, v110, v111\n\t"
    "v_mfma_f32_16x16x32_bf16 v[48:51], a[224:227], a[160:163], v[136:139]\n\t"
    "v_exp_f32_e32 v113, v113\n\t"
    "v_mfma_f32_16x16x32_bf16 v[56:59], a[224:227], a[176:179], v[140:143]\n\t"
    "ds_read_b64_tr_b16 v[212:213], v232 offset:42240\n\t"
    "ds_read_b64_tr_b16 v[214:215], v232 offset:46336\n\t"
    "v_mfma_f32_16x16x32_bf16 v[32:35], a[228:231], a[132:135], v[32:35]\n\t"
    "v_exp_f32_e32 v114, v114\n\t"
    "v_mfma_f32_16x16x32_bf16 v[40:43], a[228:231], a[148:151], v[40:43]\n\t"
    "v_cvt_pk_bf16_f32 v112, v112, v113\n\t"
    "v_mfma_f32_16x16x32_bf16 v[48:51], a[228:231], a[164:167], v[48:51]\n\t"
    "v_exp_f32_e32 v115, v115\n\t"
    "v_mfma_f32_16x16x32_bf16 v[56:59], a[228:231], a[180:183], v[56:59]\n\t"
    "ds_read_b64_tr_b16 v[216:217], v232 offset:42496\n\t"
    "ds_read_b64_tr_b16 v[218:219], v232 offset:46592\n\t"
    "v_mfma_f32_16x16x32_bf16 v[32:35], a[232:235], a[136:139], v[32:35]\n\t"
    "v_exp_f32_e32 v116, v116\n\t"
    "v_mfma_f32_16x16x32_bf16 v[40:43], a[232:235], a[152:155], v[40:43]\n\t"
    "v_cvt_pk_bf16_f32 v113, v114, v115\n\t"
    "v_mfma_f32_16x16x32_bf16 v[48:51], a[232:235], a[168:171], v[48:51]\n\t"
    "v_exp_f32_e32 v117, v117\n\t"
    "v_mfma_f32_16x16x32_bf16 v[56:59], a[232:235], a[184:187], v[56:59]\n\t"
    "ds_read_b64_tr_b16 v[220:221], v232 offset:42752\n\t"
    "ds_read_b64_tr_b16 v[222:223], v232 offset:46848\n\t"
    "v_mfma_f32_16x16x32_bf16 v[32:35], a[236:239], a[140:143], v[32:35]\n\t"
    "v_exp_f32_e32 v118, v118\n\t"
    "v_mfma_f32_16x16x32_bf16 v[40:43], a[236:239], a[156:159], v[40:43]\n\t"
    "v_cvt_pk_bf16_f32 v114, v116, v117\n\t"
    "v_mfma_f32_16x16x32_bf16 v[48:51], a[236:239], a[172:175], v[48:51]\n\t"
    "v_exp_f32_e32 v119, v119\n\t"
    "v_mfma_f32_16x16x32_bf16 v[56:59], a[236:239], a[188:191], v[56:59]\n\t"
    "buffer_load_dwordx4 v237, s[72:75], s85 offen offset:0 lds\n\t"
    "v_mfma_f32_16x16x32_bf16 v[36:39], a[240:243], a[128:131], v[128:131]\n\t"
    "v_exp_f32_e32 v120, v120\n\t"
    "v_mfma_f32_16x16x32_bf16 v[44:47], a[240:243], a[144:147], v[132:135]\n\t"
    "v_cvt_pk_bf16_f32 v115, v118, v119\n\t"
    "v_mfma_f32_16x16x32_bf16 v[52:55], a[240:243], a[160:163], v[136:139]\n\t"
    "v_exp_f32_e32 v121, v121\n\t"
    "v_mfma_f32_16x16x32_bf16 v[60:63], a[240:243], a[176:179], v[140:143]\n\t"
    "buffer_load_dwordx4 v238, s[72:75], s85 offen offset:1024 lds\n\t"
    "v_mfma_f32_16x16x32_bf16 v[36:39], a[244:247], a[132:135], v[36:39]\n\t"
    "v_exp_f32_e32 v122, v122\n\t"
    "v_mfma_f32_16x16x32_bf16 v[44:47], a[244:247], a[148:151], v[44:47]\n\t"
    "v_cvt_pk_bf16_f32 v120, v120, v121\n\t"
    "v_mfma_f32_16x16x32_bf16 v[52:55], a[244:247], a[164:167], v[52:55]\n\t"
    "v_exp_f32_e32 v123, v123\n\t"
    "v_mfma_f32_16x16x32_bf16 v[60:63], a[244:247], a[180:183], v[60:63]\n\t"
    "buffer_load_dwordx4 v237, s[72:75], s85 offen offset:2048 lds\n\t"
    "v_mfma_f32_16x16x32_bf16 v[36:39], a[248:251], a[136:139], v[36:39]\n\t"
    "v_exp_f32_e32 v124, v124\n\t"
    "v_mfma_f32_16x16x32_bf16 v[44:47], a[248:251], a[152:155], v[44:47]\n\t"
    "v_cvt_pk_bf16_f32 v121, v122, v123\n\t"
    "v_mfma_f32_16x16x32_bf16 v[52:55], a[248:251], a[168:171], v[52:55]\n\t"
    "v_exp_f32_e32 v125, v125\n\t"
    "v_mfma_f32_16x16x32_bf16 v[60:63], a[248:251], a[184:187], v[60:63]\n\t"
    "buffer_load_dwordx4 v238, s[72:75], s85 offen offset:3072 lds\n\t"
    "v_mfma_f32_16x16x32_bf16 v[36:39], a[252:255], a[140:143], v[36:39]\n\t"
    "v_exp_f32_e32 v126, v126\n\t"
    "v_mfma_f32_16x16x32_bf16 v[44:47], a[252:255], a[156:159], v[44:47]\n\t"
    "v_cvt_pk_bf16_f32 v122, v124, v125\n\t"
    "v_mfma_f32_16x16x32_bf16 v[52:55], a[252:255], a[172:175], v[52:55]\n\t"
    "v_exp_f32_e32 v127, v127\n\t"
    "v_mfma_f32_16x16x32_bf16 v[60:63], a[252:255], a[188:191], v[60:63]\n\t"
    "s_waitcnt vmcnt(8) lgkmcnt(0)\n\t"
    "s_barrier\n\t"
    "v_mfma_f32_16x16x32_bf16 a[0:3], v[160:163], v[64:67], a[0:3]\n\t"
    "v_exp_f32_e32 v0, v0\n\t"
    "v_mfma_f32_16x16x32_bf16 a[8:11], v[160:163], v[72:75], a[8:11]\n\t"
    "v_cvt_pk_bf16_f32 v123, v126, v127\n\t"
    "v_mfma_f32_16x16x32_bf16 a[16:19], v[160:163], v[80:83], a[16:19]\n\t"
    "v_exp_f32_e32 v1, v1\n\t"
    "v_mfma_f32_16x16x32_bf16 a[24:27], v[160:163], v[88:91], a[24:27]\n\t"
    "ds_read_b128 a[192:195], v228 offset:32768\n\t"
    "ds_read_b128 a[196:199], v229 offset:32768\n\t"
    "v_mfma_f32_16x16x32_bf16 a[4:7], v[164:167], v[64:67], a[4:7]\n\t"
    "v_exp_f32_e32 v2, v2\n\t"
    "v_mfma_f32_16x16x32_bf16 a[12:15], v[164:167], v[72:75], a[12:15]\n\t"
    "v_cvt_pk_bf16_f32 v0, v0, v1\n\t"
    "v_mfma_f32_16x16x32_bf16 a[20:23], v[164:167], v[80:83], a[20:23]\n\t"
    "v_exp_f32_e32 v3, v3\n\t"
    "v_mfma_f32_16x16x32_bf16 a[28:31], v[164:167], v[88:91], a[28:31]\n\t"
    "ds_read_b128 a[200:203], v230 offset:32768\n\t"
    "ds_read_b128 a[204:207], v231 offset:32768\n\t"
    "v_mfma_f32_16x16x32_bf16 a[32:35], v[168:171], v[64:67], a[32:35]\n\t"
    "v_exp_f32_e32 v4, v4\n\t"
    "v_mfma_f32_16x16x32_bf16 a[40:43], v[168:171], v[72:75], a[40:43]\n\t"
    "v_cvt_pk_bf16_f32 v1, v2, v3\n\t"
    "v_mfma_f32_16x16x32_bf16 a[48:51], v[168:171], v[80:83], a[48:51]\n\t"
    "v_exp_f32_e32 v5, v5\n\t"
    "v_mfma_f32_16x16x32_bf16 a[56:59], v[168:171], v[88:91], a[56:59]\n\t"
    "ds_read_b128 a[208:211], v228 offset:36864\n\t"
    "ds_read_b128 a[212:215], v229 offset:36864\n\t"
    "v_mfma_f32_16x16x32_bf16 a[36:39], v[172:175], v[64:67], a[36:39]\n\t"
    "v_exp_f32_e32 v6, v6\n\t"
    "v_mfma_f32_16x16x32_bf16 a[44:47], v[172:175], v[72:75], a[44:47]\n\t"
    "v_cvt_pk_bf16_f32 v2, v4, v5\n\t"
    "v_mfma_f32_16x16x32_bf16 a[52:55], v[172:175], v[80:83], a[52:55]\n\t"
    "v_exp_f32_e32 v7, v7\n\t"
    "v_mfma_f32_16x16x32_bf16 a[60:63], v[172:175], v[88:91], a[60:63]\n\t"
    "ds_read_b128 a[216:219], v230 offset:36864\n\t"
    "ds_read_b128 a[220:223], v231 offset:36864\n\t"
    "v_mfma_f32_16x16x32_bf16 a[64:67], v[176:179], v[64:67], a[64:67]\n\t"
    "v_exp_f32_e32 v8, v8\n\t"
    "v_mfma_f32_16x16x32_bf16 a[72:75], v[176:179], v[72:75], a[72:75]\n\t"
    "v_cvt_pk_bf16_f32 v3, v6, v7\n\t"
    "v_mfma_f32_16x16x32_bf16 a[80:83], v[176:179], v[80:83], a[80:83]\n\t"
    "v_exp_f32_e32 v9, v9\n\t"
    "v_mfma_f32_16x16x32_bf16 a[88:91], v[176:179], v[88:91], a[88:91]\n\t"
    "ds_read_b128 a[224:227], v228 offset:40960\n\t"
    "ds_read_b128 a[228:231], v229 offset:40960\n\t"
    "v_mfma_f32_16x16x32_bf16 a[68:71], v[180:183], v[64:67], a[68:71]\n\t"
    "v_exp_f32_e32 v10, v10\n\t"
    "v_mfma_f32_16x16x32_bf16 a[76:79], v[180:183], v[72:75], a[76:79]\n\t"
    "v_cvt_pk_bf16_f32 v8, v8, v9\n\t"
    "v_mfma_f32_16x16x32_bf16 a[84:87], v[180:183], v[80:83], a[84:87]\n\t"
    "v_exp_f32_e32 v11, v11\n\t"
    "v_mfma_f32_16x16x32_bf16 a[92:95], v[180:183], v[88:91], a[92:95]\n\t"
    "ds_read_b128 a[232:235], v230 offset:40960\n\t"
    "ds_read_b128 a[236:239], v231 offset:40960\n\t"
    "v_mfma_f32_16x16x32_bf16 a[96:99], v[184:187], v[64:67], a[96:99]\n\t"
    "v_exp_f32_e32 v12, v12\n\t"
    "v_mfma_f32_16x16x32_bf16 a[104:107], v[184:187], v[72:75], a[104:107]\n\t"
    "v_cvt_pk_bf16_f32 v9, v10, v11\n\t"
    "v_mfma_f32_16x16x32_bf16 a[112:115], v[184:187], v[80:83], a[112:115]\n\t"
    "v_exp_f32_e32 v13, v13\n\t"
    "v_mfma_f32_16x16x32_bf16 a[120:123], v[184:187], v[88:91], a[120:123]\n\t"
    "ds_read_b128 a[240:243], v228 offset:45056\n\t"
    "ds_read_b128 a[244:247], v229 offset:45056\n\t"
    "v_mfma_f32_16x16x32_bf16 a[100:103], v[188:191], v[64:67], a[100:103]\n\t"
    "v_exp_f32_e32 v14, v14\n\t"
    "v_mfma_f32_16x16x32_bf16 a[108:111], v[188:191], v[72:75], a[108:111]\n\t"
    "v_cvt_pk_bf16_f32 v10, v12, v13\n\t"
    "v_mfma_f32_16x16x32_bf16 a[116:119], v[188:191], v[80:83], a[116:119]\n\t"
    "v_exp_f32_e32 v15, v15\n\t"
    "v_mfma_f32_16x16x32_bf16 a[124:127], v[188:191], v[88:91], a[124:127]\n\t"
    "ds_read_b128 a[248:251], v230 offset:45056\n\t"
    "ds_read_b128 a[252:255], v231 offset:45056\n\t"
    "v_mfma_f32_16x16x32_bf16 v[144:147], v[224:227], v[64:67], v[144:147]\n\t"
    "v_exp_f32_e32 v16, v16\n\t"
    "v_mfma_f32_16x16x32_bf16 v[148:151], v[224:227], v[72:75], v[148:151]\n\t"
    "v_cvt_pk_bf16_f32 v11, v14, v15\n\t"
    "v_mfma_f32_16x16x32_bf16 v[152:155], v[224:227], v[80:83], v[152:155]\n\t"
    "v_exp_f32_e32 v17, v17\n\t"
    "v_mfma_f32_16x16x32_bf16 v[156:159], v[224:227], v[88:91], v[156:159]\n\t"
    "s_cmp_lt_u32 s84, 0x80000\n\t"
    "s_cselect_b32 m0, s91, s97\n\t"
    "v_mfma_f32_16x16x32_bf16 a[0:3], v[192:195], v[96:99], a[0:3]\n\t"
    "v_exp_f32_e32 v18, v18\n\t"
    "v_mfma_f32_16x16x32_bf16 a[8:11], v[192:195], v[104:107], a[8:11]\n\t"
    "v_cvt_pk_bf16_f32 v16, v16, v17\n\t"
    "v_mfma_f32_16x16x32_bf16 a[16:19], v[192:195], v[112:115], a[16:19]\n\t"
    "v_exp_f32_e32 v19, v19\n\t"
    "v_mfma_f32_16x16x32_bf16 a[24:27], v[192:195], v[120:123], a[24:27]\n\t"
    "buffer_load_dwordx4 v233, s[68:71], s84 offen offset:0 lds\n\t"
    "s_add_u32 s85, s85, 0x4000\n\t"
    "v_mfma_f32_16x16x32_bf16 a[4:7], v[196:199], v[96:99], a[4:7]\n\t"
    "v_exp_f32_e32 v20, v20\n\t"
    "v_mfma_f32_16x16x32_bf16 a[12:15], v[196:199], v[104:107], a[12:15]\n\t"
    "v_cvt_pk_bf16_f32 v17, v18, v19\n\t"
    "v_mfma_f32_16x16x32_bf16 a[20:23], v[196:199], v[112:115], a[20:23]\n\t"
    "v_exp_f32_e32 v21, v21\n\t"
    "v_mfma_f32_16x16x32_bf16 a[28:31], v[196:199], v[120:123], a[28:31]\n\t"
    "buffer_load_dwordx4 v234, s[68:71], s84 offen offset:1024 lds\n\t"
    "v_mfma_f32_16x16x32_bf16 a[32:35], v[200:203], v[96:99], a[32:35]\n\t"
    "v_exp_f32_e32 v22, v22\n\t"
    "v_mfma_f32_16x16x32_bf16 a[40:43], v[200:203], v[104:107], a[40:43]\n\t"
    "v_cvt_pk_bf16_f32 v18, v20, v21\n\t"
    "v_mfma_f32_16x16x32_bf16 a[48:51], v[200:203], v[112:115], a[48:51]\n\t"
    "v_exp_f32_e32 v23, v23\n\t"
    "v_mfma_f32_16x16x32_bf16 a[56:59], v[200:203], v[120:123], a[56:59]\n\t"
    "buffer_load_dwordx4 v235, s[68:71], s84 offen offset:2048 lds\n\t"
    "v_mfma_f32_16x16x32_bf16 a[36:39], v[204:207], v[96:99], a[36:39]\n\t"
    "v_exp_f32_e32 v24, v24\n\t"
    "v_mfma_f32_16x16x32_bf16 a[44:47], v[204:207], v[104:107], a[44:47]\n\t"
    "v_cvt_pk_bf16_f32 v19, v22, v23\n\t"
    "v_mfma_f32_16x16x32_bf16 a[52:55], v[204:207], v[112:115], a[52:55]\n\t"
    "v_exp_f32_e32 v25, v25\n\t"
    "v_mfma_f32_16x16x32_bf16 a[60:63], v[204:207], v[120:123], a[60:63]\n\t"
    "buffer_load_dwordx4 v236, s[68:71], s84 offen offset:3072 lds\n\t"
    "v_mfma_f32_16x16x32_bf16 a[64:67], v[208:211], v[96:99], a[64:67]\n\t"
    "v_exp_f32_e32 v26, v26\n\t"
    "v_mfma_f32_16x16x32_bf16 a[72:75], v[208:211], v[104:107], a[72:75]\n\t"
    "v_cvt_pk_bf16_f32 v24, v24, v25\n\t"
    "v_mfma_f32_16x16x32_bf16 a[80:83], v[208:211], v[112:115], a[80:83]\n\t"
    "v_exp_f32_e32 v27, v27\n\t"
    "v_mfma_f32_16x16x32_bf16 a[88:91], v[208:211], v[120:123], a[88:91]\n\t"
    "s_waitcnt lgkmcnt(0)\n\t"
    "ds_read_b64_tr_b16 v[160:161], v232 offset:16384\n\t"
    "v_mfma_f32_16x16x32_bf16 a[68:71], v[212:215], v[96:99], a[68:71]\n\t"
    "v_exp_f32_e32 v28, v28\n\t"
    "v_mfma_f32_16x16x32_bf16 a[76:79], v[212:215], v[104:107], a[76:79]\n\t"
    "v_cvt_pk_bf16_f32 v25, v26, v27\n\t"
    "v_mfma_f32_16x16x32_bf16 a[84:87], v[212:215], v[112:115], a[84:87]\n\t"
    "v_exp_f32_e32 v29, v29\n\t"
    "v_mfma_f32_16x16x32_bf16 a[92:95], v[212:215], v[120:123], a[92:95]\n\t"
    "ds_read_b64_tr_b16 v[162:163], v232 offset:20480\n\t"
    "ds_read_b64_tr_b16 v[164:165], v232 offset:16640\n\t"
    "v_mfma_f32_16x16x32_bf16 a[96:99], v[216:219], v[96:99], a[96:99]\n\t"
    "v_exp_f32_e32 v30, v30\n\t"
    "v_mfma_f32_16x16x32_bf16 a[104:107], v[216:219], v[104:107], a[104:107]\n\t"
    "v_cvt_pk_bf16_f32 v26, v28, v29\n\t"
    "v_mfma_f32_16x16x32_bf16 a[112:115], v[216:219], v[112:115], a[112:115]\n\t"
    "v_exp_f32_e32 v31, v31\n\t"
    "v_mfma_f32_16x16x32_bf16 a[120:123], v[216:219], v[120:123], a[120:123]\n\t"
    "ds_read_b64_tr_b16 v[166:167], v232 offset:20736\n\t"
    "ds_read_b64_tr_b16 v[168:169], v232 offset:16896\n\t"
    "v_mfma_f32_16x16x32_bf16 a[100:103], v[220:223], v[96:99], a[100:103]\n\t"
    "ds_read_b64_tr_b16 v[170:171], v232 offset:20992\n\t"
    "ds_read_b64_tr_b16 v[172:173], v232 offset:17152\n\t"
    "v_mfma_f32_16x16x32_bf16 a[108:111], v[220:223], v[104:107], a[108:111]\n\t"
    "v_cvt_pk_bf16_f32 v27, v30, v31\n\t"
    "v_mfma_f32_16x16x32_bf16 a[116:119], v[220:223], v[112:115], a[116:119]\n\t"
    "ds_read_b64_tr_b16 v[174:175], v232 offset:21248\n\t"
    "ds_read_b64_tr_b16 v[176:177], v232 offset:17408\n\t"
    "v_mfma_f32_16x16x32_bf16 a[124:127], v[220:223], v[120:123], a[124:127]\n\t"
    "ds_read_b64_tr_b16 v[178:179], v232 offset:21504\n\t"
    "s_add_u32 s84, s84, 0x4000\n\t"
    "v_mfma_f32_16x16x32_bf16 v[144:147], v[224:227], v[96:99], v[144:147]\n\t"
    "v_max3_u32 v252, v252, v240, v242\n\t"
    "v_min3_u32 v253, v253, v241, v243\n\t"
    "v_mfma_f32_16x16x32_bf16 v[148:151], v[224:227], v[104:107], v[148:151]\n\t"
    "v_max3_u32 v254, v254, v241, v243\n\t"
    "v_max3_u32 v252, v252, v244, v246\n\t"
    "v_mfma_f32_16x16x32_bf16 v[152:155], v[224:227], v[112:115], v[152:155]\n\t"
    "v_min3_u32 v253, v253, v245, v247\n\t"
    "v_max3_u32 v254, v254, v245, v247\n\t"
    "v_mfma_f32_16x16x32_bf16 v[156:159], v[224:227], v[120:123], v[156:159]\n\t"
    "v_cmp_eq_u32_e32 vcc, %[tag], v253\n\t"
    "v_cmp_eq_u32_e64 s[88:89], %[tag], v254\n\t"
    "buffer_load_dwordx4 v[240:243], v250, s[60:63], 0 offen offset:1024 sc1\n\t"
    "v_mfma_f32_16x16x32_bf16 v[64:67], a[192:195], a[128:131], v[128:131]\n\t"
    "v_exp_f32_e32 v32, v32\n\t"
    "v_mfma_f32_16x16x32_bf16 v[72:75], a[192:195], a[144:147], v[132:135]\n\t"
    "s_cmp_lt_u32 s85, 0x80000\n\t"
    "s_cselect_b32 m0, s93, s97\n\t"
    "v_mfma_f32_16x16x32_bf16 v[80:83], a[192:195], a[160:163], v[136:139]\n\t"
    "v_exp_f32_e32 v33, v33\n\t"
    "v_mfma_f32_16x16x32_bf16 v[88:91], a[192:195], a[176:179], v[140:143]\n\t"
    "ds_read_b64_tr_b16 v[180:181], v232 offset:17664\n\t"
    "ds_read_b64_tr_b16 v[182:183], v232 offset:21760\n\t"
    "s_and_b64 vcc, vcc, s[88:89]\n\t"
    "v_mfma_f32_16x16x32_bf16 v[64:67], a[196:199], a[132:135], v[64:67]\n\t"
    "v_exp_f32_e32 v34, v34\n\t"
    "v_mfma_f32_16x16x32_bf16 v[72:75], a[196:199], a[148:151], v[72:75]\n\t"
    "v_cvt_pk_bf16_f32 v32, v32, v33\n\t"
    "v_mfma_f32_16x16x32_bf16 v[80:83], a[196:199], a[164:167], v[80:83]\n\t"
    "v_exp_f32_e32 v35, v35\n\t"
    "v_mfma_f32_16x16x32_bf16 v[88:91], a[196:199], a[180:183], v[88:91]\n\t"
    "ds_read_b64_tr_b16 v[184:185], v232 offset:17920\n\t"
    "ds_read_b64_tr_b16 v[186:187], v232 offset:22016\n\t"
    "v_cndmask_b32_e32 v252, -1, v252, vcc\n\t"
    "v_mfma_f32_16x16x32_bf16 v[64:67], a[200:203], a[136:139], v[64:67]\n\t"
    "v_exp_f32_e32 v36, v36\n\t"
    "v_mfma_f32_16x16x32_bf16 v[72:75], a[200:203], a[152:155], v[72:75]\n\t"
    "v_cvt_pk_bf16_f32 v33, v34, v35\n\t"
    "v_mfma_f32_16x16x32_bf16 v[80:83], a[200:203], a[168:171], v[80:83]\n\t"
    "v_exp_f32_e32 v37, v37\n\t"
    "v_mfma_f32_16x16x32_bf16 v[88:91], a[200:203], a[184:187], v[88:91]\n\t"
    "ds_read_b64_tr_b16 v[188:189], v232 offset:18176\n\t"
    "ds_read_b64_tr_b16 v[190:191], v232 offset:22272\n\t"
    "v_max_u32_dpp v252, v252, v252 row_ror:8 row_mask:0xf bank_mask:0xf\n\t"
    "v_mfma_f32_16x16x32_bf16 v[64:67], a[204:207], a[140:143], v[64:67]\n\t"
    "v_exp_f32_e32 v38, v38\n\t"
    "v_mfma_f32_16x16x32_bf16 v[72:75], a[204:207], a[156:159], v[72:75]\n\t"
    "v_cvt_pk_bf16_f32 v34, v36, v37\n\t"
    "v_mfma_f32_16x16x32_bf16 v[80:83], a[204:207], a[172:175], v[80:83]\n\t"
    "v_exp_f32_e32 v39, v39\n\t"
    "v_mfma_f32_16x16x32_bf16 v[88:91], a[204:207], a[188:191], v[88:91]\n\t"
    "ds_read_b64_tr_b16 v[192:193], v232 offset:24576\n\t"
    "ds_read_b64_tr_b16 v[194:195], v232 offset:28672\n\t"
    "v_max_u32_dpp v252, v252, v252 row_ror:4 row_mask:0xf bank_mask:0xf\n\t"
    "v_mfma_f32_16x16x32_bf16 v[68:71], a[208:211], a[128:131], v[128:131]\n\t"
    "v_exp_f32_e32 v40, v40\n\t"
    "v_mfma_f32_16x16x32_bf16 v[76:79], a[208:211], a[144:147], v[132:135]\n\t"
    "v_cvt_pk_bf16_f32 v35, v38, v39\n\t"
    "v_mfma_f32_16x16x32_bf16 v[84:87], a[208:211], a[160:163], v[136:139]\n\t"
    "v_exp_f32_e32 v41, v41\n\t"
    "v_mfma_f32_16x16x32_bf16 v[92:95], a[208:211], a[176:179], v[140:143]\n\t"
    "ds_read_b64_tr_b16 v[196:197], v232 offset:24832\n\t"
    "ds_read_b64_tr_b16 v[198:199], v232 offset:28928\n\t"
    "v_max_u32_dpp v252, v252, v252 row_ror:2 row_mask:0xf bank_mask:0xf\n\t"
    "v_mfma_f32_16x16x32_bf16 v[68:71], a[212:215], a[132:135], v[68:71]\n\t"
    "v_exp_f32_e32 v42, v42\n\t"
    "v_mfma_f32_16x16x32_bf16 v[76:79], a[212:215], a[148:151], v[76:79]\n\t"
    "v_cvt_pk_bf16_f32 v40, v40, v41\n\t"
    "v_mfma_f32_16x16x32_bf16 v[84:87], a[212:215], a[164:167], v[84:87]\n\t"
    "v_exp_f32_e32 v43, v43\n\t"
    "v_mfma_f32_16x16x32_bf16 v[92:95], a[212:215], a[180:183], v[92:95]\n\t"
    "ds_read_b64_tr_b16 v[200:201], v232 offset:25088\n\t"
    "ds_read_b64_tr_b16 v[202:203], v232 offset:29184\n\t"
    "v_max_u32_dpp v252, v252, v252 row_ror:1 row_mask:0xf bank_mask:0xf\n\t"
    "v_mfma_f32_16x16x32_bf16 v[68:71], a[216:219], a[136:139], v[68:71]\n\t"
    "v_exp_f32_e32 v44, v44\n\t"
    "v_mfma_f32_16x16x32_bf16 v[76:79], a[216:219], a[152:155], v[76:79]\n\t"
    "v_cvt_pk_bf16_f32 v41, v42, v43\n\t"
    "v_mfma_f32_16x16x32_bf16 v[84:87], a[216:219], a[168:171], v[84:87]\n\t"
    "v_exp_f32_e32 v45, v45\n\t"
    "v_mfma_f32_16x16x32_bf16 v[92:95], a[216:219], a[184:187], v[92:95]\n\t"
    "ds_read_b64_tr_b16 v[204:205], v232 offset:25344\n\t"
    "ds_read_b64_tr_b16 v[206:207], v232 offset:29440\n\t"
    "v_mov_b32_e32 v253, v252\n\t"
    "v_mfma_f32_16x16x32_bf16 v[68:71], a[220:223], a[140:143], v[68:71]\n\t"
    "v_exp_f32_e32 v46, v46\n\t"
    "v_mfma_f32_16x16x32_bf16 v[76:79], a[220:223], a[156:159], v[76:79]\n\t"
    "v_cvt_pk_bf16_f32 v42, v44, v45\n\t"
    "v_mfma_f32_16x16x32_bf16 v[84:87], a[220:223], a[172:175], v[84:87]\n\t"
    "v_exp_f32_e32 v47, v47\n\t"
    "v_mfma_f32_16x16x32_bf16 v[92:95], a[220:223], a[188:191], v[92:95]\n\t"
    "ds_read_b64_tr_b16 v[208:209], v232 offset:25600\n\t"
    "ds_read_b64_tr_b16 v[210:211], v232 offset:29696\n\t"
    "v_permlane16_swap_b32_e32 v252, v253\n\t"
    "v_mfma_f32_16x16x32_bf16 v[96:99], a[224:227], a[128:131], v[128:131]\n\t"
    "v_exp_f32_e32 v48, v48\n\t"
    "v_mfma_f32_16x16x32_bf16 v[104:107], a[224:227], a[144:147], v[132:135]\n\t"
    "v_cvt_pk_bf16_f32 v43, v46, v47\n\t"
    "v_mfma_f32_16x16x32_bf16 v[112:115], a[224:227], a[160:163], v[136:139]\n\t"
    "v_exp_f32_e32 v49, v49\n\t"
    "v_mfma_f32_16x16x32_bf16 v[120:123], a[224:227], a[176:179], v[140:143]\n\t"
    "ds_read_b64_tr_b16 v[212:213], v232 offset:25856\n\t"
    "ds_read_b64_tr_b16 v[214:215], v232 offset:29952\n\t"
    "v_max_u32_e32 v252, v252, v253\n\t"
    "v_mfma_f32_16x16x32_bf16 v[96:99], a[228:231], a[132:135], v[96:99]\n\t"
    "v_exp_f32_e32 v50, v50\n\t"
    "v_mfma_f32_16x16x32_bf16 v[104:107], a[228:231], a[148:151], v[104:107]\n\t"
    "v_cvt_pk_bf16_f32 v48, v48, v49\n\t"
    "v_mfma_f32_16x16x32_bf16 v[112:115], a[228:231], a[164:167], v[112:115]\n\t"
    "v_exp_f32_e32 v51, v51\n\t"
    "v_mfma_f32_16x16x32_bf16 v[120:123], a[228:231], a[180:183], v[120:123]\n\t"
    "ds_read_b64_tr_b16 v[216:217], v232 offset:26112\n\t"
    "ds_read_b64_tr_b16 v[218:219], v232 offset:30208\n\t"
    "v_mov_b32_e32 v253, v252\n\t"
    "v_mfma_f32_16x16x32_bf16 v[96:99], a[232:235], a[136:139], v[96:99]\n\t"
    "v_exp_f32_e32 v52, v52\n\t"
    "v_mfma_f32_16x16x32_bf16 v[104:107], a[232:235], a[152:155], v[104:107]\n\t"
    "v_cvt_pk_bf16_f32 v49, v50, v51\n\t"
    "v_mfma_f32_16x16x32_bf16 v[112:115], a[232:235], a[168:171], v[112:115]\n\t"
    "v_exp_f32_e32 v53, v53\n\t"
    "v_mfma_f32_16x16x32_bf16 v[120:123], a[232:235], a[184:187], v[120:123]\n\t"
    "ds_read_b64_tr_b16 v[220:221], v232 offset:26368\n\t"
    "ds_read_b64_tr_b16 v[222:223], v232 offset:30464\n\t"
    "v_permlane32_swap_b32_e32 v252, v253\n\t"
    "v_mfma_f32_16x16x32_bf16 v[96:99], a[236:239], a[140:143], v[96:99]\n\t"
    "v_exp_f32_e32 v54, v54\n\t"
    "v_mfma_f32_16x16x32_bf16 v[104:107], a[236:239], a[156:159], v[104:107]\n\t"
    "v_cvt_pk_bf16_f32 v50, v52, v53\n\t"
    "v_mfma_f32_16x16x32_bf16 v[112:115], a[236:239], a[172:175], v[112:115]\n\t"
    "v_exp_f32_e32 v55, v55\n\t"
    "v_mfma_f32_16x16x32_bf16 v[120:123], a[236:239], a[188:191], v[120:123]\n\t"
    "buffer_load_dwordx4 v237, s[72:75], s85 offen offset:0 lds\n\t"
    "v_mfma_f32_16x16x32_bf16 v[100:103], a[240:243], a[128:131], v[128:131]\n\t"
    "v_exp_f32_e32 v56, v56\n\t"
    "v_mfma_f32_16x16x32_bf16 v[108:111], a[240:243], a[144:147], v[132:135]\n\t"
    "v_cvt_pk_bf16_f32 v51, v54, v55\n\t"
    "v_mfma_f32_16x16x32_bf16 v[116:119], a[240:243], a[160:163], v[136:139]\n\t"
    "v_exp_f32_e32 v57, v57\n\t"
    "v_mfma_f32_16x16x32_bf16 v[124:127], a[240:243], a[176:179], v[140:143]\n\t"
    "buffer_load_dwordx4 v238, s[72:75], s85 offen offset:1024 lds\n\t"
    "v_mfma_f32_16x16x32_bf16 v[100:103], a[244:247], a[132:135], v[100:103]\n\t"
    "v_exp_f32_e32 v58, v58\n\t"
    "v_mfma_f32_16x16x32_bf16 v[108:111], a[244:247], a[148:151], v[108:111]\n\t"
    "v_cvt_pk_bf16_f32 v56, v56, v57\n\t"
    "v_mfma_f32_16x16x32_bf16 v[116:119], a[244:247], a[164:167], v[116:119]\n\t"
    "v_exp_f32_e32 v59, v59\n\t"
    "v_mfma_f32_16x16x32_bf16 v[124:127], a[244:247], a[180:183], v[124:127]\n\t"
    "buffer_load_dwordx4 v237, s[72:75], s85 offen offset:2048 lds\n\t"
    "v_mfma_f32_16x16x32_bf16 v[100:103], a[248:251], a[136:139], v[100:103]\n\t"
    "v_exp_f32_e32 v60, v60\n\t"
    "v_mfma_f32_16x16x32_bf16 v[108:111], a[248:251], a[152:155], v[108:111]\n\t"
    "v_cvt_pk_bf16_f32 v57, v58, v59\n\t"
    "v_mfma_f32_16x16x32_bf16 v[116:119], a[248:251], a[168:171], v[116:119]\n\t"
    "v_exp_f32_e32 v61, v61\n\t"
    "v_mfma_f32_16x16x32_bf16 v[124:127], a[248:251], a[184:187], v[124:127]\n\t"
    "buffer_load_dwordx4 v238, s[72:75], s85 offen offset:3072 lds\n\t"
    "v_mfma_f32_16x16x32_bf16 v[100:103], a[252:255], a[140:143], v[100:103]\n\t"
    "v_exp_f32_e32 v62, v62\n\t"
    "v_mfma_f32_16x16x32_bf16 v[108:111], a[252:255], a[156:159], v[108:111]\n\t"
    "v_cvt_pk_bf16_f32 v58, v60, v61\n\t"
    "v_mfma_f32_16x16x32_bf16 v[116:119], a[252:255], a[172:175], v[116:119]\n\t"
    "v_exp_f32_e32 v63, v63\n\t"
    "v_mfma_f32_16x16x32_bf16 v[124:127], a[252:255], a[188:191], v[124:127]\n\t"
    "s_waitcnt vmcnt(8) lgkmcnt(0)\n\t"
    "s_barrier\n\t"
    "v_mfma_f32_16x16x32_bf16 a[0:3], v[160:163], v[0:3], a[0:3]\n\t"
    "v_exp_f32_e32 v64, v64\n\t"
    "v_mfma_f32_16x16x32_bf16 a[8:11], v[160:163], v[8:11], a[8:11]\n\t"
    "v_cvt_pk_bf16_f32 v59, v62, v63\n\t"
    "v_mfma_f32_16x16x32_bf16 a[16:19], v[160:163], v[16:19], a[16:19]\n\t"
    "v_exp_f32_e32 v65, v65\n\t"
    "v_mfma_f32_16x16x32_bf16 a[24:27], v[160:163], v[24:27], a[24:27]\n\t"
    "ds_read_b128 a[192:195], v228 offset:0\n\t"
    "ds_read_b128 a[196:199], v229 offset:0\n\t"
    "v_mfma_f32_16x16x32_bf16 a[4:7], v[164:167], v[0:3], a[4:7]\n\t"
    "v_exp_f32_e32 v66, v66\n\t"
    "v_mfma_f32_16x16x32_bf16 a[12:15], v[164:167], v[8:11], a[12:15]\n\t"
    "v_cvt_pk_bf16_f32 v64, v64, v65\n\t"
    "v_mfma_f32_16x16x32_bf16 a[20:23], v[164:167], v[16:19], a[20:23]\n\t"
    "v_exp_f32_e32 v67, v67\n\t"
    "v_mfma_f32_16x16x32_bf16 a[28:31], v[164:167], v[24:27], a[28:31]\n\t"
    "ds_read_b128 a[200:203], v230 offset:0\n\t"
    "ds_read_b128 a[204:207], v231 offset:0\n\t"
    "v_mfma_f32_16x16x32_bf16 a[32:35], v[168:171], v[0:3], a[32:35]\n\t"
    "v_exp_f32_e32 v68, v68\n\t"
    "v_mfma_f32_16x16x32_bf16 a[40:43], v[168:171], v[8:11], a[40:43]\n\t"
    "v_cvt_pk_bf16_f32 v65, v66, v67\n\t"
    "v_mfma_f32_16x16x32_bf16 a[48:51], v[168:171], v[16:19], a[48:51]\n\t"
    "v_exp_f32_e32 v69, v69\n\t"
    "v_mfma_f32_16x16x32_bf16 a[56:59], v[168:171], v[24:27], a[56:59]\n\t"
    "ds_read_b128 a[208:211], v228 offset:4096\n\t"
    "ds_read_b128 a[212:215], v229 offset:4096\n\t"
    "v_mfma_f32_16x16x32_bf16 a[36:39], v[172:175], v[0:3], a[36:39]\n\t"
    "v_exp_f32_e32 v70, v70\n\t"
    "v_mfma_f32_16x16x32_bf16 a[44:47], v[172:175], v[8:11], a[44:47]\n\t"
    "v_cvt_pk_bf16_f32 v66, v68, v69\n\t"
    "v_mfma_f32_16x16x32_bf16 a[52:55], v[172:175], v[16:19], a[52:55]\n\t"
    "v_exp_f32_e32 v71, v71\n\t"
    "v_mfma_f32_16x16x32_bf16 a[60:63], v[172:175], v[24:27], a[60:63]\n\t"
    "ds_read_b128 a[216:219], v230 offset:4096\n\t"
    "ds_read_b128 a[220:223], v231 offset:4096\n\t"
    "v_mfma_f32_16x16x32_bf16 a[64:67], v[176:179], v[0:3], a[64:67]\n\t"
    "v_exp_f32_e32 v72, v72\n\t"
    "v_mfma_f32_16x16x32_bf16 a[72:75], v[176:179], v[8:11], a[72:75]\n\t"
    "v_cvt_pk_bf16_f32 v67, v70, v71\n\t"
    "v_mfma_f32_16x16x32_bf16 a[80:83], v[176:179], v[16:19], a[80:83]\n\t"
    "v_exp_f32_e32 v73, v73\n\t"
    "v_mfma_f32_16x16x32_bf16 a[88:91], v[176:179], v[24:27], a[88:91]\n\t"
    "ds_read_b128 a[224:227], v228 offset:8192\n\t"
    "ds_read_b128 a[228:231], v229 offset:8192\n\t"
    "v_mfma_f32_16x16x32_bf16 a[68:71], v[180:183], v[0:3], a[68:71]\n\t"
    "v_exp_f32_e32 v74, v74\n\t"
    "v_mfma_f32_16x16x32_bf16 a[76:79], v[180:183], v[8:11], a[76:79]\n\t"
    "v_cvt_pk_bf16_f32 v72, v72, v73\n\t"
    "v_mfma_f32_16x16x32_bf16 a[84:87], v[180:183], v[16:19], a[84:87]\n\t"
    "v_exp_f32_e32 v75, v75\n\t"
    "v_mfma_f32_16x16x32_bf16 a[92:95], v[180:183], v[24:27], a[92:95]\n\t"
    "ds_read_b128 a[232:235], v230 offset:8192\n\t"
    "ds_read_b128 a[236:239], v231 offset:8192\n\t"
    "v_mfma_f32_16x16x32_bf16 a[96:99], v[184:187], v[0:3], a[96:99]\n\t"
    "v_exp_f32_e32 v76, v76\n\t"
    "v_mfma_f32_16x16x32_bf16 a[104:107], v[184:187], v[8:11], a[104:107]\n\t"
    "v_cvt_pk_bf16_f32 v73, v74, v75\n\t"
    "v_mfma_f32_16x16x32_bf16 a[112:115], v[184:187], v[16:19], a[112:115]\n\t"
    "v_exp_f32_e32 v77, v77\n\t"
    "v_mfma_f32_16x16x32_bf16 a[120:123], v[184:187], v[24:27], a[120:123]\n\t"
    "ds_read_b128 a[240:243], v228 offset:12288\n\t"
    "ds_read_b128 a[244:247], v229 offset:12288\n\t"
    "v_mfma_f32_16x16x32_bf16 a[100:103], v[188:191], v[0:3], a[100:103]\n\t"
    "v_exp_f32_e32 v78, v78\n\t"
    "v_mfma_f32_16x16x32_bf16 a[108:111], v[188:191], v[8:11], a[108:111]\n\t"
    "v_cvt_pk_bf16_f32 v74, v76, v77\n\t"
    "v_mfma_f32_16x16x32_bf16 a[116:119], v[188:191], v[16:19], a[116:119]\n\t"
    "v_exp_f32_e32 v79, v79\n\t"
    "v_mfma_f32_16x16x32_bf16 a[124:127], v[188:191], v[24:27], a[124:127]\n\t"
    "ds_read_b128 a[248:251], v230 offset:12288\n\t"
    "ds_read_b128 a[252:255], v231 offset:12288\n\t"
    "v_mfma_f32_16x16x32_bf16 v[144:147], v[224:227], v[0:3], v[144:147]\n\t"
    "v_exp_f32_e32 v80, v80\n\t"
    "v_mfma_f32_16x16x32_bf16 v[148:151], v[224:227], v[8:11], v[148:151]\n\t"
    "v_cvt_pk_bf16_f32 v75, v78, v79\n\t"
    "v_mfma_f32_16x16x32_bf16 v[152:155], v[224:227], v[16:19], v[152:155]\n\t"
    "v_exp_f32_e32 v81, v81\n\t"
    "v_mfma_f32_16x16x32_bf16 v[156:159], v[224:227], v[24:27], v[156:159]\n\t"
    "s_cmp_lt_u32 s84, 0x80000\n\t"
    "s_cselect_b32 m0, s92, s97\n\t"
    "v_mfma_f32_16x16x32_bf16 a[0:3], v[192:195], v[32:35], a[0:3]\n\t"
    "v_exp_f32_e32 v82, v82\n\t"
    "v_mfma_f32_16x16x32_bf16 a[8:11], v[192:195], v[40:43], a[8:11]\n\t"
    "v_cvt_pk_bf16_f32 v80, v80, v81\n\t"
    "v_mfma_f32_16x16x32_bf16 a[16:19], v[192:195], v[48:51], a[16:19]\n\t"
    "v_exp_f32_e32 v83, v83\n\t"
    "v_mfma_f32_16x16x32_bf16 a[24:27], v[192:195], v[56:59], a[24:27]\n\t"
    "buffer_load_dwordx4 v233, s[68:71], s84 offen offset:0 lds\n\t"
    "s_add_u32 s85, s85, 0x4000\n\t"
    "v_mfma_f32_16x16x32_bf16 a[4:7], v[196:199], v[32:35], a[4:7]\n\t"
    "v_exp_f32_e32 v84, v84\n\t"
    "v_mfma_f32_16x16x32_bf16 a[12:15], v[196:199], v[40:43], a[12:15]\n\t"
    "v_cvt_pk_bf16_f32 v81, v82, v83\n\t"
    "v_mfma_f32_16x16x32_bf16 a[20:23], v[196:199], v[48:51], a[20:23]\n\t"
    "v_exp_f32_e32 v85, v85\n\t"
    "v_mfma_f32_16x16x32_bf16 a[28:31], v[196:199], v[56:59], a[28:31]\n\t"
    "buffer_load_dwordx4 v234, s[68:71], s84 offen offset:1024 lds\n\t"
    "v_mfma_f32_16x16x32_bf16 a[32:35], v[200:203], v[32:35], a[32:35]\n\t"
    "v_exp_f32_e32 v86, v86\n\t"
    "v_mfma_f32_16x16x32_bf16 a[40:43], v[200:203], v[40:43], a[40:43]\n\t"
    "v_cvt_pk_bf16_f32 v82, v84, v85\n\t"
    "v_mfma_f32_16x16x32_bf16 a[48:51], v[200:203], v[48:51], a[48:51]\n\t"
    "v_exp_f32_e32 v87, v87\n\t"
    "v_mfma_f32_16x16x32_bf16 a[56:59], v[200:203], v[56:59], a[56:59]\n\t"
    "buffer_load_dwordx4 v235, s[68:71], s84 offen offset:2048 lds\n\t"
    "v_mfma_f32_16x16x32_bf16 a[36:39], v[204:207], v[32:35], a[36:39]\n\t"
    "v_exp_f32_e32 v88, v88\n\t"
    "v_mfma_f32_16x16x32_bf16 a[44:47], v[204:207], v[40:43], a[44:47]\n\t"
    "v_cvt_pk_bf16_f32 v83, v86, v87\n\t"
    "v_mfma_f32_16x16x32_bf16 a[52:55], v[204:207], v[48:51], a[52:55]\n\t"
    "v_exp_f32_e32 v89, v89\n\t"
    "v_mfma_f32_16x16x32_bf16 a[60:63], v[204:207], v[56:59], a[60:63]\n\t"
    "buffer_load_dwordx4 v236, s[68:71], s84 offen offset:3072 lds\n\t"
    "v_mfma_f32_16x16x32_bf16 a[64:67], v[208:211], v[32:35], a[64:67]\n\t"
    "v_exp_f32_e32 v90, v90\n\t"
    "v_mfma_f32_16x16x32_bf16 a[72:75], v[208:211], v[40:43], a[72:75]\n\t"
    "v_cvt_pk_bf16_f32 v88, v88, v89\n\t"
    "v_mfma_f32_16x16x32_bf16 a[80:83], v[208:211], v[48:51], a[80:83]\n\t"
    "v_exp_f32_e32 v91, v91\n\t"
    "v_mfma_f32_16x16x32_bf16 a[88:91], v[208:211], v[56:59], a[88:91]\n\t"
    "s_waitcnt lgkmcnt(0)\n\t"
    "ds_read_b64_tr_b16 v[160:161], v232 offset:0\n\t"
    "v_mfma_f32_16x16x32_bf16 a[68:71], v[212:215], v[32:35], a[68:71]\n\t"
    "v_exp_f32_e32 v92, v92\n\t"
    "v_mfma_f32_16x16x32_bf16 a[76:79], v[212:215], v[40:43], a[76:79]\n\t"
    "v_cvt_pk_bf16_f32 v89, v90, v91\n\t"
    "v_mfma_f32_16x16x32_bf16 a[84:87], v[212:215], v[48:51], a[84:87]\n\t"
    "v_exp_f32_e32 v93, v93\n\t"
    "v_mfma_f32_16x16x32_bf16 a[92:95], v[212:215], v[56:59], a[92:95]\n\t"
    "ds_read_b64_tr_b16 v[162:163], v232 offset:4096\n\t"
    "ds_read_b64_tr_b16 v[164:165], v232 offset:256\n\t"
    "v_mfma_f32_16x16x32_bf16 a[96:99], v[216:219], v[32:35], a[96:99]\n\t"
    "v_exp_f32_e32 v94, v94\n\t"
    "v_mfma_f32_16x16x32_bf16 a[104:107], v[216:219], v[40:43], a[104:107]\n\t"
    "v_cvt_pk_bf16_f32 v90, v92, v93\n\t"
    "v_mfma_f32_16x16x32_bf16 a[112:115], v[216:219], v[48:51], a[112:115]\n\t"
    "v_exp_f32_e32 v95, v95\n\t"
    "v_mfma_f32_16x16x32_bf16 a[120:123], v[216:219], v[56:59], a[120:123]\n\t"
    "ds_read_b64_tr_b16 v[166:167], v232 offset:4352\n\t"
    "ds_read_b64_tr_b16 v[168:169], v232 offset:512\n\t"
    "v_mfma_f32_16x16x32_bf16 a[100:103], v[220:223], v[32:35], a[100:103]\n\t"
    "ds_read_b64_tr_b16 v[170:171], v232 offset:4608\n\t"
    "ds_read_b64_tr_b16 v[172:173], v232 offset:768\n\t"
    "v_mfma_f32_16x16x32_bf16 a[108:111], v[220:223], v[40:43], a[108:111]\n\t"
    "v_cvt_pk_bf16_f32 v91, v94, v95\n\t"
    "v_mfma_f32_16x16x32_bf16 a[116:119], v[220:223], v[48:51], a[116:119]\n\t"
    "ds_read_b64_tr_b16 v[174:175], v232 offset:4864\n\t"
    "ds_read_b64_tr_b16 v[176:177], v232 offset:1024\n\t"
    "v_mfma_f32_16x16x32_bf16 a[124:127], v[220:223], v[56:59], a[124:127]\n\t"
    "ds_read_b64_tr_b16 v[178:179], v232 offset:5120\n\t"
    "s_add_u32 s84, s84, 0x4000\n\t"
    "v_mfma_f32_16x16x32_bf16 v[144:147], v[224:227], v[32:35], v[144:147]\n\t"
    "v_max_u32_e32 v252, v252, v253\n\t"
    "v_mfma_f32_16x16x32_bf16 v[148:151], v[224:227], v[40:43], v[148:151]\n\t"
    "ds_write2_b32 v249, v252, v252 offset1:4\n\t"
    "v_mfma_f32_16x16x32_bf16 v[152:155], v[224:227], v[48:51], v[152:155]\n\t"
    "v_mfma_f32_16x16x32_bf16 v[156:159], v[224:227], v[56:59], v[156:159]\n\t"
    "v_mfma_f32_16x16x32_bf16 v[0:3], a[192:195], a[128:131], v[128:131]\n\t"
    "v_exp_f32_e32 v96, v96\n\t"
    "v_mfma_f32_16x16x32_bf16 v[8:11], a[192:195], a[144:147], v[132:135]\n\t"
    "s_cmp_lt_u32 s85, 0x80000\n\t"
    "s_cselect_b32 m0, s94, s97\n\t"
    "v_mfma_f32_16x16x32_bf16 v[16:19], a[192:195], a[160:163], v[136:139]\n\t"
    "v_exp_f32_e32 v97, v97\n\t"
    "v_mfma_f32_16x16x32_bf16 v[24:27], a[192:195], a[176:179], v[140:143]\n\t"
    "ds_read_b64_tr_b16 v[180:181], v232 offset:1280\n\t"
    "ds_read_b64_tr_b16 v[182:183], v232 offset:5376\n\t"
    "v_mfma_f32_16x16x32_bf16 v[0:3], a[196:199], a[132:135], v[0:3]\n\t"
    "v_exp_f32_e32 v98, v98\n\t"
    "v_mfma_f32_16x16x32_bf16 v[8:11], a[196:199], a[148:151], v[8:11]\n\t"
    "v_cvt_pk_bf16_f32 v96, v96, v97\n\t"
    "v_mfma_f32_16x16x32_bf16 v[16:19], a[196:199], a[164:167], v[16:19]\n\t"
    "v_exp_f32_e32 v99, v99\n\t"
    "v_mfma_f32_16x16x32_bf16 v[24:27], a[196:199], a[180:183], v[24:27]\n\t"
    "ds_read_b64_tr_b16 v[184:185], v232 offset:1536\n\t"
    "ds_read_b64_tr_b16 v[186:187], v232 offset:5632\n\t"
    "v_mfma_f32_16x16x32_bf16 v[0:3], a[200:203], a[136:139], v[0:3]\n\t"
    "v_exp_f32_e32 v100, v100\n\t"
    "v_mfma_f32_16x16x32_bf16 v[8:11], a[200:203], a[152:155], v[8:11]\n\t"
    "v_cvt_pk_bf16_f32 v97, v98, v99\n\t"
    "v_mfma_f32_16x16x32_bf16 v[16:19], a[200:203], a[168:171], v[16:19]\n\t"
    "v_exp_f32_e32 v101, v101\n\t"
    "v_mfma_f32_16x16x32_bf16 v[24:27], a[200:203], a[184:187], v[24:27]\n\t"
    "ds_read_b64_tr_b16 v[188:189], v232 offset:1792\n\t"
    "ds_read_b64_tr_b16 v[190:191], v232 offset:5888\n\t"
    "v_mfma_f32_16x16x32_bf16 v[0:3], a[204:207], a[140:143], v[0:3]\n\t"
    "v_exp_f32_e32 v102, v102\n\t"
    "v_mfma_f32_16x16x32_bf16 v[8:11], a[204:207], a[156:159], v[8:11]\n\t"
    "v_cvt_pk_bf16_f32 v98, v100, v101\n\t"
    "v_mfma_f32_16x16x32_bf16 v[16:19], a[204:207], a[172:175], v[16:19]\n\t"
    "v_exp_f32_e32 v103, v103\n\t"
    "v_mfma_f32_16x16x32_bf16 v[24:27], a[204:207], a[188:191], v[24:27]\n\t"
    "ds_read_b64_tr_b16 v[192:193], v232 offset:8192\n\t"
    "ds_read_b64_tr_b16 v[194:195], v232 offset:12288\n\t"
    "v_mfma_f32_16x16x32_bf16 v[4:7], a[208:211], a[128:131], v[128:131]\n\t"
    "v_exp_f32_e32 v104, v104\n\t"
    "v_mfma_f32_16x16x32_bf16 v[12:15], a[208:211], a[144:147], v[132:135]\n\t"
    "v_cvt_pk_bf16_f32 v99, v102, v103\n\t"
    "v_mfma_f32_16x16x32_bf16 v[20:23], a[208:211], a[160:163], v[136:139]\n\t"
    "v_exp_f32_e32 v105, v105\n\t"
    "v_mfma_f32_16x16x32_bf16 v[28:31], a[208:211], a[176:179], v[140:143]\n\t"
    "ds_read_b64_tr_b16 v[196:197], v232 offset:8448\n\t"
    "ds_read_b64_tr_b16 v[198:199], v232 offset:12544\n\t"
    "v_mfma_f32_16x16x32_bf16 v[4:7], a[212:215], a[132:135], v[4:7]\n\t"
    "v_exp_f32_e32 v106, v106\n\t"
    "v_mfma_f32_16x16x32_bf16 v[12:15], a[212:215], a[148:151], v[12:15]\n\t"
    "v_cvt_pk_bf16_f32 v104, v104, v105\n\t"
    "v_mfma_f32_16x16x32_bf16 v[20:23], a[212:215], a[164:167], v[20:23]\n\t"
    "v_exp_f32_e32 v107, v107\n\t"
    "v_mfma_f32_16x16x32_bf16 v[28:31], a[212:215], a[180:183], v[28:31]\n\t"
    "ds_read_b64_tr_b16 v[200:201], v232 offset:8704\n\t"
    "ds_read_b64_tr_b16 v[202:203], v232 offset:12800\n\t"
    "v_mfma_f32_16x16x32_bf16 v[4:7], a[216:219], a[136:139], v[4:7]\n\t"
    "v_exp_f32_e32 v108, v108\n\t"
    "v_mfma_f32_16x16x32_bf16 v[12:15], a[216:219], a[152:155], v[12:15]\n\t"
    "v_cvt_pk_bf16_f32 v105, v106, v107\n\t"
    "v_mfma_f32_16x16x32_bf16 v[20:23], a[216:219], a[168:171], v[20:23]\n\t"
    "v_exp_f32_e32 v109, v109\n\t"
    "v_mfma_f32_16x16x32_bf16 v[28:31], a[216:219], a[184:187], v[28:31]\n\t"
    "ds_read_b64_tr_b16 v[204:205], v232 offset:8960\n\t"
    "ds_read_b64_tr_b16 v[206:207], v232 offset:13056\n\t"
    "v_mfma_f32_16x16x32_bf16 v[4:7], a[220:223], a[140:143], v[4:7]\n\t"
    "v_exp_f32_e32 v110, v110\n\t"
    "v_mfma_f32_16x16x32_bf16 v[12:15], a[220:223], a[156:159], v[12:15]\n\t"
    "v_cvt_pk_bf16_f32 v106, v108, v109\n\t"
    "v_mfma_f32_16x16x32_bf16 v[20:23], a[220:223], a[172:175], v[20:23]\n\t"
    "v_exp_f32_e32 v111, v111\n\t"
    "v_mfma_f32_16x16x32_bf16 v[28:31], a[220:223], a[188:191], v[28:31]\n\t"
    "ds_read_b64_tr_b16 v[208:209], v232 offset:9216\n\t"
    "ds_read_b64_tr_b16 v[210:211], v232 offset:13312\n\t"
    "v_mfma_f32_16x16x32_bf16 v[32:35], a[224:227], a[128:131], v[128:131]\n\t"
    "v_exp_f32_e32 v112, v112\n\t"
    "v_mfma_f32_16x16x32_bf16 v[40:43], a[224:227], a[144:147], v[132:135]\n\t"
    "v_cvt_pk_bf16_f32 v107, v110, v111\n\t"
    "v_mfma_f32_16x16x32_bf16 v[48:51], a[224:227], a[160:163], v[136:139]\n\t"
    "v_exp_f32_e32 v113, v113\n\t"
    "v_mfma_f32_16x16x32_bf16 v[56:59], a[224:227], a[176:179], v[140:143]\n\t"
    "ds_read_b64_tr_b16 v[212:213], v232 offset:9472\n\t"
    "ds_read_b64_tr_b16 v[214:215], v232 offset:13568\n\t"
    "v_mfma_f32_16x16x32_bf16 v[32:35], a[228:231], a[132:135], v[32:35]\n\t"
    "v_exp_f32_e32 v114, v114\n\t"
    "v_mfma_f32_16x16x32_bf16 v[40:43], a[228:231], a[148:151], v[40:43]\n\t"
    "v_cvt_pk_bf16_f32 v112, v112, v113\n\t"
    "v_mfma_f32_16x16x32_bf16 v[48:51], a[228:231], a[164:167], v[48:51]\n\t"
    "v_exp_f32_e32 v115, v115\n\t"
    "v_mfma_f32_16x16x32_bf16 v[56:59], a[228:231], a[180:183], v[56:59]\n\t"
    "ds_read_b64_tr_b16 v[216:217], v232 offset:9728\n\t"
    "ds_read_b64_tr_b16 v[218:219], v232 offset:13824\n\t"
    "v_mfma_f32_16x16x32_bf16 v[32:35], a[232:235], a[136:139], v[32:35]\n\t"
    "v_exp_f32_e32 v116, v116\n\t"
    "v_mfma_f32_16x16x32_bf16 v[40:43], a[232:235], a[152:155], v[40:43]\n\t"
    "v_cvt_pk_bf16_f32 v113, v114, v115\n\t"
    "v_mfma_f32_16x16x32_bf16 v[48:51], a[232:235], a[168:171], v[48:51]\n\t"
    "v_exp_f32_e32 v117, v117\n\t"
    "v_mfma_f32_16x16x32_bf16 v[56:59], a[232:235], a[184:187], v[56:59]\n\t"
    "ds_read_b64_tr_b16 v[220:221], v232 offset:9984\n\t"
    "ds_read_b64_tr_b16 v[222:223], v232 offset:14080\n\t"
    "v_mfma_f32_16x16x32_bf16 v[32:35], a[236:239], a[140:143], v[32:35]\n\t"
    "v_exp_f32_e32 v118, v118\n\t"
    "v_mfma_f32_16x16x32_bf16 v[40:43], a[236:239], a[156:159], v[40:43]\n\t"
    "v_cvt_pk_bf16_f32 v114, v116, v117\n\t"
    "v_mfma_f32_16x16x32_bf16 v[48:51], a[236:239], a[172:175], v[48:51]\n\t"
    "v_exp_f32_e32 v119, v119\n\t"
    "v_mfma_f32_16x16x32_bf16 v[56:59], a[236:239], a[188:191], v[56:59]\n\t"
    "buffer_load_dwordx4 v237, s[72:75], s85 offen offset:0 lds\n\t"
    "v_mfma_f32_16x16x32_bf16 v[36:39], a[240:243], a[128:131], v[128:131]\n\t"
    "v_exp_f32_e32 v120, v120\n\t"
    "v_mfma_f32_16x16x32_bf16 v[44:47], a[240:243], a[144:147], v[132:135]\n\t"
    "v_cvt_pk_bf16_f32 v115, v118, v119\n\t"
    "v_mfma_f32_16x16x32_bf16 v[52:55], a[240:243], a[160:163], v[136:139]\n\t"
    "v_exp_f32_e32 v121, v121\n\t"
    "v_mfma_f32_16x16x32_bf16 v[60:63], a[240:243], a[176:179], v[140:143]\n\t"
    "buffer_load_dwordx4 v238, s[72:75], s85 offen offset:1024 lds\n\t"
    "v_mfma_f32_16x16x32_bf16 v[36:39], a[244:247], a[132:135], v[36:39]\n\t"
    "v_exp_f32_e32 v122, v122\n\t"
    "v_mfma_f32_16x16x32_bf16 v[44:47], a[244:247], a[148:151], v[44:47]\n\t"
    "v_cvt_pk_bf16_f32 v120, v120, v121\n\t"
    "v_mfma_f32_16x16x32_bf16 v[52:55], a[244:247], a[164:167], v[52:55]\n\t"
    "v_exp_f32_e32 v123, v123\n\t"
    "v_mfma_f32_16x16x32_bf16 v[60:63], a[244:247], a[180:183], v[60:63]\n\t"
    "buffer_load_dwordx4 v237, s[72:75], s85 offen offset:2048 lds\n\t"
    "v_mfma_f32_16x16x32_bf16 v[36:39], a[248:251], a[136:139], v[36:39]\n\t"
    "v_exp_f32_e32 v124, v124\n\t"
    "v_mfma_f32_16x16x32_bf16 v[44:47], a[248:251], a[152:155], v[44:47]\n\t"
    "v_cvt_pk_bf16_f32 v121, v122, v123\n\t"
    "v_mfma_f32_16x16x32_bf16 v[52:55], a[248:251], a[168:171], v[52:55]\n\t"
    "v_exp_f32_e32 v125, v125\n\t"
    "v_mfma_f32_16x16x32_bf16 v[60:63], a[248:251], a[184:187], v[60:63]\n\t"
    "buffer_load_dwordx4 v238, s[72:75], s85 offen offset:3072 lds\n\t"
    "v_mfma_f32_16x16x32_bf16 v[36:39], a[252:255], a[140:143], v[36:39]\n\t"
    "v_exp_f32_e32 v126, v126\n\t"
    "v_mfma_f32_16x16x32_bf16 v[44:47], a[252:255], a[156:159], v[44:47]\n\t"
    "v_cvt_pk_bf16_f32 v122, v124, v125\n\t"
    "v_mfma_f32_16x16x32_bf16 v[52:55], a[252:255], a[172:175], v[52:55]\n\t"
    "v_exp_f32_e32 v127, v127\n\t"
    "v_mfma_f32_16x16x32_bf16 v[60:63], a[252:255], a[188:191], v[60:63]\n\t"
    "s_waitcnt vmcnt(8) lgkmcnt(0)\n\t"
    "s_barrier\n\t"
    "v_mfma_f32_16x16x32_bf16 a[0:3], v[160:163], v[64:67], a[0:3]\n\t"
    "v_exp_f32_e32 v0, v0\n\t"
    "v_mfma_f32_16x16x32_bf16 a[8:11], v[160:163], v[72:75], a[8:11]\n\t"
    "v_cvt_pk_bf16_f32 v123, v126, v127\n\t"
    "v_mfma_f32_16x16x32_bf16 a[16:19], v[160:163], v[80:83], a[16:19]\n\t"
    "v_exp_f32_e32 v1, v1\n\t"
    "v_mfma_f32_16x16x32_bf16 a[24:27], v[160:163], v[88:91], a[24:27]\n\t"
    "ds_read_b128 a[192:195], v228 offset:16384\n\t"
    "ds_read_b128 a[196:199], v229 offset:16384\n\t"
    "v_mfma_f32_16x16x32_bf16 a[4:7], v[164:167], v[64:67], a[4:7]\n\t"
    "v_exp_f32_e32 v2, v2\n\t"
    "v_mfma_f32_16x16x32_bf16 a[12:15], v[164:167], v[72:75], a[12:15]\n\t"
    "v_cvt_pk_bf16_f32 v0, v0, v1\n\t"
    "v_mfma_f32_16x16x32_bf16 a[20:23], v[164:167], v[80:83], a[20:23]\n\t"
    "v_exp_f32_e32 v3, v3\n\t"
    "v_mfma_f32_16x16x32_bf16 a[28:31], v[164:167], v[88:91], a[28:31]\n\t"
    "ds_read_b128 a[200:203], v230 offset:16384\n\t"
    "ds_read_b128 a[204:207], v231 offset:16384\n\t"
    "v_mfma_f32_16x16x32_bf16 a[32:35], v[168:171], v[64:67], a[32:35]\n\t"
    "v_exp_f32_e32 v4, v4\n\t"
    "v_mfma_f32_16x16x32_bf16 a[40:43], v[168:171], v[72:75], a[40:43]\n\t"
    "v_cvt_pk_bf16_f32 v1, v2, v3\n\t"
    "v_mfma_f32_16x16x32_bf16 a[48:51], v[168:171], v[80:83], a[48:51]\n\t"
    "v_exp_f32_e32 v5, v5\n\t"
    "v_mfma_f32_16x16x32_bf16 a[56:59], v[168:171], v[88:91], a[56:59]\n\t"
    "ds_read_b128 a[208:211], v228 offset:20480\n\t"
    "ds_read_b128 a[212:215], v229 offset:20480\n\t"
    "v_mfma_f32_16x16x32_bf16 a[36:39], v[172:175], v[64:67], a[36:39]\n\t"
    "v_exp_f32_e32 v6, v6\n\t"
    "v_mfma_f32_16x16x32_bf16 a[44:47], v[172:175], v[72:75], a[44:47]\n\t"
    "v_cvt_pk_bf16_f32 v2, v4, v5\n\t"
    "v_mfma_f32_16x16x32_bf16 a[52:55], v[172:175], v[80:83], a[52:55]\n\t"
    "v_exp_f32_e32 v7, v7\n\t"
    "v_mfma_f32_16x16x32_bf16 a[60:63], v[172:175], v[88:91], a[60:63]\n\t"
    "ds_read_b128 a[216:219], v230 offset:20480\n\t"
    "ds_read_b128 a[220:223], v231 offset:20480\n\t"
    "v_mfma_f32_16x16x32_bf16 a[64:67], v[176:179], v[64:67], a[64:67]\n\t"
    "v_exp_f32_e32 v8, v8\n\t"
    "v_mfma_f32_16x16x32_bf16 a[72:75], v[176:179], v[72:75], a[72:75]\n\t"
    "v_cvt_pk_bf16_f32 v3, v6, v7\n\t"
    "v_mfma_f32_16x16x32_bf16 a[80:83], v[176:179], v[80:83], a[80:83]\n\t"
    "v_exp_f32_e32 v9, v9\n\t"
    "v_mfma_f32_16x16x32_bf16 a[88:91], v[176:179], v[88:91], a[88:91]\n\t"
    "ds_read_b128 a[224:227], v228 offset:24576\n\t"
    "ds_read_b128 a[228:231], v229 offset:24576\n\t"
    "v_mfma_f32_16x16x32_bf16 a[68:71], v[180:183], v[64:67], a[68:71]\n\t"
    "v_exp_f32_e32 v10, v10\n\t"
    "v_mfma_f32_16x16x32_bf16 a[76:79], v[180:183], v[72:75], a[76:79]\n\t"
    "v_cvt_pk_bf16_f32 v8, v8, v9\n\t"
    "v_mfma_f32_16x16x32_bf16 a[84:87], v[180:183], v[80:83], a[84:87]\n\t"
    "v_exp_f32_e32 v11, v11\n\t"
    "v_mfma_f32_16x16x32_bf16 a[92:95], v[180:183], v[88:91], a[92:95]\n\t"
    "ds_read_b128 a[232:235], v230 offset:24576\n\t"
    "ds_read_b128 a[236:239], v231 offset:24576\n\t"
    "v_mfma_f32_16x16x32_bf16 a[96:99], v[184:187], v[64:67], a[96:99]\n\t"
    "v_exp_f32_e32 v12, v12\n\t"
    "v_mfma_f32_16x16x32_bf16 a[104:107], v[184:187], v[72:75], a[104:107]\n\t"
    "v_cvt_pk_bf16_f32 v9, v10, v11\n\t"
    "v_mfma_f32_16x16x32_bf16 a[112:115], v[184:187], v[80:83], a[112:115]\n\t"
    "v_exp_f32_e32 v13, v13\n\t"
    "v_mfma_f32_16x16x32_bf16 a[120:123], v[184:187], v[88:91], a[120:123]\n\t"
    "ds_read_b128 a[240:243], v228 offset:28672\n\t"
    "ds_read_b128 a[244:247], v229 offset:28672\n\t"
    "v_mfma_f32_16x16x32_bf16 a[100:103], v[188:191], v[64:67], a[100:103]\n\t"
    "v_exp_f32_e32 v14, v14\n\t"
    "v_mfma_f32_16x16x32_bf16 a[108:111], v[188:191], v[72:75], a[108:111]\n\t"
    "v_cvt_pk_bf16_f32 v10, v12, v13\n\t"
    "v_mfma_f32_16x16x32_bf16 a[116:119], v[188:191], v[80:83], a[116:119]\n\t"
    "v_exp_f32_e32 v15, v15\n\t"
    "v_mfma_f32_16x16x32_bf16 a[124:127], v[188:191], v[88:91], a[124:127]\n\t"
    "ds_read_b128 a[248:251], v230 offset:28672\n\t"
    "ds_read_b128 a[252:255], v231 offset:28672\n\t"
    "v_mfma_f32_16x16x32_bf16 v[144:147], v[224:227], v[64:67], v[144:147]\n\t"
    "v_exp_f32_e32 v16, v16\n\t"
    "v_mfma_f32_16x16x32_bf16 v[148:151], v[224:227], v[72:75], v[148:151]\n\t"
    "v_cvt_pk_bf16_f32 v11, v14, v15\n\t"
    "v_mfma_f32_16x16x32_bf16 v[152:155], v[224:227], v[80:83], v[152:155]\n\t"
    "v_exp_f32_e32 v17, v17\n\t"
    "v_mfma_f32_16x16x32_bf16 v[156:159], v[224:227], v[88:91], v[156:159]\n\t"
    "s_cmp_lt_u32 s84, 0x80000\n\t"
    "s_cselect_b32 m0, s90, s97\n\t"
    "v_mfma_f32_16x16x32_bf16 a[0:3], v[192:195], v[96:99], a[0:3]\n\t"
    "v_exp_f32_e32 v18, v18\n\t"
    "v_mfma_f32_16x16x32_bf16 a[8:11], v[192:195], v[104:107], a[8:11]\n\t"
    "v_cvt_pk_bf16_f32 v16, v16, v17\n\t"
    "v_mfma_f32_16x16x32_bf16 a[16:19], v[192:195], v[112:115], a[16:19]\n\t"
    "v_exp_f32_e32 v19, v19\n\t"
    "v_mfma_f32_16x16x32_bf16 a[24:27], v[192:195], v[120:123], a[24:27]\n\t"
    "buffer_load_dwordx4 v233, s[68:71], s84 offen offset:0 lds\n\t"
    "s_add_u32 s85, s85, 0x4000\n\t"
    "v_mfma_f32_16x16x32_bf16 a[4:7], v[196:199], v[96:99], a[4:7]\n\t"
    "v_exp_f32_e32 v20, v20\n\t"
    "v_mfma_f32_16x16x32_bf16 a[12:15], v[196:199], v[104:107], a[12:15]\n\t"
    "v_cvt_pk_bf16_f32 v17, v18, v19\n\t"
    "v_mfma_f32_16x16x32_bf16 a[20:23], v[196:199], v[112:115], a[20:23]\n\t"
    "v_exp_f32_e32 v21, v21\n\t"
    "v_mfma_f32_16x16x32_bf16 a[28:31], v[196:199], v[120:123], a[28:31]\n\t"
    "buffer_load_dwordx4 v234, s[68:71], s84 offen offset:1024 lds\n\t"
    "v_mfma_f32_16x16x32_bf16 a[32:35], v[200:203], v[96:99], a[32:35]\n\t"
    "v_exp_f32_e32 v22, v22\n\t"
    "v_mfma_f32_16x16x32_bf16 a[40:43], v[200:203], v[104:107], a[40:43]\n\t"
    "v_cvt_pk_bf16_f32 v18, v20, v21\n\t"
    "v_mfma_f32_16x16x32_bf16 a[48:51], v[200:203], v[112:115], a[48:51]\n\t"
    "v_exp_f32_e32 v23, v23\n\t"
    "v_mfma_f32_16x16x32_bf16 a[56:59], v[200:203], v[120:123], a[56:59]\n\t"
    "buffer_load_dwordx4 v235, s[68:71], s84 offen offset:2048 lds\n\t"
    "v_mfma_f32_16x16x32_bf16 a[36:39], v[204:207], v[96:99], a[36:39]\n\t"
    "v_exp_f32_e32 v24, v24\n\t"
    "v_mfma_f32_16x16x32_bf16 a[44:47], v[204:207], v[104:107], a[44:47]\n\t"
    "v_cvt_pk_bf16_f32 v19, v22, v23\n\t"
    "v_mfma_f32_16x16x32_bf16 a[52:55], v[204:207], v[112:115], a[52:55]\n\t"
    "v_exp_f32_e32 v25, v25\n\t"
    "v_mfma_f32_16x16x32_bf16 a[60:63], v[204:207], v[120:123], a[60:63]\n\t"
    "buffer_load_dwordx4 v236, s[68:71], s84 offen offset:3072 lds\n\t"
    "v_mfma_f32_16x16x32_bf16 a[64:67], v[208:211], v[96:99], a[64:67]\n\t"
    "v_exp_f32_e32 v26, v26\n\t"
    "v_mfma_f32_16x16x32_bf16 a[72:75], v[208:211], v[104:107], a[72:75]\n\t"
    "v_cvt_pk_bf16_f32 v24, v24, v25\n\t"
    "v_mfma_f32_16x16x32_bf16 a[80:83], v[208:211], v[112:115], a[80:83]\n\t"
    "v_exp_f32_e32 v27, v27\n\t"
    "v_mfma_f32_16x16x32_bf16 a[88:91], v[208:211], v[120:123], a[88:91]\n\t"
    "s_waitcnt lgkmcnt(0)\n\t"
    "ds_read_b64_tr_b16 v[160:161], v232 offset:32768\n\t"
    "v_mfma_f32_16x16x32_bf16 a[68:71], v[212:215], v[96:99], a[68:71]\n\t"
    "v_exp_f32_e32 v28, v28\n\t"
    "v_mfma_f32_16x16x32_bf16 a[76:79], v[212:215], v[104:107], a[76:79]\n\t"
    "v_cvt_pk_bf16_f32 v25, v26, v27\n\t"
    "v_mfma_f32_16x16x32_bf16 a[84:87], v[212:215], v[112:115], a[84:87]\n\t"
    "v_exp_f32_e32 v29, v29\n\t"
    "v_mfma_f32_16x16x32_bf16 a[92:95], v[212:215], v[120:123], a[92:95]\n\t"
    "ds_read_b64_tr_b16 v[162:163], v232 offset:36864\n\t"
    "ds_read_b64_tr_b16 v[164:165], v232 offset:33024\n\t"
    "v_mfma_f32_16x16x32_bf16 a[96:99], v[216:219], v[96:99], a[96:99]\n\t"
    "v_exp_f32_e32 v30, v30\n\t"
    "v_mfma_f32_16x16x32_bf16 a[104:107], v[216:219], v[104:107], a[104:107]\n\t"
    "v_cvt_pk_bf16_f32 v26, v28, v29\n\t"
    "v_mfma_f32_16x16x32_bf16 a[112:115], v[216:219], v[112:115], a[112:115]\n\t"
    "v_exp_f32_e32 v31, v31\n\t"
    "v_mfma_f32_16x16x32_bf16 a[120:123], v[216:219], v[120:123], a[120:123]\n\t"
    "ds_read_b64_tr_b16 v[166:167], v232 offset:37120\n\t"
    "ds_read_b64_tr_b16 v[168:169], v232 offset:33280\n\t"
    "v_mfma_f32_16x16x32_bf16 a[100:103], v[220:223], v[96:99], a[100:103]\n\t"
    "ds_read_b64_tr_b16 v[170:171], v232 offset:37376\n\t"
    "ds_read_b64_tr_b16 v[172:173], v232 offset:33536\n\t"
    "v_mfma_f32_16x16x32_bf16 a[108:111], v[220:223], v[104:107], a[108:111]\n\t"
    "v_cvt_pk_bf16_f32 v27, v30, v31\n\t"
    "v_mfma_f32_16x16x32_bf16 a[116:119], v[220:223], v[112:115], a[116:119]\n\t"
    "ds_read_b64_tr_b16 v[174:175], v232 offset:37632\n\t"
    "ds_read_b64_tr_b16 v[176:177], v232 offset:33792\n\t"
    "v_mfma_f32_16x16x32_bf16 a[124:127], v[220:223], v[120:123], a[124:127]\n\t"
    "ds_read_b64_tr_b16 v[178:179], v232 offset:37888\n\t"
    "s_add_u32 s84, s84, 0x4000\n\t"
    "v_mfma_f32_16x16x32_bf16 v[144:147], v[224:227], v[96:99], v[144:147]\n\t"
    "ds_read_b32 v252, v251 offset:59392\n\t"
    "v_mfma_f32_16x16x32_bf16 v[148:151], v[224:227], v[104:107], v[148:151]\n\t"
    "v_mfma_f32_16x16x32_bf16 v[152:155], v[224:227], v[112:115], v[152:155]\n\t"
    "v_mfma_f32_16x16x32_bf16 v[156:159], v[224:227], v[120:123], v[156:159]\n\t"
    "v_mfma_f32_16x16x32_bf16 v[64:67], a[192:195], a[128:131], v[128:131]\n\t"
    "v_exp_f32_e32 v32, v32\n\t"
    "v_mfma_f32_16x16x32_bf16 v[72:75], a[192:195], a[144:147], v[132:135]\n\t"
    "s_cmp_lt_u32 s85, 0x80000\n\t"
    "s_cselect_b32 m0, s95, s97\n\t"
    "v_mfma_f32_16x16x32_bf16 v[80:83], a[192:195], a[160:163], v[136:139]\n\t"
    "v_exp_f32_e32 v33, v33\n\t"
    "v_mfma_f32_16x16x32_bf16 v[88:91], a[192:195], a[176:179], v[140:143]\n\t"
    "ds_read_b64_tr_b16 v[180:181], v232 offset:34048\n\t"
    "ds_read_b64_tr_b16 v[182:183], v232 offset:38144\n\t"
    "v_min3_u32 v253, v240, v241, v242\n\t"
    "v_mfma_f32_16x16x32_bf16 v[64:67], a[196:199], a[132:135], v[64:67]\n\t"
    "v_exp_f32_e32 v34, v34\n\t"
    "v_mfma_f32_16x16x32_bf16 v[72:75], a[196:199], a[148:151], v[72:75]\n\t"
    "v_cvt_pk_bf16_f32 v32, v32, v33\n\t"
    "v_mfma_f32_16x16x32_bf16 v[80:83], a[196:199], a[164:167], v[80:83]\n\t"
    "v_exp_f32_e32 v35, v35\n\t"
    "v_mfma_f32_16x16x32_bf16 v[88:91], a[196:199], a[180:183], v[88:91]\n\t"
    "ds_read_b64_tr_b16 v[184:185], v232 offset:34304\n\t"
    "ds_read_b64_tr_b16 v[186:187], v232 offset:38400\n\t"
    "v_max3_u32 v254, v240, v241, v242\n\t"
    "v_mfma_f32_16x16x32_bf16 v[64:67], a[200:203], a[136:139], v[64:67]\n\t"
    "v_exp_f32_e32 v36, v36\n\t"
    "v_mfma_f32_16x16x32_bf16 v[72:75], a[200:203], a[152:155], v[72:75]\n\t"
    "v_cvt_pk_bf16_f32 v33, v34, v35\n\t"
    "v_mfma_f32_16x16x32_bf16 v[80:83], a[200:203], a[168:171], v[80:83]\n\t"
    "v_exp_f32_e32 v37, v37\n\t"
    "v_mfma_f32_16x16x32_bf16 v[88:91], a[200:203], a[184:187], v[88:91]\n\t"
    "ds_read_b64_tr_b16 v[188:189], v232 offset:34560\n\t"
    "ds_read_b64_tr_b16 v[190:191], v232 offset:38656\n\t"
    "v_min_u32_e32 v253, v253, v243\n\t"
    "v_mfma_f32_16x16x32_bf16 v[64:67], a[204:207], a[140:143], v[64:67]\n\t"
    "v_exp_f32_e32 v38, v38\n\t"
    "v_mfma_f32_16x16x32_bf16 v[72:75], a[204:207], a[156:159], v[72:75]\n\t"
    "v_cvt_pk_bf16_f32 v34, v36, v37\n\t"
    "v_mfma_f32_16x16x32_bf16 v[80:83], a[204:207], a[172:175], v[80:83]\n\t"
    "v_exp_f32_e32 v39, v39\n\t"
    "v_mfma_f32_16x16x32_bf16 v[88:91], a[204:207], a[188:191], v[88:91]\n\t"
    "ds_read_b64_tr_b16 v[192:193], v232 offset:40960\n\t"
    "ds_read_b64_tr_b16 v[194:195], v232 offset:45056\n\t"
    "v_max_u32_e32 v254, v254, v243\n\t"
    "v_mfma_f32_16x16x32_bf16 v[68:71], a[208:211], a[128:131], v[128:131]\n\t"
    "v_exp_f32_e32 v40, v40\n\t"
    "v_mfma_f32_16x16x32_bf16 v[76:79], a[208:211], a[144:147], v[132:135]\n\t"
    "v_cvt_pk_bf16_f32 v35, v38, v39\n\t"
    "v_mfma_f32_16x16x32_bf16 v[84:87], a[208:211], a[160:163], v[136:139]\n\t"
    "v_exp_f32_e32 v41, v41\n\t"
    "v_mfma_f32_16x16x32_bf16 v[92:95], a[208:211], a[176:179], v[140:143]\n\t"
    "ds_read_b64_tr_b16 v[196:197], v232 offset:41216\n\t"
    "ds_read_b64_tr_b16 v[198:199], v232 offset:45312\n\t"
    "v_cmp_ne_u32_e32 vcc, %[tag], v253\n\t"
    "v_mfma_f32_16x16x32_bf16 v[68:71], a[212:215], a[132:135], v[68:71]\n\t"
    "v_exp_f32_e32 v42, v42\n\t"
    "v_mfma_f32_16x16x32_bf16 v[76:79], a[212:215], a[148:151], v[76:79]\n\t"
    "v_cvt_pk_bf16_f32 v40, v40, v41\n\t"
    "v_mfma_f32_16x16x32_bf16 v[84:87], a[212:215], a[164:167], v[84:87]\n\t"
    "v_exp_f32_e32 v43, v43\n\t"
    "v_mfma_f32_16x16x32_bf16 v[92:95], a[212:215], a[180:183], v[92:95]\n\t"
    "ds_read_b64_tr_b16 v[200:201], v232 offset:41472\n\t"
    "ds_read_b64_tr_b16 v[202:203], v232 offset:45568\n\t"
    "v_cmp_ne_u32_e64 s[88:89], %[tag], v254\n\t"
    "v_mfma_f32_16x16x32_bf16 v[68:71], a[216:219], a[136:139], v[68:71]\n\t"
    "v_exp_f32_e32 v44, v44\n\t"
    "v_mfma_f32_16x16x32_bf16 v[76:79], a[216:219], a[152:155], v[76:79]\n\t"
    "v_cvt_pk_bf16_f32 v41, v42, v43\n\t"
    "v_mfma_f32_16x16x32_bf16 v[84:87], a[216:219], a[168:171], v[84:87]\n\t"
    "v_exp_f32_e32 v45, v45\n\t"
    "v_mfma_f32_16x16x32_bf16 v[92:95], a[216:219], a[184:187], v[92:95]\n\t"
    "ds_read_b64_tr_b16 v[204:205], v232 offset:41728\n\t"
    "ds_read_b64_tr_b16 v[206:207], v232 offset:45824\n\t"
    "s_or_b64 vcc, vcc, s[88:89]\n\t"
    "s_cselect_b32 s50, 0, 1\n\t"
    "v_mfma_f32_16x16x32_bf16 v[68:71], a[220:223], a[140:143], v[68:71]\n\t"
    "v_exp_f32_e32 v46, v46\n\t"
    "v_mfma_f32_16x16x32_bf16 v[76:79], a[220:223], a[156:159], v[76:79]\n\t"
    "v_cvt_pk_bf16_f32 v42, v44, v45\n\t"
    "v_mfma_f32_16x16x32_bf16 v[84:87], a[220:223], a[172:175], v[84:87]\n\t"
    "v_exp_f32_e32 v47, v47\n\t"
    "v_mfma_f32_16x16x32_bf16 v[92:95], a[220:223], a[188:191], v[92:95]\n\t"
    "ds_read_b64_tr_b16 v[208:209], v232 offset:41984\n\t"
    "ds_read_b64_tr_b16 v[210:211], v232 offset:46080\n\t"
    "s_cmp_le_u32 s86, 3\n\t"
    "s_cselect_b32 s47, s50, 0\n\t"
    "v_mfma_f32_16x16x32_bf16 v[96:99], a[224:227], a[128:131], v[128:131]\n\t"
    "v_exp_f32_e32 v48, v48\n\t"
    "v_mfma_f32_16x16x32_bf16 v[104:107], a[224:227], a[144:147], v[132:135]\n\t"
    "v_cvt_pk_bf16_f32 v43, v46, v47\n\t"
    "v_mfma_f32_16x16x32_bf16 v[112:115], a[224:227], a[160:163], v[136:139]\n\t"
    "v_exp_f32_e32 v49, v49\n\t"
    "v_mfma_f32_16x16x32_bf16 v[120:123], a[224:227], a[176:179], v[140:143]\n\t"
    "ds_read_b64_tr_b16 v[212:213], v232 offset:42240\n\t"
    "ds_read_b64_tr_b16 v[214:215], v232 offset:46336\n\t"
    "s_mul_i32 s66, s47, 32768\n\t"
    "s_mul_i32 s47, s47, 0\n\t"
    "v_mfma_f32_16x16x32_bf16 v[96:99], a[228:231], a[132:135], v[96:99]\n\t"
    "v_exp_f32_e32 v50, v50\n\t"
    "v_mfma_f32_16x16x32_bf16 v[104:107], a[228:231], a[148:151], v[104:107]\n\t"
    "v_cvt_pk_bf16_f32 v48, v48, v49\n\t"
    "v_mfma_f32_16x16x32_bf16 v[112:115], a[228:231], a[164:167], v[112:115]\n\t"
    "v_exp_f32_e32 v51, v51\n\t"
    "v_mfma_f32_16x16x32_bf16 v[120:123], a[228:231], a[180:183], v[120:123]\n\t"
    "ds_read_b64_tr_b16 v[216:217], v232 offset:42496\n\t"
    "ds_read_b64_tr_b16 v[218:219], v232 offset:46592\n\t"
    "v_mfma_f32_16x16x32_bf16 v[96:99], a[232:235], a[136:139], v[96:99]\n\t"
    "v_exp_f32_e32 v52, v52\n\t"
    "v_mfma_f32_16x16x32_bf16 v[104:107], a[232:235], a[152:155], v[104:107]\n\t"
    "v_cvt_pk_bf16_f32 v49, v50, v51\n\t"
    "v_mfma_f32_16x16x32_bf16 v[112:115], a[232:235], a[168:171], v[112:115]\n\t"
    "v_exp_f32_e32 v53, v53\n\t"
    "v_mfma_f32_16x16x32_bf16 v[120:123], a[232:235], a[184:187], v[120:123]\n\t"
    "ds_read_b64_tr_b16 v[220:221], v232 offset:42752\n\t"
    "ds_read_b64_tr_b16 v[222:223], v232 offset:46848\n\t"
    "v_mfma_f32_16x16x32_bf16 v[96:99], a[236:239], a[140:143], v[96:99]\n\t"
    "v_exp_f32_e32 v54, v54\n\t"
    "v_mfma_f32_16x16x32_bf16 v[104:107], a[236:239], a[156:159], v[104:107]\n\t"
    "v_cvt_pk_bf16_f32 v50, v52, v53\n\t"
    "v_mfma_f32_16x16x32_bf16 v[112:115], a[236:239], a[172:175], v[112:115]\n\t"
    "v_exp_f32_e32 v55, v55\n\t"
    "v_mfma_f32_16x16x32_bf16 v[120:123], a[236:239], a[188:191], v[120:123]\n\t"
    "buffer_load_dwordx4 v237, s[72:75], s85 offen offset:0 lds\n\t"
    "v_mfma_f32_16x16x32_bf16 v[100:103], a[240:243], a[128:131], v[128:131]\n\t"
    "v_exp_f32_e32 v56, v56\n\t"
    "v_mfma_f32_16x16x32_bf16 v[108:111], a[240:243], a[144:147], v[132:135]\n\t"
    "v_cvt_pk_bf16_f32 v51, v54, v55\n\t"
    "v_mfma_f32_16x16x32_bf16 v[116:119], a[240:243], a[160:163], v[136:139]\n\t"
    "v_exp_f32_e32 v57, v57\n\t"
    "v_mfma_f32_16x16x32_bf16 v[124:127], a[240:243], a[176:179], v[140:143]\n\t"
    "buffer_load_dwordx4 v238, s[72:75], s85 offen offset:1024 lds\n\t"
    "v_mfma_f32_16x16x32_bf16 v[100:103], a[244:247], a[132:135], v[100:103]\n\t"
    "v_exp_f32_e32 v58, v58\n\t"
    "v_mfma_f32_16x16x32_bf16 v[108:111], a[244:247], a[148:151], v[108:111]\n\t"
    "v_cvt_pk_bf16_f32 v56, v56, v57\n\t"
    "v_mfma_f32_16x16x32_bf16 v[116:119], a[244:247], a[164:167], v[116:119]\n\t"
    "v_exp_f32_e32 v59, v59\n\t"
    "v_mfma_f32_16x16x32_bf16 v[124:127], a[244:247], a[180:183], v[124:127]\n\t"
    "buffer_load_dwordx4 v237, s[72:75], s85 offen offset:2048 lds\n\t"
    "v_mfma_f32_16x16x32_bf16 v[100:103], a[248:251], a[136:139], v[100:103]\n\t"
    "v_exp_f32_e32 v60, v60\n\t"
    "v_mfma_f32_16x16x32_bf16 v[108:111], a[248:251], a[152:155], v[108:111]\n\t"
    "v_cvt_pk_bf16_f32 v57, v58, v59\n\t"
    "v_mfma_f32_16x16x32_bf16 v[116:119], a[248:251], a[168:171], v[116:119]\n\t"
    "v_exp_f32_e32 v61, v61\n\t"
    "v_mfma_f32_16x16x32_bf16 v[124:127], a[248:251], a[184:187], v[124:127]\n\t"
    "buffer_load_dwordx4 v238, s[72:75], s85 offen offset:3072 lds\n\t"
    "v_mfma_f32_16x16x32_bf16 v[100:103], a[252:255], a[140:143], v[100:103]\n\t"
    "v_exp_f32_e32 v62, v62\n\t"
    "v_mfma_f32_16x16x32_bf16 v[108:111], a[252:255], a[156:159], v[108:111]\n\t"
    "v_cvt_pk_bf16_f32 v58, v60, v61\n\t"
    "v_mfma_f32_16x16x32_bf16 v[116:119], a[252:255], a[172:175], v[116:119]\n\t"
    "v_exp_f32_e32 v63, v63\n\t"
    "v_mfma_f32_16x16x32_bf16 v[124:127], a[252:255], a[188:191], v[124:127]\n\t"
    "s_waitcnt vmcnt(8) lgkmcnt(0)\n\t"
    "s_barrier\n\t"
    "v_mfma_f32_16x16x32_bf16 a[0:3], v[160:163], v[0:3], a[0:3]\n\t"
    "v_exp_f32_e32 v64, v64\n\t"
    "v_mfma_f32_16x16x32_bf16 a[8:11], v[160:163], v[8:11], a[8:11]\n\t"
    "v_cvt_pk_bf16_f32 v59, v62, v63\n\t"
    "v_mfma_f32_16x16x32_bf16 a[16:19], v[160:163], v[16:19], a[16:19]\n\t"
    "v_exp_f32_e32 v65, v65\n\t"
    "v_mfma_f32_16x16x32_bf16 a[24:27], v[160:163], v[24:27], a[24:27]\n\t"
    "ds_read_b128 a[192:195], v228 offset:32768\n\t"
    "ds_read_b128 a[196:199], v229 offset:32768\n\t"
    "v_mfma_f32_16x16x32_bf16 a[4:7], v[164:167], v[0:3], a[4:7]\n\t"
    "v_exp_f32_e32 v66, v66\n\t"
    "v_mfma_f32_16x16x32_bf16 a[12:15], v[164:167], v[8:11], a[12:15]\n\t"
    "v_cvt_pk_bf16_f32 v64, v64, v65\n\t"
    "v_mfma_f32_16x16x32_bf16 a[20:23], v[164:167], v[16:19], a[20:23]\n\t"
    "v_exp_f32_e32 v67, v67\n\t"
    "v_mfma_f32_16x16x32_bf16 a[28:31], v[164:167], v[24:27], a[28:31]\n\t"
    "ds_read_b128 a[200:203], v230 offset:32768\n\t"
    "ds_read_b128 a[204:207], v231 offset:32768\n\t"
    "v_mfma_f32_16x16x32_bf16 a[32:35], v[168:171], v[0:3], a[32:35]\n\t"
    "v_exp_f32_e32 v68, v68\n\t"
    "v_mfma_f32_16x16x32_bf16 a[40:43], v[168:171], v[8:11], a[40:43]\n\t"
    "v_cvt_pk_bf16_f32 v65, v66, v67\n\t"
    "v_mfma_f32_16x16x32_bf16 a[48:51], v[168:171], v[16:19], a[48:51]\n\t"
    "v_exp_f32_e32 v69, v69\n\t"
    "v_mfma_f32_16x16x32_bf16 a[56:59], v[168:171], v[24:27], a[56:59]\n\t"
    "ds_read_b128 a[208:211], v228 offset:36864\n\t"
    "ds_read_b128 a[212:215], v229 offset:36864\n\t"
    "v_mfma_f32_16x16x32_bf16 a[36:39], v[172:175], v[0:3], a[36:39]\n\t"
    "v_exp_f32_e32 v70, v70\n\t"
    "v_mfma_f32_16x16x32_bf16 a[44:47], v[172:175], v[8:11], a[44:47]\n\t"
    "v_cvt_pk_bf16_f32 v66, v68, v69\n\t"
    "v_mfma_f32_16x16x32_bf16 a[52:55], v[172:175], v[16:19], a[52:55]\n\t"
    "v_exp_f32_e32 v71, v71\n\t"
    "v_mfma_f32_16x16x32_bf16 a[60:63], v[172:175], v[24:27], a[60:63]\n\t"
    "ds_read_b128 a[216:219], v230 offset:36864\n\t"
    "ds_read_b128 a[220:223], v231 offset:36864\n\t"
    "v_mfma_f32_16x16x32_bf16 a[64:67], v[176:179], v[0:3], a[64:67]\n\t"
    "v_exp_f32_e32 v72, v72\n\t"
    "v_mfma_f32_16x16x32_bf16 a[72:75], v[176:179], v[8:11], a[72:75]\n\t"
    "v_cvt_pk_bf16_f32 v67, v70, v71\n\t"
    "v_mfma_f32_16x16x32_bf16 a[80:83], v[176:179], v[16:19], a[80:83]\n\t"
    "v_exp_f32_e32 v73, v73\n\t"
    "v_mfma_f32_16x16x32_bf16 a[88:91], v[176:179], v[24:27], a[88:91]\n\t"
    "ds_read_b128 a[224:227], v228 offset:40960\n\t"
    "ds_read_b128 a[228:231], v229 offset:40960\n\t"
    "v_mfma_f32_16x16x32_bf16 a[68:71], v[180:183], v[0:3], a[68:71]\n\t"
    "v_exp_f32_e32 v74, v74\n\t"
    "v_mfma_f32_16x16x32_bf16 a[76:79], v[180:183], v[8:11], a[76:79]\n\t"
    "v_cvt_pk_bf16_f32 v72, v72, v73\n\t"
    "v_mfma_f32_16x16x32_bf16 a[84:87], v[180:183], v[16:19], a[84:87]\n\t"
    "v_exp_f32_e32 v75, v75\n\t"
    "v_mfma_f32_16x16x32_bf16 a[92:95], v[180:183], v[24:27], a[92:95]\n\t"
    "ds_read_b128 a[232:235], v230 offset:40960\n\t"
    "ds_read_b128 a[236:239], v231 offset:40960\n\t"
    "v_mfma_f32_16x16x32_bf16 a[96:99], v[184:187], v[0:3], a[96:99]\n\t"
    "v_exp_f32_e32 v76, v76\n\t"
    "v_mfma_f32_16x16x32_bf16 a[104:107], v[184:187], v[8:11], a[104:107]\n\t"
    "v_cvt_pk_bf16_f32 v73, v74, v75\n\t"
    "v_mfma_f32_16x16x32_bf16 a[112:115], v[184:187], v[16:19], a[112:115]\n\t"
    "v_exp_f32_e32 v77, v77\n\t"
    "v_mfma_f32_16x16x32_bf16 a[120:123], v[184:187], v[24:27], a[120:123]\n\t"
    "ds_read_b128 a[240:243], v228 offset:45056\n\t"
    "ds_read_b128 a[244:247], v229 offset:45056\n\t"
    "v_mfma_f32_16x16x32_bf16 a[100:103], v[188:191], v[0:3], a[100:103]\n\t"
    "v_exp_f32_e32 v78, v78\n\t"
    "v_mfma_f32_16x16x32_bf16 a[108:111], v[188:191], v[8:11], a[108:111]\n\t"
    "v_cvt_pk_bf16_f32 v74, v76, v77\n\t"
    "v_mfma_f32_16x16x32_bf16 a[116:119], v[188:191], v[16:19], a[116:119]\n\t"
    "v_exp_f32_e32 v79, v79\n\t"
    "v_mfma_f32_16x16x32_bf16 a[124:127], v[188:191], v[24:27], a[124:127]\n\t"
    "ds_read_b128 a[248:251], v230 offset:45056\n\t"
    "ds_read_b128 a[252:255], v231 offset:45056\n\t"
    "v_mfma_f32_16x16x32_bf16 v[144:147], v[224:227], v[0:3], v[144:147]\n\t"
    "v_exp_f32_e32 v80, v80\n\t"
    "v_mfma_f32_16x16x32_bf16 v[148:151], v[224:227], v[8:11], v[148:151]\n\t"
    "v_cvt_pk_bf16_f32 v75, v78, v79\n\t"
    "v_mfma_f32_16x16x32_bf16 v[152:155], v[224:227], v[16:19], v[152:155]\n\t"
    "v_exp_f32_e32 v81, v81\n\t"
    "v_mfma_f32_16x16x32_bf16 v[156:159], v[224:227], v[24:27], v[156:159]\n\t"
    "s_cmp_lt_u32 s84, 0x80000\n\t"
    "s_cselect_b32 m0, s91, s97\n\t"
    "v_mfma_f32_16x16x32_bf16 a[0:3], v[192:195], v[32:35], a[0:3]\n\t"
    "v_exp_f32_e32 v82, v82\n\t"
    "v_mfma_f32_16x16x32_bf16 a[8:11], v[192:195], v[40:43], a[8:11]\n\t"
    "v_cvt_pk_bf16_f32 v80, v80, v81\n\t"
    "v_mfma_f32_16x16x32_bf16 a[16:19], v[192:195], v[48:51], a[16:19]\n\t"
    "v_exp_f32_e32 v83, v83\n\t"
    "v_mfma_f32_16x16x32_bf16 a[24:27], v[192:195], v[56:59], a[24:27]\n\t"
    "buffer_load_dwordx4 v233, s[68:71], s84 offen offset:0 lds\n\t"
    "s_add_u32 s85, s85, 0x4000\n\t"
    "v_mfma_f32_16x16x32_bf16 a[4:7], v[196:199], v[32:35], a[4:7]\n\t"
    "v_exp_f32_e32 v84, v84\n\t"
    "v_mfma_f32_16x16x32_bf16 a[12:15], v[196:199], v[40:43], a[12:15]\n\t"
    "v_cvt_pk_bf16_f32 v81, v82, v83\n\t"
    "v_mfma_f32_16x16x32_bf16 a[20:23], v[196:199], v[48:51], a[20:23]\n\t"
    "v_exp_f32_e32 v85, v85\n\t"
    "v_mfma_f32_16x16x32_bf16 a[28:31], v[196:199], v[56:59], a[28:31]\n\t"
    "buffer_load_dwordx4 v234, s[68:71], s84 offen offset:1024 lds\n\t"
    "v_mfma_f32_16x16x32_bf16 a[32:35], v[200:203], v[32:35], a[32:35]\n\t"
    "v_exp_f32_e32 v86, v86\n\t"
    "v_mfma_f32_16x16x32_bf16 a[40:43], v[200:203], v[40:43], a[40:43]\n\t"
    "v_cvt_pk_bf16_f32 v82, v84, v85\n\t"
    "v_mfma_f32_16x16x32_bf16 a[48:51], v[200:203], v[48:51], a[48:51]\n\t"
    "v_exp_f32_e32 v87, v87\n\t"
    "v_mfma_f32_16x16x32_bf16 a[56:59], v[200:203], v[56:59], a[56:59]\n\t"
    "buffer_load_dwordx4 v235, s[68:71], s84 offen offset:2048 lds\n\t"
    "v_mfma_f32_16x16x32_bf16 a[36:39], v[204:207], v[32:35], a[36:39]\n\t"
    "v_exp_f32_e32 v88, v88\n\t"
    "v_mfma_f32_16x16x32_bf16 a[44:47], v[204:207], v[40:43], a[44:47]\n\t"
    "v_cvt_pk_bf16_f32 v83, v86, v87\n\t"
    "v_mfma_f32_16x16x32_bf16 a[52:55], v[204:207], v[48:51], a[52:55]\n\t"
    "v_exp_f32_e32 v89, v89\n\t"
    "v_mfma_f32_16x16x32_bf16 a[60:63], v[204:207], v[56:59], a[60:63]\n\t"
    "buffer_load_dwordx4 v236, s[68:71], s84 offen offset:3072 lds\n\t"
    "v_mfma_f32_16x16x32_bf16 a[64:67], v[208:211], v[32:35], a[64:67]\n\t"
    "v_exp_f32_e32 v90, v90\n\t"
    "v_mfma_f32_16x16x32_bf16 a[72:75], v[208:211], v[40:43], a[72:75]\n\t"
    "v_cvt_pk_bf16_f32 v88, v88, v89\n\t"
    "v_mfma_f32_16x16x32_bf16 a[80:83], v[208:211], v[48:51], a[80:83]\n\t"
    "v_exp_f32_e32 v91, v91\n\t"
    "v_mfma_f32_16x16x32_bf16 a[88:91], v[208:211], v[56:59], a[88:91]\n\t"
    "s_waitcnt lgkmcnt(0)\n\t"
    "ds_read_b64_tr_b16 v[160:161], v232 offset:16384\n\t"
    "v_mfma_f32_16x16x32_bf16 a[68:71], v[212:215], v[32:35], a[68:71]\n\t"
    "v_exp_f32_e32 v92, v92\n\t"
    "v_mfma_f32_16x16x32_bf16 a[76:79], v[212:215], v[40:43], a[76:79]\n\t"
    "v_cvt_pk_bf16_f32 v89, v90, v91\n\t"
    "v_mfma_f32_16x16x32_bf16 a[84:87], v[212:215], v[48:51], a[84:87]\n\t"
    "v_exp_f32_e32 v93, v93\n\t"
    "v_mfma_f32_16x16x32_bf16 a[92:95], v[212:215], v[56:59], a[92:95]\n\t"
    "ds_read_b64_tr_b16 v[162:163], v232 offset:20480\n\t"
    "ds_read_b64_tr_b16 v[164:165], v232 offset:16640\n\t"
    "v_mfma_f32_16x16x32_bf16 a[96:99], v[216:219], v[32:35], a[96:99]\n\t"
    "v_exp_f32_e32 v94, v94\n\t"
    "v_mfma_f32_16x16x32_bf16 a[104:107], v[216:219], v[40:43], a[104:107]\n\t"
    "v_cvt_pk_bf16_f32 v90, v92, v93\n\t"
    "v_mfma_f32_16x16x32_bf16 a[112:115], v[216:219], v[48:51], a[112:115]\n\t"
    "v_exp_f32_e32 v95, v95\n\t"
    "v_mfma_f32_16x16x32_bf16 a[120:123], v[216:219], v[56:59], a[120:123]\n\t"
    "ds_read_b64_tr_b16 v[166:167], v232 offset:20736\n\t"
    "ds_read_b64_tr_b16 v[168:169], v232 offset:16896\n\t"
    "v_mfma_f32_16x16x32_bf16 a[100:103], v[220:223], v[32:35], a[100:103]\n\t"
    "ds_read_b64_tr_b16 v[170:171], v232 offset:20992\n\t"
    "ds_read_b64_tr_b16 v[172:173], v232 offset:17152\n\t"
    "v_max_u32_dpp v252, v252, v252 quad_perm:[1,0,3,2] row_mask:0xf bank_mask:0xf\n\t"
    "v_mfma_f32_16x16x32_bf16 a[108:111], v[220:223], v[40:43], a[108:111]\n\t"
    "v_cvt_pk_bf16_f32 v91, v94, v95\n\t"
    "v_mfma_f32_16x16x32_bf16 a[116:119], v[220:223], v[48:51], a[116:119]\n\t"
    "ds_read_b64_tr_b16 v[174:175], v232 offset:21248\n\t"
    "ds_read_b64_tr_b16 v[176:177], v232 offset:17408\n\t"
    "s_nop 0\n\t"
    "v_max_u32_dpp v252, v252, v252 quad_perm:[2,3,0,1] row_mask:0xf bank_mask:0xf\n\t"
    "v_mfma_f32_16x16x32_bf16 a[124:127], v[220:223], v[56:59], a[124:127]\n\t"
    "ds_read_b64_tr_b16 v[178:179], v232 offset:21504\n\t"
    "s_add_u32 s84, s84, 0x4000\n\t"
    "v_mfma_f32_16x16x32_bf16 v[144:147], v[224:227], v[32:35], v[144:147]\n\t"
    "v_readfirstlane_b32 s51, v252\n\t"
    "v_mfma_f32_16x16x32_bf16 v[148:151], v[224:227], v[40:43], v[148:151]\n\t"
    "s_andn2_b32 s33, 0x7ff, s51\n\t"
    "s_lshl_b32 s34, s33, 10\n\t"
    "s_add_u32 m0, s99, 54272\n\t"
    "s_lshl_b32 s33, s33, 3\n\t"
    "v_mfma_f32_16x16x32_bf16 v[152:155], v[224:227], v[48:51], v[152:155]\n\t"
    "buffer_load_dwordx4 v250, s[80:83], s34 offen lds\n\t"
    "s_add_u32 s33, s33, 4096\n\t"
    "s_lshr_b32 s34, s99, 2\n\t"
    "s_add_u32 m0, s34, 58368\n\t"
    "v_mfma_f32_16x16x32_bf16 v[156:159], v[224:227], v[56:59], v[156:159]\n\t"
    "buffer_load_dword v251, s[60:63], s33 offen sc1 lds\n\t"
    "s_sub_u32 s86, s86, 1\n\t"
    "s_cmp_lg_u32 s86, 0\n\t"
    "s_cbranch_scc1 L_loop_%=\n\t"
    "ds_read_b64_tr_b16 v[180:181], v232 offset:17664\n\t"
    "ds_read_b64_tr_b16 v[182:183], v232 offset:21760\n\t"
    "ds_read_b64_tr_b16 v[184:185], v232 offset:17920\n\t"
    "ds_read_b64_tr_b16 v[186:187], v232 offset:22016\n\t"
    "ds_read_b64_tr_b16 v[188:189], v232 offset:18176\n\t"
    "ds_read_b64_tr_b16 v[190:191], v232 offset:22272\n\t"
    "s_cmp_lg_u32 s50, 0\n\t"
    "s_cbranch_scc1 L_gw_%=\n\t"
    "s_mov_b32 s86, 200000\n\t"
    "L_pw_%=:\n\t"
    "buffer_load_dwordx4 v[240:243], v250, s[60:63], 0 offen offset:1024 sc1\n\t"
    "s_waitcnt vmcnt(0)\n\t"
    "v_cmp_ne_u32_e32 vcc, %[tag], v240\n\t"
    "v_cmp_ne_u32_e64 s[88:89], %[tag], v241\n\t"
    "s_or_b64 vcc, vcc, s[88:89]\n\t"
    "v_cmp_ne_u32_e64 s[88:89], %[tag], v242\n\t"
    "s_or_b64 vcc, vcc, s[88:89]\n\t"
    "v_cmp_ne_u32_e64 s[88:89], %[tag], v243\n\t"
    "s_or_b64 vcc, vcc, s[88:89]\n\t"
    "s_cbranch_vccz L_gw_%=\n\t"
    "s_sub_u32 s86, s86, 1\n\t"
    "s_cbranch_scc0 L_pw_%=\n\t"
    "L_gw_%=:\n\t"
    "s_mul_i32 s33, %[b], 524288\n\t"
    "s_mul_i32 s34, %[wid], 131072\n\t"
    "s_add_u32 s33, s33, s34\n\t"
    "s_add_u32 s33, s33, 18874368\n\t"
    "s_add_u32 s64, s58, s33\n\t"
    "s_addc_u32 s65, s59, 0\n\t"
    "s_and_b32 s65, s65, 0xffff\n\t"
    "s_mov_b32 s66, 131072\n\t"
    "s_mov_b32 s67, 0x20000\n\t"
    "s_waitcnt lgkmcnt(8)\n\t"
    "v_mfma_f32_16x16x32_bf16 a[0:3], v[160:163], v[64:67], a[0:3]\n\t"
    "v_exp_f32_e32 v96, v96\n\t"
    "ds_read_b64_tr_b16 v[192:193], v232 offset:24576\n\t"
    "v_mfma_f32_16x16x32_bf16 a[8:11], v[160:163], v[72:75], a[8:11]\n\t"
    "v_exp_f32_e32 v97, v97\n\t"
    "ds_read_b64_tr_b16 v[194:195], v232 offset:28672\n\t"
    "v_mfma_f32_16x16x32_bf16 a[16:19], v[160:163], v[80:83], a[16:19]\n\t"
    "v_exp_f32_e32 v98, v98\n\t"
    "ds_read_b64_tr_b16 v[196:197], v232 offset:24832\n\t"
    "s_mov_b32 s87, 0\n\t"
    "v_mfma_f32_16x16x32_bf16 a[24:27], v[160:163], v[88:91], a[24:27]\n\t"
    "v_cvt_pk_bf16_f32 v96, v96, v97\n\t"
    "v_exp_f32_e32 v99, v99\n\t"
    "ds_read_b64_tr_b16 v[198:199], v232 offset:28928\n\t"
    "v_mfma_f32_16x16x32_bf16 a[4:7], v[164:167], v[64:67], a[4:7]\n\t"
    "v_exp_f32_e32 v100, v100\n\t"
    "ds_read_b64_tr_b16 v[200:201], v232 offset:25088\n\t"
    "v_mfma_f32_16x16x32_bf16 a[12:15], v[164:167], v[72:75], a[12:15]\n\t"
    "v_cvt_pk_bf16_f32 v97, v98, v99\n\t"
    "v_exp_f32_e32 v101, v101\n\t"
    "ds_read_b64_tr_b16 v[202:203], v232 offset:29184\n\t"
    "buffer_load_dwordx4 a[128:131], v250, s[64:67], s87 offen offset:0 sc1\n\t"
    "v_mfma_f32_16x16x32_bf16 a[20:23], v[164:167], v[80:83], a[20:23]\n\t"
    "v_exp_f32_e32 v102, v102\n\t"
    "ds_read_b64_tr_b16 v[204:205], v232 offset:25344\n\t"
    "v_mfma_f32_16x16x32_bf16 a[28:31], v[164:167], v[88:91], a[28:31]\n\t"
    "v_cvt_pk_bf16_f32 v98, v100, v101\n\t"
    "v_exp_f32_e32 v103, v103\n\t"
    "ds_read_b64_tr_b16 v[206:207], v232 offset:29440\n\t"
    "v_mfma_f32_16x16x32_bf16 a[32:35], v[168:171], v[64:67], a[32:35]\n\t"
    "v_exp_f32_e32 v104, v104\n\t"
    "ds_read_b64_tr_b16 v[208:209], v232 offset:25600\n\t"
    "buffer_load_dwordx4 a[132:135], v250, s[64:67], s87 offen offset:1024 sc1\n\t"
    "v_mfma_f32_16x16x32_bf16 a[40:43], v[168:171], v[72:75], a[40:43]\n\t"
    "v_cvt_pk_bf16_f32 v99, v102, v103\n\t"
    "v_exp_f32_e32 v105, v105\n\t"
    "ds_read_b64_tr_b16 v[210:211], v232 offset:29696\n\t"
    "v_mfma_f32_16x16x32_bf16 a[48:51], v[168:171], v[80:83], a[48:51]\n\t"
    "v_exp_f32_e32 v106, v106\n\t"
    "ds_read_b64_tr_b16 v[212:213], v232 offset:25856\n\t"
    "v_mfma_f32_16x16x32_bf16 a[56:59], v[168:171], v[88:91], a[56:59]\n\t"
    "v_cvt_pk_bf16_f32 v104, v104, v105\n\t"
    "v_exp_f32_e32 v107, v107\n\t"
    "ds_read_b64_tr_b16 v[214:215], v232 offset:29952\n\t"
    "v_mfma_f32_16x16x32_bf16 a[36:39], v[172:175], v[64:67], a[36:39]\n\t"
    "v_exp_f32_e32 v108, v108\n\t"
    "ds_read_b64_tr_b16 v[216:217], v232 offset:26112\n\t"
    "buffer_load_dwordx4 a[136:139], v250, s[64:67], s87 offen offset:2048 sc1\n\t"
    "v_mfma_f32_16x16x32_bf16 a[44:47], v[172:175], v[72:75], a[44:47]\n\t"
    "v_cvt_pk_bf16_f32 v105, v106, v107\n\t"
    "v_exp_f32_e32 v109, v109\n\t"
    "ds_read_b64_tr_b16 v[218:219], v232 offset:30208\n\t"
    "v_mfma_f32_16x16x32_bf16 a[52:55], v[172:175], v[80:83], a[52:55]\n\t"
    "v_exp_f32_e32 v110, v110\n\t"
    "ds_read_b64_tr_b16 v[220:221], v232 offset:26368\n\t"
    "v_mfma_f32_16x16x32_bf16 a[60:63], v[172:175], v[88:91], a[60:63]\n\t"
    "v_cvt_pk_bf16_f32 v106, v108, v109\n\t"
    "v_exp_f32_e32 v111, v111\n\t"
    "ds_read_b64_tr_b16 v[222:223], v232 offset:30464\n\t"
    "s_waitcnt lgkmcnt(15)\n\t"
    "buffer_load_dwordx4 a[140:143], v250, s[64:67], s87 offen offset:3072 sc1\n\t"
    "v_mfma_f32_16x16x32_bf16 a[64:67], v[176:179], v[64:67], a[64:67]\n\t"
    "v_exp_f32_e32 v112, v112\n\t"
    "v_mfma_f32_16x16x32_bf16 a[72:75], v[176:179], v[72:75], a[72:75]\n\t"
    "v_cvt_pk_bf16_f32 v107, v110, v111\n\t"
    "v_exp_f32_e32 v113, v113\n\t"
    "v_mfma_f32_16x16x32_bf16 a[80:83], v[176:179], v[80:83], a[80:83]\n\t"
    "v_exp_f32_e32 v114, v114\n\t"
    "v_mfma_f32_16x16x32_bf16 a[88:91], v[176:179], v[88:91], a[88:91]\n\t"
    "v_cvt_pk_bf16_f32 v112, v112, v113\n\t"
    "v_exp_f32_e32 v115, v115\n\t"
    "s_mov_b32 s88, 4096\n\t"
    "v_mfma_f32_16x16x32_bf16 a[68:71], v[180:183], v[64:67], a[68:71]\n\t"
    "v_exp_f32_e32 v116, v116\n\t"
    "v_mfma_f32_16x16x32_bf16 a[76:79], v[180:183], v[72:75], a[76:79]\n\t"
    "v_cvt_pk_bf16_f32 v113, v114, v115\n\t"
    "v_exp_f32_e32 v117, v117\n\t"
    "v_mfma_f32_16x16x32_bf16 a[84:87], v[180:183], v[80:83], a[84:87]\n\t"
    "v_exp_f32_e32 v118, v118\n\t"
    "buffer_load_dwordx4 a[144:147], v250, s[64:67], s88 offen offset:0 sc1\n\t"
    "v_mfma_f32_16x16x32_bf16 a[92:95], v[180:183], v[88:91], a[92:95]\n\t"
    "v_cvt_pk_bf16_f32 v114, v116, v117\n\t"
    "v_exp_f32_e32 v119, v119\n\t"
    "v_mfma_f32_16x16x32_bf16 a[96:99], v[184:187], v[64:67], a[96:99]\n\t"
    "v_exp_f32_e32 v120, v120\n\t"
    "v_mfma_f32_16x16x32_bf16 a[104:107], v[184:187], v[72:75], a[104:107]\n\t"
    "v_cvt_pk_bf16_f32 v115, v118, v119\n\t"
    "v_exp_f32_e32 v121, v121\n\t"
    "v_mfma_f32_16x16x32_bf16 a[112:115], v[184:187], v[80:83], a[112:115]\n\t"
    "v_exp_f32_e32 v122, v122\n\t"
    "buffer_load_dwordx4 a[148:151], v250, s[64:67], s88 offen offset:1024 sc1\n\t"
    "v_mfma_f32_16x16x32_bf16 a[120:123], v[184:187], v[88:91], a[120:123]\n\t"
    "v_cvt_pk_bf16_f32 v120, v120, v121\n\t"
    "v_exp_f32_e32 v123, v123\n\t"
    "v_mfma_f32_16x16x32_bf16 a[100:103], v[188:191], v[64:67], a[100:103]\n\t"
    "v_exp_f32_e32 v124, v124\n\t"
    "v_mfma_f32_16x16x32_bf16 a[108:111], v[188:191], v[72:75], a[108:111]\n\t"
    "v_cvt_pk_bf16_f32 v121, v122, v123\n\t"
    "v_exp_f32_e32 v125, v125\n\t"
    "buffer_load_dwordx4 a[152:155], v250, s[64:67], s88 offen offset:2048 sc1\n\t"
    "v_mfma_f32_16x16x32_bf16 a[116:119], v[188:191], v[80:83], a[116:119]\n\t"
    "v_exp_f32_e32 v126, v126\n\t"
    "v_mfma_f32_16x16x32_bf16 a[124:127], v[188:191], v[88:91], a[124:127]\n\t"
    "v_cvt_pk_bf16_f32 v122, v124, v125\n\t"
    "v_exp_f32_e32 v127, v127\n\t"
    "v_mfma_f32_16x16x32_bf16 v[144:147], v[224:227], v[64:67], v[144:147]\n\t"
    "v_mfma_f32_16x16x32_bf16 v[148:151], v[224:227], v[72:75], v[148:151]\n\t"
    "v_cvt_pk_bf16_f32 v123, v126, v127\n\t"
    "buffer_load_dwordx4 a[156:159], v250, s[64:67], s88 offen offset:3072 sc1\n\t"
    "v_mfma_f32_16x16x32_bf16 v[152:155], v[224:227], v[80:83], v[152:155]\n\t"
    "v_mfma_f32_16x16x32_bf16 v[156:159], v[224:227], v[88:91], v[156:159]\n\t"
    "s_waitcnt lgkmcnt(0)\n\t"
    "v_mfma_f32_16x16x32_bf16 a[0:3], v[192:195], v[96:99], a[0:3]\n\t"
    "s_mov_b32 s87, 8192\n\t"
    "v_mfma_f32_16x16x32_bf16 a[8:11], v[192:195], v[104:107], a[8:11]\n\t"
    "v_mfma_f32_16x16x32_bf16 a[16:19], v[192:195], v[112:115], a[16:19]\n\t"
    "v_mfma_f32_16x16x32_bf16 a[24:27], v[192:195], v[120:123], a[24:27]\n\t"
    "buffer_load_dwordx4 a[160:163], v250, s[64:67], s87 offen offset:0 sc1\n\t"
    "v_mfma_f32_16x16x32_bf16 a[4:7], v[196:199], v[96:99], a[4:7]\n\t"
    "v_mfma_f32_16x16x32_bf16 a[12:15], v[196:199], v[104:107], a[12:15]\n\t"
    "v_mfma_f32_16x16x32_bf16 a[20:23], v[196:199], v[112:115], a[20:23]\n\t"
    "v_mfma_f32_16x16x32_bf16 a[28:31], v[196:199], v[120:123], a[28:31]\n\t"
    "buffer_load_dwordx4 a[164:167], v250, s[64:67], s87 offen offset:1024 sc1\n\t"
    "v_mfma_f32_16x16x32_bf16 a[32:35], v[200:203], v[96:99], a[32:35]\n\t"
    "v_mfma_f32_16x16x32_bf16 a[40:43], v[200:203], v[104:107], a[40:43]\n\t"
    "v_mfma_f32_16x16x32_bf16 a[48:51], v[200:203], v[112:115], a[48:51]\n\t"
    "buffer_load_dwordx4 a[168:171], v250, s[64:67], s87 offen offset:2048 sc1\n\t"
    "v_mfma_f32_16x16x32_bf16 a[56:59], v[200:203], v[120:123], a[56:59]\n\t"
    "v_mfma_f32_16x16x32_bf16 a[36:39], v[204:207], v[96:99], a[36:39]\n\t"
    "v_mfma_f32_16x16x32_bf16 a[44:47], v[204:207], v[104:107], a[44:47]\n\t"
    "v_mfma_f32_16x16x32_bf16 a[52:55], v[204:207], v[112:115], a[52:55]\n\t"
    "buffer_load_dwordx4 a[172:175], v250, s[64:67], s87 offen offset:3072 sc1\n\t"
    "v_mfma_f32_16x16x32_bf16 a[60:63], v[204:207], v[120:123], a[60:63]\n\t"
    "v_mfma_f32_16x16x32_bf16 a[64:67], v[208:211], v[96:99], a[64:67]\n\t"
    "v_mfma_f32_16x16x32_bf16 a[72:75], v[208:211], v[104:107], a[72:75]\n\t"
    "s_mov_b32 s88, 12288\n\t"
    "v_mfma_f32_16x16x32_bf16 a[80:83], v[208:211], v[112:115], a[80:83]\n\t"
    "v_mfma_f32_16x16x32_bf16 a[88:91], v[208:211], v[120:123], a[88:91]\n\t"
    "v_mfma_f32_16x16x32_bf16 a[68:71], v[212:215], v[96:99], a[68:71]\n\t"
    "v_mfma_f32_16x16x32_bf16 a[76:79], v[212:215], v[104:107], a[76:79]\n\t"
    "buffer_load_dwordx4 a[176:179], v250, s[64:67], s88 offen offset:0 sc1\n\t"
    "v_mfma_f32_16x16x32_bf16 a[84:87], v[212:215], v[112:115], a[84:87]\n\t"
    "v_mfma_f32_16x16x32_bf16 a[92:95], v[212:215], v[120:123], a[92:95]\n\t"
    "v_mfma_f32_16x16x32_bf16 a[96:99], v[216:219], v[96:99], a[96:99]\n\t"
    "buffer_load_dwordx4 a[180:183], v250, s[64:67], s88 offen offset:1024 sc1\n\t"
    "v_mfma_f32_16x16x32_bf16 a[104:107], v[216:219], v[104:107], a[104:107]\n\t"
    "v_mfma_f32_16x16x32_bf16 a[112:115], v[216:219], v[112:115], a[112:115]\n\t"
    "v_mfma_f32_16x16x32_bf16 a[120:123], v[216:219], v[120:123], a[120:123]\n\t"
    "v_mfma_f32_16x16x32_bf16 a[100:103], v[220:223], v[96:99], a[100:103]\n\t"
    "buffer_load_dwordx4 a[184:187], v250, s[64:67], s88 offen offset:2048 sc1\n\t"
    "v_mfma_f32_16x16x32_bf16 a[108:111], v[220:223], v[104:107], a[108:111]\n\t"
    "v_mfma_f32_16x16x32_bf16 a[116:119], v[220:223], v[112:115], a[116:119]\n\t"
    "v_mfma_f32_16x16x32_bf16 a[124:127], v[220:223], v[120:123], a[124:127]\n\t"
    "buffer_load_dwordx4 a[188:191], v250, s[64:67], s88 offen offset:3072 sc1\n\t"
    "v_mfma_f32_16x16x32_bf16 v[144:147], v[224:227], v[96:99], v[144:147]\n\t"
    "v_mfma_f32_16x16x32_bf16 v[148:151], v[224:227], v[104:107], v[148:151]\n\t"
    "v_mfma_f32_16x16x32_bf16 v[152:155], v[224:227], v[112:115], v[152:155]\n\t"
    "v_mfma_f32_16x16x32_bf16 v[156:159], v[224:227], v[120:123], v[156:159]\n\t"
    "s_mul_i32 s33, %[b], 2097152\n\t"
    "s_mul_i32 s34, %[wid], 524288\n\t"
    "s_add_u32 s33, s33, s34\n\t"
    "s_mul_i32 s34, %[h], 4096\n\t"
    "s_add_u32 s33, s33, s34\n\t"
    "s_add_u32 s33, s33, 2097152\n\t"
    "s_add_u32 s76, s58, s33\n\t"
    "s_addc_u32 s77, s59, 0\n\t"
    "s_and_b32 s77, s77, 0xffff\n\t"
    "s_mov_b32 s78, 397312\n\t"
    "s_mov_b32 s79, 0x20000\n\t"
    "s_mov_b32 s84, 131072\n\t"
    "s_mov_b32 s85, 262144\n\t"
    "s_mov_b32 s86, 393216\n\t"
    "s_nop 3\n\t"
    "v_mov_b32_e32 v145, v144\n\t"
    "v_mov_b32_e32 v149, v148\n\t"
    "v_mov_b32_e32 v153, v152\n\t"
    "v_mov_b32_e32 v157, v156\n\t"
    "s_nop 1\n\t"
    "v_permlane16_swap_b32_e32 v144, v145\n\t"
    "v_permlane16_swap_b32_e32 v148, v149\n\t"
    "v_permlane16_swap_b32_e32 v152, v153\n\t"
    "v_permlane16_swap_b32_e32 v156, v157\n\t"
    "v_add_f32_e32 v144, v144, v145\n\t"
    "v_add_f32_e32 v148, v148, v149\n\t"
    "v_add_f32_e32 v152, v152, v153\n\t"
    "v_add_f32_e32 v156, v156, v157\n\t"
    "v_mov_b32_e32 v145, v144\n\t"
    "v_mov_b32_e32 v149, v148\n\t"
    "v_mov_b32_e32 v153, v152\n\t"
    "v_mov_b32_e32 v157, v156\n\t"
    "s_nop 1\n\t"
    "v_permlane32_swap_b32_e32 v144, v145\n\t"
    "v_permlane32_swap_b32_e32 v148, v149\n\t"
    "v_permlane32_swap_b32_e32 v152, v153\n\t"
    "v_permlane32_swap_b32_e32 v156, v157\n\t"
    "v_add_f32_e32 v144, v144, v145\n\t"
    "v_add_f32_e32 v148, v148, v149\n\t"
    "v_add_f32_e32 v152, v152, v153\n\t"
    "v_add_f32_e32 v156, v156, v157\n\t"
    "v_cmp_nge_f32_e32 vcc, s98, v144\n\t"
    "v_cmp_nge_f32_e64 s[88:89], s98, v148\n\t"
    "s_or_b64 vcc, vcc, s[88:89]\n\t"
    "v_cmp_nge_f32_e64 s[88:89], s98, v152\n\t"
    "s_or_b64 vcc, vcc, s[88:89]\n\t"
    "v_cmp_nge_f32_e64 s[88:89], s98, v156\n\t"
    "s_or_b64 vcc, vcc, s[88:89]\n\t"
    "v_rcp_f32_e32 v144, v144\n\t"
    "v_rcp_f32_e32 v148, v148\n\t"
    "v_rcp_f32_e32 v152, v152\n\t"
    "v_rcp_f32_e32 v156, v156\n\t"
    "s_cmp_lg_u64 vcc, 0\n\t"
    "s_cselect_b32 %[bad], 1, 0\n\t"
    "v_accvgpr_read_b32 v0, a0\n\t"
    "v_accvgpr_read_b32 v1, a1\n\t"
    "v_accvgpr_read_b32 v2, a2\n\t"
    "v_accvgpr_read_b32 v3, a3\n\t"
    "v_accvgpr_read_b32 v4, a4\n\t"
    "v_accvgpr_read_b32 v5, a5\n\t"
    "v_accvgpr_read_b32 v6, a6\n\t"
    "v_accvgpr_read_b32 v7, a7\n\t"
    "v_pk_mul_f32 v[0:1], v[0:1], v[144:145] op_sel_hi:[1,0]\n\t"
    "v_pk_mul_f32 v[2:3], v[2:3], v[144:145] op_sel_hi:[1,0]\n\t"
    "v_pk_mul_f32 v[4:5], v[4:5], v[144:145] op_sel_hi:[1,0]\n\t"
    "v_pk_mul_f32 v[6:7], v[6:7], v[144:145] op_sel_hi:[1,0]\n\t"
    "v_cvt_pk_bf16_f32 v0, v0, v1\n\t"
    "v_cvt_pk_bf16_f32 v1, v2, v3\n\t"
    "v_cvt_pk_bf16_f32 v2, v4, v5\n\t"
    "v_cvt_pk_bf16_f32 v3, v6, v7\n\t"
    "buffer_store_dwordx4 v[0:3], v250, s[76:79], 0 offen offset:0 sc1\n\t"
    "v_accvgpr_read_b32 v8, a8\n\t"
    "v_accvgpr_read_b32 v9, a9\n\t"
    "v_accvgpr_read_b32 v10, a10\n\t"
    "v_accvgpr_read_b32 v11, a11\n\t"
    "v_accvgpr_read_b32 v12, a12\n\t"
    "v_accvgpr_read_b32 v13, a13\n\t"
    "v_accvgpr_read_b32 v14, a14\n\t"
    "v_accvgpr_read_b32 v15, a15\n\t"
    "v_pk_mul_f32 v[8:9], v[8:9], v[148:149] op_sel_hi:[1,0]\n\t"
    "v_pk_mul_f32 v[10:11], v[10:11], v[148:149] op_sel_hi:[1,0]\n\t"
    "v_pk_mul_f32 v[12:13], v[12:13], v[148:149] op_sel_hi:[1,0]\n\t"
    "v_pk_mul_f32 v[14:15], v[14:15], v[148:149] op_sel_hi:[1,0]\n\t"
    "v_cvt_pk_bf16_f32 v8, v8, v9\n\t"
    "v_cvt_pk_bf16_f32 v9, v10, v11\n\t"
    "v_cvt_pk_bf16_f32 v10, v12, v13\n\t"
    "v_cvt_pk_bf16_f32 v11, v14, v15\n\t"
    "buffer_store_dwordx4 v[8:11], v250, s[76:79], s84 offen offset:0 sc1\n\t"
    "v_accvgpr_read_b32 v16, a16\n\t"
    "v_accvgpr_read_b32 v17, a17\n\t"
    "v_accvgpr_read_b32 v18, a18\n\t"
    "v_accvgpr_read_b32 v19, a19\n\t"
    "v_accvgpr_read_b32 v20, a20\n\t"
    "v_accvgpr_read_b32 v21, a21\n\t"
    "v_accvgpr_read_b32 v22, a22\n\t"
    "v_accvgpr_read_b32 v23, a23\n\t"
    "v_pk_mul_f32 v[16:17], v[16:17], v[152:153] op_sel_hi:[1,0]\n\t"
    "v_pk_mul_f32 v[18:19], v[18:19], v[152:153] op_sel_hi:[1,0]\n\t"
    "v_pk_mul_f32 v[20:21], v[20:21], v[152:153] op_sel_hi:[1,0]\n\t"
    "v_pk_mul_f32 v[22:23], v[22:23], v[152:153] op_sel_hi:[1,0]\n\t"
    "v_cvt_pk_bf16_f32 v16, v16, v17\n\t"
    "v_cvt_pk_bf16_f32 v17, v18, v19\n\t"
    "v_cvt_pk_bf16_f32 v18, v20, v21\n\t"
    "v_cvt_pk_bf16_f32 v19, v22, v23\n\t"
    "buffer_store_dwordx4 v[16:19], v250, s[76:79], s85 offen offset:0 sc1\n\t"
    "v_accvgpr_read_b32 v24, a24\n\t"
    "v_accvgpr_read_b32 v25, a25\n\t"
    "v_accvgpr_read_b32 v26, a26\n\t"
    "v_accvgpr_read_b32 v27, a27\n\t"
    "v_accvgpr_read_b32 v28, a28\n\t"
    "v_accvgpr_read_b32 v29, a29\n\t"
    "v_accvgpr_read_b32 v30, a30\n\t"
    "v_accvgpr_read_b32 v31, a31\n\t"
    "v_pk_mul_f32 v[24:25], v[24:25], v[156:157] op_sel_hi:[1,0]\n\t"
    "v_pk_mul_f32 v[26:27], v[26:27], v[156:157] op_sel_hi:[1,0]\n\t"
    "v_pk_mul_f32 v[28:29], v[28:29], v[156:157] op_sel_hi:[1,0]\n\t"
    "v_pk_mul_f32 v[30:31], v[30:31], v[156:157] op_sel_hi:[1,0]\n\t"
    "v_cvt_pk_bf16_f32 v24, v24, v25\n\t"
    "v_cvt_pk_bf16_f32 v25, v26, v27\n\t"
    "v_cvt_pk_bf16_f32 v26, v28, v29\n\t"
    "v_cvt_pk_bf16_f32 v27, v30, v31\n\t"
    "buffer_store_dwordx4 v[24:27], v250, s[76:79], s86 offen offset:0 sc1\n\t"
    "v_accvgpr_read_b32 v32, a32\n\t"
    "v_accvgpr_read_b32 v33, a33\n\t"
    "v_accvgpr_read_b32 v34, a34\n\t"
    "v_accvgpr_read_b32 v35, a35\n\t"
    "v_accvgpr_read_b32 v36, a36\n\t"
    "v_accvgpr_read_b32 v37, a37\n\t"
    "v_accvgpr_read_b32 v38, a38\n\t"
    "v_accvgpr_read_b32 v39, a39\n\t"
    "v_pk_mul_f32 v[32:33], v[32:33], v[144:145] op_sel_hi:[1,0]\n\t"
    "v_pk_mul_f32 v[34:35], v[34:35], v[144:145] op_sel_hi:[1,0]\n\t"
    "v_pk_mul_f32 v[36:37], v[36:37], v[144:145] op_sel_hi:[1,0]\n\t"
    "v_pk_mul_f32 v[38:39], v[38:39], v[144:145] op_sel_hi:[1,0]\n\t"
    "v_cvt_pk_bf16_f32 v32, v32, v33\n\t"
    "v_cvt_pk_bf16_f32 v33, v34, v35\n\t"
    "v_cvt_pk_bf16_f32 v34, v36, v37\n\t"
    "v_cvt_pk_bf16_f32 v35, v38, v39\n\t"
    "buffer_store_dwordx4 v[32:35], v250, s[76:79], 0 offen offset:1024 sc1\n\t"
    "v_accvgpr_read_b32 v40, a40\n\t"
    "v_accvgpr_read_b32 v41, a41\n\t"
    "v_accvgpr_read_b32 v42, a42\n\t"
    "v_accvgpr_read_b32 v43, a43\n\t"
    "v_accvgpr_read_b32 v44, a44\n\t"
    "v_accvgpr_read_b32 v45, a45\n\t"
    "v_accvgpr_read_b32 v46, a46\n\t"
    "v_accvgpr_read_b32 v47, a47\n\t"
    "v_pk_mul_f32 v[40:41], v[40:41], v[148:149] op_sel_hi:[1,0]\n\t"
    "v_pk_mul_f32 v[42:43], v[42:43], v[148:149] op_sel_hi:[1,0]\n\t"
    "v_pk_mul_f32 v[44:45], v[44:45], v[148:149] op_sel_hi:[1,0]\n\t"
    "v_pk_mul_f32 v[46:47], v[46:47], v[148:149] op_sel_hi:[1,0]\n\t"
    "v_cvt_pk_bf16_f32 v40, v40, v41\n\t"
    "v_cvt_pk_bf16_f32 v41, v42, v43\n\t"
    "v_cvt_pk_bf16_f32 v42, v44, v45\n\t"
    "v_cvt_pk_bf16_f32 v43, v46, v47\n\t"
    "buffer_store_dwordx4 v[40:43], v250, s[76:79], s84 offen offset:1024 sc1\n\t"
    "v_accvgpr_read_b32 v48, a48\n\t"
    "v_accvgpr_read_b32 v49, a49\n\t"
    "v_accvgpr_read_b32 v50, a50\n\t"
    "v_accvgpr_read_b32 v51, a51\n\t"
    "v_accvgpr_read_b32 v52, a52\n\t"
    "v_accvgpr_read_b32 v53, a53\n\t"
    "v_accvgpr_read_b32 v54, a54\n\t"
    "v_accvgpr_read_b32 v55, a55\n\t"
    "v_pk_mul_f32 v[48:49], v[48:49], v[152:153] op_sel_hi:[1,0]\n\t"
    "v_pk_mul_f32 v[50:51], v[50:51], v[152:153] op_sel_hi:[1,0]\n\t"
    "v_pk_mul_f32 v[52:53], v[52:53], v[152:153] op_sel_hi:[1,0]\n\t"
    "v_pk_mul_f32 v[54:55], v[54:55], v[152:153] op_sel_hi:[1,0]\n\t"
    "v_cvt_pk_bf16_f32 v48, v48, v49\n\t"
    "v_cvt_pk_bf16_f32 v49, v50, v51\n\t"
    "v_cvt_pk_bf16_f32 v50, v52, v53\n\t"
    "v_cvt_pk_bf16_f32 v51, v54, v55\n\t"
    "buffer_store_dwordx4 v[48:51], v250, s[76:79], s85 offen offset:1024 sc1\n\t"
    "v_accvgpr_read_b32 v56, a56\n\t"
    "v_accvgpr_read_b32 v57, a57\n\t"
    "v_accvgpr_read_b32 v58, a58\n\t"
    "v_accvgpr_read_b32 v59, a59\n\t"
    "v_accvgpr_read_b32 v60, a60\n\t"
    "v_accvgpr_read_b32 v61, a61\n\t"
    "v_accvgpr_read_b32 v62, a62\n\t"
    "v_accvgpr_read_b32 v63, a63\n\t"
    "v_pk_mul_f32 v[56:57], v[56:57], v[156:157] op_sel_hi:[1,0]\n\t"
    "v_pk_mul_f32 v[58:59], v[58:59], v[156:157] op_sel_hi:[1,0]\n\t"
    "v_pk_mul_f32 v[60:61], v[60:61], v[156:157] op_sel_hi:[1,0]\n\t"
    "v_pk_mul_f32 v[62:63], v[62:63], v[156:157] op_sel_hi:[1,0]\n\t"
    "v_cvt_pk_bf16_f32 v56, v56, v57\n\t"
    "v_cvt_pk_bf16_f32 v57, v58, v59\n\t"
    "v_cvt_pk_bf16_f32 v58, v60, v61\n\t"
    "v_cvt_pk_bf16_f32 v59, v62, v63\n\t"
    "buffer_store_dwordx4 v[56:59], v250, s[76:79], s86 offen offset:1024 sc1\n\t"
    "v_accvgpr_read_b32 v64, a64\n\t"
    "v_accvgpr_read_b32 v65, a65\n\t"
    "v_accvgpr_read_b32 v66, a66\n\t"
    "v_accvgpr_read_b32 v67, a67\n\t"
    "v_accvgpr_read_b32 v68, a68\n\t"
    "v_accvgpr_read_b32 v69, a69\n\t"
    "v_accvgpr_read_b32 v70, a70\n\t"
    "v_accvgpr_read_b32 v71, a71\n\t"
    "v_pk_mul_f32 v[64:65], v[64:65], v[144:145] op_sel_hi:[1,0]\n\t"
    "v_pk_mul_f32 v[66:67], v[66:67], v[144:145] op_sel_hi:[1,0]\n\t"
    "v_pk_mul_f32 v[68:69], v[68:69], v[144:145] op_sel_hi:[1,0]\n\t"
    "v_pk_mul_f32 v[70:71], v[70:71], v[144:145] op_sel_hi:[1,0]\n\t"
    "v_cvt_pk_bf16_f32 v64, v64, v65\n\t"
    "v_cvt_pk_bf16_f32 v65, v66, v67\n\t"
    "v_cvt_pk_bf16_f32 v66, v68, v69\n\t"
    "v_cvt_pk_bf16_f32 v67, v70, v71\n\t"
    "buffer_store_dwordx4 v[64:67], v250, s[76:79], 0 offen offset:2048 sc1\n\t"
    "v_accvgpr_read_b32 v72, a72\n\t"
    "v_accvgpr_read_b32 v73, a73\n\t"
    "v_accvgpr_read_b32 v74, a74\n\t"
    "v_accvgpr_read_b32 v75, a75\n\t"
    "v_accvgpr_read_b32 v76, a76\n\t"
    "v_accvgpr_read_b32 v77, a77\n\t"
    "v_accvgpr_read_b32 v78, a78\n\t"
    "v_accvgpr_read_b32 v79, a79\n\t"
    "v_pk_mul_f32 v[72:73], v[72:73], v[148:149] op_sel_hi:[1,0]\n\t"
    "v_pk_mul_f32 v[74:75], v[74:75], v[148:149] op_sel_hi:[1,0]\n\t"
    "v_pk_mul_f32 v[76:77], v[76:77], v[148:149] op_sel_hi:[1,0]\n\t"
    "v_pk_mul_f32 v[78:79], v[78:79], v[148:149] op_sel_hi:[1,0]\n\t"
    "v_cvt_pk_bf16_f32 v72, v72, v73\n\t"
    "v_cvt_pk_bf16_f32 v73, v74, v75\n\t"
    "v_cvt_pk_bf16_f32 v74, v76, v77\n\t"
    "v_cvt_pk_bf16_f32 v75, v78, v79\n\t"
    "buffer_store_dwordx4 v[72:75], v250, s[76:79], s84 offen offset:2048 sc1\n\t"
    "v_accvgpr_read_b32 v80, a80\n\t"
    "v_accvgpr_read_b32 v81, a81\n\t"
    "v_accvgpr_read_b32 v82, a82\n\t"
    "v_accvgpr_read_b32 v83, a83\n\t"
    "v_accvgpr_read_b32 v84, a84\n\t"
    "v_accvgpr_read_b32 v85, a85\n\t"
    "v_accvgpr_read_b32 v86, a86\n\t"
    "v_accvgpr_read_b32 v87, a87\n\t"
    "v_pk_mul_f32 v[80:81], v[80:81], v[152:153] op_sel_hi:[1,0]\n\t"
    "v_pk_mul_f32 v[82:83], v[82:83], v[152:153] op_sel_hi:[1,0]\n\t"
    "v_pk_mul_f32 v[84:85], v[84:85], v[152:153] op_sel_hi:[1,0]\n\t"
    "v_pk_mul_f32 v[86:87], v[86:87], v[152:153] op_sel_hi:[1,0]\n\t"
    "v_cvt_pk_bf16_f32 v80, v80, v81\n\t"
    "v_cvt_pk_bf16_f32 v81, v82, v83\n\t"
    "v_cvt_pk_bf16_f32 v82, v84, v85\n\t"
    "v_cvt_pk_bf16_f32 v83, v86, v87\n\t"
    "buffer_store_dwordx4 v[80:83], v250, s[76:79], s85 offen offset:2048 sc1\n\t"
    "v_accvgpr_read_b32 v88, a88\n\t"
    "v_accvgpr_read_b32 v89, a89\n\t"
    "v_accvgpr_read_b32 v90, a90\n\t"
    "v_accvgpr_read_b32 v91, a91\n\t"
    "v_accvgpr_read_b32 v92, a92\n\t"
    "v_accvgpr_read_b32 v93, a93\n\t"
    "v_accvgpr_read_b32 v94, a94\n\t"
    "v_accvgpr_read_b32 v95, a95\n\t"
    "v_pk_mul_f32 v[88:89], v[88:89], v[156:157] op_sel_hi:[1,0]\n\t"
    "v_pk_mul_f32 v[90:91], v[90:91], v[156:157] op_sel_hi:[1,0]\n\t"
    "v_pk_mul_f32 v[92:93], v[92:93], v[156:157] op_sel_hi:[1,0]\n\t"
    "v_pk_mul_f32 v[94:95], v[94:95], v[156:157] op_sel_hi:[1,0]\n\t"
    "v_cvt_pk_bf16_f32 v88, v88, v89\n\t"
    "v_cvt_pk_bf16_f32 v89, v90, v91\n\t"
    "v_cvt_pk_bf16_f32 v90, v92, v93\n\t"
    "v_cvt_pk_bf16_f32 v91, v94, v95\n\t"
    "buffer_store_dwordx4 v[88:91], v250, s[76:79], s86 offen offset:2048 sc1\n\t"
    "v_accvgpr_read_b32 v96, a96\n\t"
    "v_accvgpr_read_b32 v97, a97\n\t"
    "v_accvgpr_read_b32 v98, a98\n\t"
    "v_accvgpr_read_b32 v99, a99\n\t"
    "v_accvgpr_read_b32 v100, a100\n\t"
    "v_accvgpr_read_b32 v101, a101\n\t"
    "v_accvgpr_read_b32 v102, a102\n\t"
    "v_accvgpr_read_b32 v103, a103\n\t"
    "v_pk_mul_f32 v[96:97], v[96:97], v[144:145] op_sel_hi:[1,0]\n\t"
    "v_pk_mul_f32 v[98:99], v[98:99], v[144:145] op_sel_hi:[1,0]\n\t"
    "v_pk_mul_f32 v[100:101], v[100:101], v[144:145] op_sel_hi:[1,0]\n\t"
    "v_pk_mul_f32 v[102:103], v[102:103], v[144:145] op_sel_hi:[1,0]\n\t"
    "v_cvt_pk_bf16_f32 v96, v96, v97\n\t"
    "v_cvt_pk_bf16_f32 v97, v98, v99\n\t"
    "v_cvt_pk_bf16_f32 v98, v100, v101\n\t"
    "v_cvt_pk_bf16_f32 v99, v102, v103\n\t"
    "buffer_store_dwordx4 v[96:99], v250, s[76:79], 0 offen offset:3072 sc1\n\t"
    "v_accvgpr_read_b32 v104, a104\n\t"
    "v_accvgpr_read_b32 v105, a105\n\t"
    "v_accvgpr_read_b32 v106, a106\n\t"
    "v_accvgpr_read_b32 v107, a107\n\t"
    "v_accvgpr_read_b32 v108, a108\n\t"
    "v_accvgpr_read_b32 v109, a109\n\t"
    "v_accvgpr_read_b32 v110, a110\n\t"
    "v_accvgpr_read_b32 v111, a111\n\t"
    "v_pk_mul_f32 v[104:105], v[104:105], v[148:149] op_sel_hi:[1,0]\n\t"
    "v_pk_mul_f32 v[106:107], v[106:107], v[148:149] op_sel_hi:[1,0]\n\t"
    "v_pk_mul_f32 v[108:109], v[108:109], v[148:149] op_sel_hi:[1,0]\n\t"
    "v_pk_mul_f32 v[110:111], v[110:111], v[148:149] op_sel_hi:[1,0]\n\t"
    "v_cvt_pk_bf16_f32 v104, v104, v105\n\t"
    "v_cvt_pk_bf16_f32 v105, v106, v107\n\t"
    "v_cvt_pk_bf16_f32 v106, v108, v109\n\t"
    "v_cvt_pk_bf16_f32 v107, v110, v111\n\t"
    "buffer_store_dwordx4 v[104:107], v250, s[76:79], s84 offen offset:3072 sc1\n\t"
    "v_accvgpr_read_b32 v112, a112\n\t"
    "v_accvgpr_read_b32 v113, a113\n\t"
    "v_accvgpr_read_b32 v114, a114\n\t"
    "v_accvgpr_read_b32 v115, a115\n\t"
    "v_accvgpr_read_b32 v116, a116\n\t"
    "v_accvgpr_read_b32 v117, a117\n\t"
    "v_accvgpr_read_b32 v118, a118\n\t"
    "v_accvgpr_read_b32 v119, a119\n\t"
    "v_pk_mul_f32 v[112:113], v[112:113], v[152:153] op_sel_hi:[1,0]\n\t"
    "v_pk_mul_f32 v[114:115], v[114:115], v[152:153] op_sel_hi:[1,0]\n\t"
    "v_pk_mul_f32 v[116:117], v[116:117], v[152:153] op_sel_hi:[1,0]\n\t"
    "v_pk_mul_f32 v[118:119], v[118:119], v[152:153] op_sel_hi:[1,0]\n\t"
    "v_cvt_pk_bf16_f32 v112, v112, v113\n\t"
    "v_cvt_pk_bf16_f32 v113, v114, v115\n\t"
    "v_cvt_pk_bf16_f32 v114, v116, v117\n\t"
    "v_cvt_pk_bf16_f32 v115, v118, v119\n\t"
    "buffer_store_dwordx4 v[112:115], v250, s[76:79], s85 offen offset:3072 sc1\n\t"
    "v_accvgpr_read_b32 v120, a120\n\t"
    "v_accvgpr_read_b32 v121, a121\n\t"
    "v_accvgpr_read_b32 v122, a122\n\t"
    "v_accvgpr_read_b32 v123, a123\n\t"
    "v_accvgpr_read_b32 v124, a124\n\t"
    "v_accvgpr_read_b32 v125, a125\n\t"
    "v_accvgpr_read_b32 v126, a126\n\t"
    "v_accvgpr_read_b32 v127, a127\n\t"
    "v_pk_mul_f32 v[120:121], v[120:121], v[156:157] op_sel_hi:[1,0]\n\t"
    "v_pk_mul_f32 v[122:123], v[122:123], v[156:157] op_sel_hi:[1,0]\n\t"
    "v_pk_mul_f32 v[124:125], v[124:125], v[156:157] op_sel_hi:[1,0]\n\t"
    "v_pk_mul_f32 v[126:127], v[126:127], v[156:157] op_sel_hi:[1,0]\n\t"
    "v_cvt_pk_bf16_f32 v120, v120, v121\n\t"
    "v_cvt_pk_bf16_f32 v121, v122, v123\n\t"
    "v_cvt_pk_bf16_f32 v122, v124, v125\n\t"
    "v_cvt_pk_bf16_f32 v123, v126, v127\n\t"
    "buffer_store_dwordx4 v[120:123], v250, s[76:79], s86 offen offset:3072 sc1\n\t"
    "s_mov_b32 s87, 16384\n\t"
    "buffer_load_dwordx4 a[192:195], v250, s[64:67], s87 offen offset:0 sc1\n\t"
    "buffer_load_dwordx4 a[196:199], v250, s[64:67], s87 offen offset:1024 sc1\n\t"
    "buffer_load_dwordx4 a[200:203], v250, s[64:67], s87 offen offset:2048 sc1\n\t"
    "buffer_load_dwordx4 a[204:207], v250, s[64:67], s87 offen offset:3072 sc1\n\t"
    "s_mov_b32 s88, 20480\n\t"
    "buffer_load_dwordx4 a[208:211], v250, s[64:67], s88 offen offset:0 sc1\n\t"
    "buffer_load_dwordx4 a[212:215], v250, s[64:67], s88 offen offset:1024 sc1\n\t"
    "buffer_load_dwordx4 a[216:219], v250, s[64:67], s88 offen offset:2048 sc1\n\t"
    "buffer_load_dwordx4 a[220:223], v250, s[64:67], s88 offen offset:3072 sc1\n\t"
    "s_mov_b32 s87, 24576\n\t"
    "buffer_load_dwordx4 a[224:227], v250, s[64:67], s87 offen offset:0 sc1\n\t"
    "buffer_load_dwordx4 a[228:231], v250, s[64:67], s87 offen offset:1024 sc1\n\t"
    "buffer_load_dwordx4 a[232:235], v250, s[64:67], s87 offen offset:2048 sc1\n\t"
    "buffer_load_dwordx4 a[236:239], v250, s[64:67], s87 offen offset:3072 sc1\n\t"
    "s_mov_b32 s88, 28672\n\t"
    "buffer_load_dwordx4 a[240:243], v250, s[64:67], s88 offen offset:0 sc1\n\t"
    "buffer_load_dwordx4 a[244:247], v250, s[64:67], s88 offen offset:1024 sc1\n\t"
    "buffer_load_dwordx4 a[248:251], v250, s[64:67], s88 offen offset:2048 sc1\n\t"
    "buffer_load_dwordx4 a[252:255], v250, s[64:67], s88 offen offset:3072 sc1\n\t"
    "s_mul_i32 s33, %[b], 512\n\t"
    "s_mul_i32 s34, %[wid], 128\n\t"
    "s_add_u32 s33, s33, s34\n\t"
    "s_mul_i32 s34, %[h], 4\n\t"
    "s_add_u32 s33, s33, s34\n\t"
    "s_add_u32 s33, s33, 36864\n\t"
    "v_mov_b32_e32 v240, %[tag]\n\t"
    "s_waitcnt vmcnt(16)\n\t"
    "s_cmp_lg_u32 %[bad], 0\n\t"
    "s_cbranch_scc1 L_fo_%=\n\t"
    "s_mov_b64 exec, 1\n\t"
    "buffer_store_dword v240, off, s[60:63], s33 sc1\n\t"
    "s_mov_b64 exec, -1\n\t"
    "L_fo_%=:\n\t"
    "s_mov_b32 s87, 32768\n\t"
    "buffer_load_dwordx4 a[0:3], v250, s[64:67], s87 offen offset:0 sc1\n\t"
    "buffer_load_dwordx4 a[4:7], v250, s[64:67], s87 offen offset:1024 sc1\n\t"
    "buffer_load_dwordx4 a[8:11], v250, s[64:67], s87 offen offset:2048 sc1\n\t"
    "buffer_load_dwordx4 a[12:15], v250, s[64:67], s87 offen offset:3072 sc1\n\t"
    "s_mov_b32 s88, 36864\n\t"
    "buffer_load_dwordx4 a[16:19], v250, s[64:67], s88 offen offset:0 sc1\n\t"
    "buffer_load_dwordx4 a[20:23], v250, s[64:67], s88 offen offset:1024 sc1\n\t"
    "buffer_load_dwordx4 a[24:27], v250, s[64:67], s88 offen offset:2048 sc1\n\t"
    "buffer_load_dwordx4 a[28:31], v250, s[64:67], s88 offen offset:3072 sc1\n\t"
    "s_mov_b32 s87, 40960\n\t"
    "buffer_load_dwordx4 a[32:35], v250, s[64:67], s87 offen offset:0 sc1\n\t"
    "buffer_load_dwordx4 a[36:39], v250, s[64:67], s87 offen offset:1024 sc1\n\t"
    "buffer_load_dwordx4 a[40:43], v250, s[64:67], s87 offen offset:2048 sc1\n\t"
    "buffer_load_dwordx4 a[44:47], v250, s[64:67], s87 offen offset:3072 sc1\n\t"
    "s_mov_b32 s88, 45056\n\t"
    "buffer_load_dwordx4 a[48:51], v250, s[64:67], s88 offen offset:0 sc1\n\t"
    "buffer_load_dwordx4 a[52:55], v250, s[64:67], s88 offen offset:1024 sc1\n\t"
    "buffer_load_dwordx4 a[56:59], v250, s[64:67], s88 offen offset:2048 sc1\n\t"
    "buffer_load_dwordx4 a[60:63], v250, s[64:67], s88 offen offset:3072 sc1\n\t"
    "s_mov_b32 s87, 49152\n\t"
    "buffer_load_dwordx4 a[64:67], v250, s[64:67], s87 offen offset:0 sc1\n\t"
    "buffer_load_dwordx4 a[68:71], v250, s[64:67], s87 offen offset:1024 sc1\n\t"
    "buffer_load_dwordx4 a[72:75], v250, s[64:67], s87 offen offset:2048 sc1\n\t"
    "buffer_load_dwordx4 a[76:79], v250, s[64:67], s87 offen offset:3072 sc1\n\t"
    "s_mov_b32 s88, 53248\n\t"
    "buffer_load_dwordx4 a[80:83], v250, s[64:67], s88 offen offset:0 sc1\n\t"
    "buffer_load_dwordx4 a[84:87], v250, s[64:67], s88 offen offset:1024 sc1\n\t"
    "buffer_load_dwordx4 a[88:91], v250, s[64:67], s88 offen offset:2048 sc1\n\t"
    "buffer_load_dwordx4 a[92:95], v250, s[64:67], s88 offen offset:3072 sc1\n\t"
    "s_mov_b32 s87, 57344\n\t"
    "buffer_load_dwordx4 a[96:99], v250, s[64:67], s87 offen offset:0 sc1\n\t"
    "buffer_load_dwordx4 a[100:103], v250, s[64:67], s87 offen offset:1024 sc1\n\t"
    "buffer_load_dwordx4 a[104:107], v250, s[64:67], s87 offen offset:2048 sc1\n\t"
    "buffer_load_dwordx4 a[108:111], v250, s[64:67], s87 offen offset:3072 sc1\n\t"
    "s_mov_b32 s88, 61440\n\t"
    "buffer_load_dwordx4 a[112:115], v250, s[64:67], s88 offen offset:0 sc1\n\t"
    "buffer_load_dwordx4 a[116:119], v250, s[64:67], s88 offen offset:1024 sc1\n\t"
    "buffer_load_dwordx4 a[120:123], v250, s[64:67], s88 offen offset:2048 sc1\n\t"
    "buffer_load_dwordx4 a[124:127], v250, s[64:67], s88 offen offset:3072 sc1\n\t"
    "s_waitcnt vmcnt(40)\n\t"
    "s_mov_b32 %[best], s51\n\t"
    "s_nop 1\n\t"
    : [bad] "=&s"(bad), [best] "=&s"(best), [tag] "=&s"(tag)
    : [wid] "s"(wid), [h] "s"(h), [b] "s"(b), [bx] "s"(bx), [pj] "s"(bx >> 3), [fj] "s"(fj), [ka] "s"(ka)
    : "v0", "v1", "v2", "v3", "v4", "v5", "v6", "v7", "v8", "v9", "v10", "v11", "v12", "v13", "v14", "v15", "v16", "v17", "v18", "v19", "v20", "v21", "v22", "v23", "v24", "v25", "v26", "v27", "v28", "v29", "v30", "v31", "v32", "v33", "v34", "v35", "v36", "v37", "v38", "v39", "v40", "v41", "v42", "v43", "v44", "v45", "v46", "v47", "v48", "v49", "v50", "v51", "v52", "v53", "v54", "v55", "v56", "v57", "v58", "v59", "v60", "v61", "v62", "v63", "v64", "v65", "v66", "v67", "v68", "v69", "v70", "v71", "v72", "v73", "v74", "v75", "v76", "v77", "v78", "v79", "v80", "v81", "v82", "v83", "v84", "v85", "v86", "v87", "v88", "v89", "v90", "v91", "v92", "v93", "v94", "v95", "v96", "v97", "v98", "v99", "v100", "v101", "v102", "v103", "v104", "v105", "v106", "v107", "v108", "v109", "v110", "v111", "v112", "v113", "v114", "v115", "v116", "v117", "v118", "v119", "v120", "v121", "v122", "v123", "v124", "v125", "v126", "v127", "v128", "v129", "v130", "v131", "v132", "v133", "v134", "v135", "v136", "v137", "v138", "v139", "v140", "v141", "v142", "v143", "v144", "v145", "v146", "v147", "v148", "v149", "v150", "v151", "v152", "v153", "v154", "v155", "v156", "v157", "v158", "v159", "v160", "v161", "v162", "v163", "v164", "v165", "v166", "v167", "v168", "v169", "v170", "v171", "v172", "v173", "v174", "v175", "v176", "v177", "v178", "v179", "v180", "v181", "v182", "v183", "v184", "v185", "v186", "v187", "v188", "v189", "v190", "v191", "v192", "v193", "v194", "v195", "v196", "v197", "v198", "v199", "v200", "v201", "v202", "v203", "v204", "v205", "v206", "v207", "v208", "v209", "v210", "v211", "v212", "v213", "v214", "v215", "v216", "v217", "v218", "v219", "v220", "v221", "v222", "v223", "v224", "v225", "v226", "v227", "v228", "v229", "v230", "v231", "v232", "v233", "v234", "v235", "v236", "v237", "v238", "v239", "v240", "v241", "v242", "v243", "v244", "v245", "v246", "v247", "v248", "v249", "v250", "v251", "v252", "v253", "v254", "v255", "a0", "a1", "a2", "a3", "a4", "a5", "a6", "a7", "a8", "a9", "a10", "a11", "a12", "a13", "a14", "a15", "a16", "a17", "a18", "a19", "a20", "a21", "a22", "a23", "a24", "a25", "a26", "a27", "a28", "a29", "a30", "a31", "a32", "a33", "a34", "a35", "a36", "a37", "a38", "a39", "a40", "a41", "a42", "a43", "a44", "a45", "a46", "a47", "a48", "a49", "a50", "a51", "a52", "a53", "a54", "a55", "a56", "a57", "a58", "a59", "a60", "a61", "a62", "a63", "a64", "a65", "a66", "a67", "a68", "a69", "a70", "a71", "a72", "a73", "a74", "a75", "a76", "a77", "a78", "a79", "a80", "a81", "a82", "a83", "a84", "a85", "a86", "a87", "a88", "a89", "a90", "a91", "a92", "a93", "a94", "a95", "a96", "a97", "a98", "a99", "a100", "a101", "a102", "a103", "a104", "a105", "a106", "a107", "a108", "a109", "a110", "a111", "a112", "a113", "a114", "a115", "a116", "a117", "a118", "a119", "a120", "a121", "a122", "a123", "a124", "a125", "a126", "a127", "a128", "a129", "a130", "a131", "a132", "a133", "a134", "a135", "a136", "a137", "a138", "a139", "a140", "a141", "a142", "a143", "a144", "a145", "a146", "a147", "a148", "a149", "a150", "a151", "a152", "a153", "a154", "a155", "a156", "a157", "a158", "a159", "a160", "a161", "a162", "a163", "a164", "a165", "a166", "a167", "a168", "a169", "a170", "a171", "a172", "a173", "a174", "a175", "a176", "a177", "a178", "a179", "a180", "a181", "a182", "a183", "a184", "a185", "a186", "a187", "a188", "a189", "a190", "a191", "a192", "a193", "a194", "a195", "a196", "a197", "a198", "a199", "a200", "a201", "a202", "a203", "a204", "a205", "a206", "a207", "a208", "a209", "a210", "a211", "a212", "a213", "a214", "a215", "a216", "a217", "a218", "a219", "a220", "a221", "a222", "a223", "a224", "a225", "a226", "a227", "a228", "a229", "a230", "a231", "a232", "a233", "a234", "a235", "a236", "a237", "a238", "a239", "a240", "a241", "a242", "a243", "a244", "a245", "a246", "a247", "a248", "a249", "a250", "a251", "a252", "a253", "a254", "a255", "s33", "s34", "s35", "s36", "s37", "s38", "s39", "s40", "s41", "s42", "s43", "s44", "s45", "s46", "s47", "s48", "s49", "s50", "s51", "s52", "s53", "s54", "s55", "s56", "s57", "s58", "s59", "s60", "s61", "s62", "s63", "s64", "s65", "s66", "s67", "s68", "s69", "s70", "s71", "s72", "s73", "s74", "s75", "s76", "s77", "s78", "s79", "s80", "s81", "s82", "s83", "s84", "s85", "s86", "s87", "s88", "s89", "s90", "s91", "s92", "s93", "s94", "s95", "s96", "s97", "s98", "s99", "vcc", "scc", "memory");
  }
  u32 idx, rsb; u32 st0; u32x4 xa, xc, ga, gc;
  {
    ka = again(ka); const u16 *x = gp<const u16>(ka[0]), *K = gp<const u16>(ka[1]), *V = gp<const u16>(ka[2]), *wq = gp<const u16>(ka[3]); char* ws = gp<char>(ka[9]);
    const u32 lane = lane_id(), tid = 64 * wid + lane;
    if (bad) { slow_rows(lds, x, wq, K, V, ws, h, b, wid, lane); if (lane == 0) __hip_atomic_store((gu32*)(ws + WS_FO) + 128 * b + 32 * wid + h, tag, RLX); }
    u32* red = (u32*)(lds + L_SC + 64); float* fr = (float*)(lds + L_SC + 256);
    const bool staged = best != 0xffffffffu;
    if (!staged) {
      u32 bb = 0;
      for (int i = 0; i < 8; ++i) { u64 g = 0;
        for (int spin = 0; spin < SPIN; ++spin) { g = __hip_atomic_load((gu64*)(ws + WS_G2) + tid + 256 * i, RLX); if ((u32)(g >> 32) == tag) break; }
        bb = max(bb, (u32)g); }
      for (int o = 32; o; o >>= 1) bb = max(bb, (u32)__shfl_xor((int)bb, o));
      red[wid] = bb; bar();
      best = __builtin_amdgcn_readfirstlane(max(max(red[0], red[1]), max(red[2], red[3])));
    }
    idx = ~best & 2047u; const u32 key = best >> 16, rb = (key & 0x8000u) ? (key & 0x7fffu) : (~key & 0xffffu);
    rsb = __builtin_amdgcn_readfirstlane(__float_as_uint(rbf(rcp(1.f + __expf(-lo16(rb))))));
    float y0 = 0, y1 = 0;
    if (ffn) {
      u32 xv0, xv1; u64 st;
      if (staged) { xv0 = *(const u16*)(lds + L_XS + 1024 * wid + 2 * tid); xv1 = *(const u16*)(lds + L_XS + 1024 * wid + 2 * tid + 512); st = *(const u64*)(lds + L_GS + 256 * wid); }
      else { xv0 = x[idx * 512 + tid]; xv1 = x[idx * 512 + tid + 256]; st = ~(u64)tag << 32; }
      for (int spin = 0; (u32)(st >> 32) != tag && spin < SPIN; ++spin) st = __hip_atomic_load((gu64*)(ws + WS_G1) + idx, RLX);
      const float mb = lo16((u32)st), sd = rcp(hi16((u32)st));
      const float xa = xnorm(lo16(xv0), mb, sd), xc = xnorm(lo16(xv1), mb, sd);
      const u32x4 g0 = *(const u32x4*)(lds + L_GT + 16 * tid), g1 = *(const u32x4*)(lds + L_GT + 4096 + 16 * tid);
      const u32x4 u0 = *(const u32x4*)(lds + L_UP + 16 * tid), u1 = *(const u32x4*)(lds + L_UP + 4096 + 16 * tid);
      float acc[16];
#pragma unroll
      for (int e = 0; e < 4; ++e) {
        acc[2 * e] = xa * lo16(g0[e]) + xc * lo16(g1[e]); acc[2 * e + 1] = xa * hi16(g0[e]) + xc * hi16(g1[e]);
        acc[8 + 2 * e] = xa * lo16(u0[e]) + xc * lo16(u1[e]); acc[9 + 2 * e] = xa * hi16(u0[e]) + xc * hi16(u1[e]);
      }
#pragma unroll
      for (int e = 0; e < 16; ++e) acc[e] = sum16(acc[e]);
      if ((lane & 15) == 0) {
#pragma unroll
        for (int e = 0; e < 4; ++e) *(f32x4*)(fr + 16 * (tid >> 4) + 4 * e) = f32x4{acc[4 * e], acc[4 * e + 1], acc[4 * e + 2], acc[4 * e + 3]};
      }
      bar();
      float t = fr[lane] + fr[64 + lane] + fr[128 + lane] + fr[192 + lane];
      { auto p = __builtin_amdgcn_permlane16_swap(__float_as_uint(t), __float_as_uint(t), false, false); t = __uint_as_float(p[0]) + __uint_as_float(p[1]);
        auto q = __builtin_amdgcn_permlane32_swap(__float_as_uint(t), __float_as_uint(t), false, false); t = rbf(__uint_as_float(q[0]) + __uint_as_float(q[1])); }
      const float hh = rbf(rbf(silu(t)) * dpp<0x108>(t));
#pragma unroll
      for (int i = 0; i < 8; ++i) {
        const float hv = __uint_as_float(__builtin_amdgcn_readlane(__float_as_uint(hh), i)); const u32 w = *(const u32*)(lds + L_DN + 1024 * i + 4 * tid);
        y0 += hv * lo16(w); y1 += hv * hi16(w);
      }
    }
    if (bx == 0 && tid == 0) __hip_atomic_store((gu32*)(ws + WS_E), tag, RLX);
    if (ffn) {
      gu64* yp = (gu64*)(ws + WS_YP) + fj * 512 + 2 * tid;
      __hip_atomic_store(yp, ((u64)tag << 32) | __float_as_uint(y0), RLX); __hip_atomic_store(yp + 1, ((u64)tag << 32) | __float_as_uint(y1), RLX);
    }
    asm volatile(
    "s_nop 4\n\t"
    "s_load_dwordx16 s[40:55], %[ka], 0x0\n\t"
    "s_load_dwordx4 s[56:59], %[ka], 0x40\n\t"
    "s_waitcnt lgkmcnt(0)\n\t"
    "v_mbcnt_lo_u32_b32 v255, -1, 0\n\t"
    "v_mbcnt_hi_u32_b32 v255, -1, v255\n\t"
    "v_lshlrev_b32_e32 v240, 4, v255\n\t"
    "s_mul_i32 s33, %[h], 524288\n\t"
    "s_mul_i32 s34, %[wid], 32768\n\t"
    "s_add_u32 s33, s33, s34\n\t"
    "s_add_u32 s33, s33, 2097152\n\t"
    "s_add_u32 s76, s58, s33\n\t"
    "s_addc_u32 s77, s59, 0\n\t"
    "s_and_b32 s77, s77, 0xffff\n\t"
    "s_mov_b32 s78, 425984\n\t"
    "s_mov_b32 s79, 0x20000\n\t"
    "v_add_u32_e32 v246, 131072, v240\n\t"
    "v_add_u32_e32 v247, 262144, v240\n\t"
    "v_add_u32_e32 v248, 393216, v240\n\t"
    "s_mul_i32 s33, %[b], 524288\n\t"
    "s_mul_i32 s34, %[wid], 131072\n\t"
    "s_add_u32 s33, s33, s34\n\t"
    "s_add_u32 s33, s33, 18874368\n\t"
    "s_add_u32 s64, s58, s33\n\t"
    "s_addc_u32 s65, s59, 0\n\t"
    "s_and_b32 s65, s65, 0xffff\n\t"
    "s_mov_b32 s66, 131072\n\t"
    "s_mov_b32 s67, 0x20000\n\t"
    "s_add_u32 s60, s58, 0\n\t"
    "s_addc_u32 s61, s59, 0\n\t"
    "s_and_b32 s61, s61, 0xffff\n\t"
    "s_mov_b32 s62, 65536\n\t"
    "s_mov_b32 s63, 0x20000\n\t"
    "s_mul_i32 s33, %[h], 128\n\t"
    "s_mul_i32 s34, %[wid], 32\n\t"
    "s_add_u32 s33, s33, s34\n\t"
    "s_add_u32 s33, s33, 36864\n\t"
    "v_and_b32_e32 v242, 7, v255\n\t"
    "v_lshlrev_b32_e32 v242, 2, v242\n\t"
    "s_mov_b32 s86, 400000\n\t"
    "buffer_load_dword v244, v242, s[60:63], s33 offen sc1\n\t"
    "s_mov_b32 s87, 65536\n\t"
    "buffer_load_dwordx4 v[128:131], v240, s[64:67], s87 offen offset:0 sc1\n\t"
    "buffer_load_dwordx4 v[132:135], v240, s[64:67], s87 offen offset:1024 sc1\n\t"
    "buffer_load_dwordx4 v[136:139], v240, s[64:67], s87 offen offset:2048 sc1\n\t"
    "buffer_load_dwordx4 v[140:143], v240, s[64:67], s87 offen offset:3072 sc1\n\t"
    "s_mov_b32 s88, 69632\n\t"
    "buffer_load_dwordx4 v[144:147], v240, s[64:67], s88 offen offset:0 sc1\n\t"
    "buffer_load_dwordx4 v[148:151], v240, s[64:67], s88 offen offset:1024 sc1\n\t"
    "buffer_load_dwordx4 v[152:155], v240, s[64:67], s88 offen offset:2048 sc1\n\t"
    "buffer_load_dwordx4 v[156:159], v240, s[64:67], s88 offen offset:3072 sc1\n\t"
    "s_mov_b32 s87, 73728\n\t"
    "buffer_load_dwordx4 v[160:163], v240, s[64:67], s87 offen offset:0 sc1\n\t"
    "buffer_load_dwordx4 v[164:167], v240, s[64:67], s87 offen offset:1024 sc1\n\t"
    "buffer_load_dwordx4 v[168:171], v240, s[64:67], s87 offen offset:2048 sc1\n\t"
    "buffer_load_dwordx4 v[172:175], v240, s[64:67], s87 offen offset:3072 sc1\n\t"
    "s_mov_b32 s88, 77824\n\t"
    "buffer_load_dwordx4 v[176:179], v240, s[64:67], s88 offen offset:0 sc1\n\t"
    "buffer_load_dwordx4 v[180:183], v240, s[64:67], s88 offen offset:1024 sc1\n\t"
    "buffer_load_dwordx4 v[184:187], v240, s[64:67], s88 offen offset:2048 sc1\n\t"
    "buffer_load_dwordx4 v[188:191], v240, s[64:67], s88 offen offset:3072 sc1\n\t"
    "s_waitcnt vmcnt(16)\n\t"
    "L_po_%=:\n\t"
    "v_cmp_ne_u32_e32 vcc, %[tag], v244\n\t"
    "s_cbranch_vccz L_go_%=\n\t"
    "s_sub_u32 s86, s86, 1\n\t"
    "s_cbranch_scc1 L_go_%=\n\t"
    "buffer_load_dword v244, v242, s[60:63], s33 offen sc1\n\t"
    "s_waitcnt vmcnt(0)\n\t"
    "s_branch L_po_%=\n\t"
    "L_go_%=:\n\t"
    "s_mov_b32 s84, 0\n\t"
    "buffer_load_dwordx4 v[64:67], v240, s[76:79], s84 offen offset:0 sc1\n\t"
    "buffer_load_dwordx4 v[68:71], v246, s[76:79], s84 offen offset:0 sc1\n\t"
    "buffer_load_dwordx4 v[72:75], v247, s[76:79], s84 offen offset:0 sc1\n\t"
    "buffer_load_dwordx4 v[76:79], v248, s[76:79], s84 offen offset:0 sc1\n\t"
    "buffer_load_dwordx4 v[80:83], v240, s[76:79], s84 offen offset:1024 sc1\n\t"
    "buffer_load_dwordx4 v[84:87], v246, s[76:79], s84 offen offset:1024 sc1\n\t"
    "buffer_load_dwordx4 v[88:91], v247, s[76:79], s84 offen offset:1024 sc1\n\t"
    "buffer_load_dwordx4 v[92:95], v248, s[76:79], s84 offen offset:1024 sc1\n\t"
    "buffer_load_dwordx4 v[96:99], v240, s[76:79], s84 offen offset:2048 sc1\n\t"
    "buffer_load_dwordx4 v[100:103], v246, s[76:79], s84 offen offset:2048 sc1\n\t"
    "buffer_load_dwordx4 v[104:107], v247, s[76:79], s84 offen offset:2048 sc1\n\t"
    "buffer_load_dwordx4 v[108:111], v248, s[76:79], s84 offen offset:2048 sc1\n\t"
    "buffer_load_dwordx4 v[112:115], v240, s[76:79], s84 offen offset:3072 sc1\n\t"
    "buffer_load_dwordx4 v[116:119], v246, s[76:79], s84 offen offset:3072 sc1\n\t"
    "buffer_load_dwordx4 v[120:123], v247, s[76:79], s84 offen offset:3072 sc1\n\t"
    "buffer_load_dwordx4 v[124:127], v248, s[76:79], s84 offen offset:3072 sc1\n\t"
    "s_mov_b32 s84, 4096\n\t"
    "buffer_load_dwordx4 v[192:195], v240, s[76:79], s84 offen offset:0 sc1\n\t"
    "buffer_load_dwordx4 v[196:199], v246, s[76:79], s84 offen offset:0 sc1\n\t"
    "buffer_load_dwordx4 v[200:203], v247, s[76:79], s84 offen offset:0 sc1\n\t"
    "buffer_load_dwordx4 v[204:207], v248, s[76:79], s84 offen offset:0 sc1\n\t"
    :: [wid] "s"(wid), [h] "s"(h), [b] "s"(b), [bx] "s"(bx), [tag] "s"(tag), [ka] "s"(ka)
    : "v64", "v65", "v66", "v67", "v68", "v69", "v70", "v71", "v72", "v73", "v74", "v75", "v76", "v77", "v78", "v79", "v80", "v81", "v82", "v83", "v84", "v85", "v86", "v87", "v88", "v89", "v90", "v91", "v92", "v93", "v94", "v95", "v96", "v97", "v98", "v99", "v100", "v101", "v102", "v103", "v104", "v105", "v106", "v107", "v108", "v109", "v110", "v111", "v112", "v113", "v114", "v115", "v116", "v117", "v118", "v119", "v120", "v121", "v122", "v123", "v124", "v125", "v126", "v127", "v128", "v129", "v130", "v131", "v132", "v133", "v134", "v135", "v136", "v137", "v138", "v139", "v140", "v141", "v142", "v143", "v144", "v145", "v146", "v147", "v148", "v149", "v150", "v151", "v152", "v153", "v154", "v155", "v156", "v157", "v158", "v159", "v160", "v161", "v162", "v163", "v164", "v165", "v166", "v167", "v168", "v169", "v170", "v171", "v172", "v173", "v174", "v175", "v176", "v177", "v178", "v179", "v180", "v181", "v182", "v183", "v184", "v185", "v186", "v187", "v188", "v189", "v190", "v191", "v192", "v193", "v194", "v195", "v196", "v197", "v198", "v199", "v200", "v201", "v202", "v203", "v204", "v205", "v206", "v207", "v208", "v209", "v210", "v211", "v212", "v213", "v214", "v215", "v216", "v217", "v218", "v219", "v220", "v221", "v222", "v223", "v224", "v225", "v226", "v227", "v228", "v229", "v230", "v231", "v232", "v233", "v234", "v235", "v236", "v237", "v238", "v239", "v240", "v241", "v242", "v243", "v244", "v245", "v246", "v247", "v248", "v249", "v250", "v251", "v252", "v253", "v254", "v255", "s33", "s34", "s35", "s36", "s37", "s38", "s39", "s40", "s41", "s42", "s43", "s44", "s45", "s46", "s47", "s48", "s49", "s50", "s51", "s52", "s53", "s54", "s55", "s56", "s57", "s58", "s59", "s60", "s61", "s62", "s63", "s64", "s65", "s66", "s67", "s68", "s69", "s70", "s71", "s72", "s73", "s74", "s75", "s76", "s77", "s78", "s79", "s80", "s81", "s82", "s83", "s84", "s85", "s86", "s87", "s88", "s89", "s90", "s91", "s92", "s93", "s94", "s95", "s96", "s97", "s98", "s99", "vcc", "scc", "memory");
    {
      const u32 q = tid >> 2, n0 = 16 * (tid & 3);
      xa = *(const u32x4*)(lds + L_XT + 128 * q + 2 * n0); xc = *(const u32x4*)(lds + L_XT + 128 * q + 2 * n0 + 16);
      st0 = (u32)granule(lds, L_G1 + 8 * q, ws, WS_G1 + 8 * (64 * h + q), tag);
    }
  }
  idx = __builtin_amdgcn_readfirstlane(idx);
  const u32 own = __builtin_amdgcn_readfirstlane((u32)((idx >> 6) == h));
  {
    asm volatile(
    "s_nop 4\n\t"
    "s_load_dwordx16 s[40:55], %[ka], 0x0\n\t"
    "s_load_dwordx4 s[56:59], %[ka], 0x40\n\t"
    "s_waitcnt lgkmcnt(0)\n\t"
    "v_mbcnt_lo_u32_b32 v255, -1, 0\n\t"
    "v_mbcnt_hi_u32_b32 v255, -1, v255\n\t"
    "v_lshlrev_b32_e32 v240, 4, v255\n\t"
    "s_mul_i32 s33, %[h], 524288\n\t"
    "s_mul_i32 s34, %[wid], 32768\n\t"
    "s_add_u32 s33, s33, s34\n\t"
    "s_add_u32 s33, s33, 2097152\n\t"
    "s_add_u32 s76, s58, s33\n\t"
    "s_addc_u32 s77, s59, 0\n\t"
    "s_and_b32 s77, s77, 0xffff\n\t"
    "s_mov_b32 s78, 425984\n\t"
    "s_mov_b32 s79, 0x20000\n\t"
    "v_add_u32_e32 v246, 131072, v240\n\t"
    "v_add_u32_e32 v247, 262144, v240\n\t"
    "v_add_u32_e32 v248, 393216, v240\n\t"
    "s_mul_i32 s33, %[b], 524288\n\t"
    "s_mul_i32 s34, %[wid], 131072\n\t"
    "s_add_u32 s33, s33, s34\n\t"
    "s_add_u32 s33, s33, 18874368\n\t"
    "s_add_u32 s64, s58, s33\n\t"
    "s_addc_u32 s65, s59, 0\n\t"
    "s_and_b32 s65, s65, 0xffff\n\t"
    "s_mov_b32 s66, 131072\n\t"
    "s_mov_b32 s67, 0x20000\n\t"
    "s_mov_b32 s84, 4096\n\t"
    "s_mul_i32 s33, %[b], 512\n\t"
    "s_mul_i32 s34, %[wid], 8192\n\t"
    "s_add_u32 s33, s33, s34\n\t"
    "s_add_u32 s33, s33, 65536\n\t"
    "s_add_u32 s68, s58, s33\n\t"
    "s_addc_u32 s69, s59, 0\n\t"
    "s_and_b32 s69, s69, 0xffff\n\t"
    "s_mov_b32 s70, 0\n\t"
    "s_mov_b32 s71, 0x20000\n\t"
    "s_mul_i32 s70, %[b], -512\n\t"
    "s_mul_i32 s34, %[wid], -8192\n\t"
    "s_add_u32 s70, s70, s34\n\t"
    "s_add_u32 s70, s70, 704512\n\t"
    "v_and_b32_e32 v241, 15, v255\n\t"
    "v_lshrrev_b32_e32 v245, 4, v255\n\t"
    "v_xor_b32_e32 v245, v245, v241\n\t"
    "v_lshlrev_b32_e32 v245, 4, v245\n\t"
    "v_lshl_add_u32 v241, v241, 8, v245\n\t"
    "s_lshl_b32 s86, %[wid], 14\n\t"
    "s_add_u32 s86, s86, 0x18000\n\t"
    "v_add_u32_e32 v241, s86, v241\n\t"
    "v_lshrrev_b32_e32 v245, 5, v255\n\t"
    "v_and_b32_e32 v243, 31, v255\n\t"
    "v_lshlrev_b32_e32 v243, 4, v243\n\t"
    "v_lshl_add_u32 v243, v245, 12, v243\n\t"
    "s_lshl_b32 s96, %[wid], 10\n\t"
    "s_waitcnt vmcnt(16)\n\t"
    "v_mfma_f32_16x16x32_bf16 v[0:3], a[128:131], v[64:67], 0\n\t"
    "v_mfma_f32_16x16x32_bf16 v[4:7], a[128:131], v[68:71], 0\n\t"
    "v_mfma_f32_16x16x32_bf16 v[8:11], a[128:131], v[72:75], 0\n\t"
    "v_mfma_f32_16x16x32_bf16 v[12:15], a[128:131], v[76:79], 0\n\t"
    "v_mfma_f32_16x16x32_bf16 v[16:19], a[132:135], v[64:67], 0\n\t"
    "v_mfma_f32_16x16x32_bf16 v[20:23], a[132:135], v[68:71], 0\n\t"
    "v_mfma_f32_16x16x32_bf16 v[24:27], a[132:135], v[72:75], 0\n\t"
    "v_mfma_f32_16x16x32_bf16 v[28:31], a[132:135], v[76:79], 0\n\t"
    "v_mfma_f32_16x16x32_bf16 v[32:35], a[136:139], v[64:67], 0\n\t"
    "v_mfma_f32_16x16x32_bf16 v[36:39], a[136:139], v[68:71], 0\n\t"
    "v_mfma_f32_16x16x32_bf16 v[40:43], a[136:139], v[72:75], 0\n\t"
    "v_mfma_f32_16x16x32_bf16 v[44:47], a[136:139], v[76:79], 0\n\t"
    "v_mfma_f32_16x16x32_bf16 v[48:51], a[140:143], v[64:67], 0\n\t"
    "v_mfma_f32_16x16x32_bf16 v[52:55], a[140:143], v[68:71], 0\n\t"
    "v_mfma_f32_16x16x32_bf16 v[56:59], a[140:143], v[72:75], 0\n\t"
    "v_mfma_f32_16x16x32_bf16 v[60:63], a[140:143], v[76:79], 0\n\t"
    "s_waitcnt vmcnt(12)\n\t"
    "v_mfma_f32_16x16x32_bf16 v[0:3], a[144:147], v[80:83], v[0:3]\n\t"
    "buffer_load_dwordx4 v[208:211], v240, s[76:79], s84 offen offset:1024 sc1\n\t"
    "v_mfma_f32_16x16x32_bf16 v[4:7], a[144:147], v[84:87], v[4:7]\n\t"
    "v_mfma_f32_16x16x32_bf16 v[8:11], a[144:147], v[88:91], v[8:11]\n\t"
    "buffer_load_dwordx4 v[212:215], v246, s[76:79], s84 offen offset:1024 sc1\n\t"
    "v_mfma_f32_16x16x32_bf16 v[12:15], a[144:147], v[92:95], v[12:15]\n\t"
    "v_mfma_f32_16x16x32_bf16 v[16:19], a[148:151], v[80:83], v[16:19]\n\t"
    "buffer_load_dwordx4 v[216:219], v247, s[76:79], s84 offen offset:1024 sc1\n\t"
    "v_mfma_f32_16x16x32_bf16 v[20:23], a[148:151], v[84:87], v[20:23]\n\t"
    "v_mfma_f32_16x16x32_bf16 v[24:27], a[148:151], v[88:91], v[24:27]\n\t"
    "buffer_load_dwordx4 v[220:223], v248, s[76:79], s84 offen offset:1024 sc1\n\t"
    "v_mfma_f32_16x16x32_bf16 v[28:31], a[148:151], v[92:95], v[28:31]\n\t"
    "v_mfma_f32_16x16x32_bf16 v[32:35], a[152:155], v[80:83], v[32:35]\n\t"
    "s_mov_b32 s87, 81920\n\t"
    "buffer_load_dwordx4 a[128:131], v240, s[64:67], s87 offen offset:0 sc1\n\t"
    "v_mfma_f32_16x16x32_bf16 v[36:39], a[152:155], v[84:87], v[36:39]\n\t"
    "v_mfma_f32_16x16x32_bf16 v[40:43], a[152:155], v[88:91], v[40:43]\n\t"
    "buffer_load_dwordx4 a[132:135], v240, s[64:67], s87 offen offset:1024 sc1\n\t"
    "v_mfma_f32_16x16x32_bf16 v[44:47], a[152:155], v[92:95], v[44:47]\n\t"
    "v_mfma_f32_16x16x32_bf16 v[48:51], a[156:159], v[80:83], v[48:51]\n\t"
    "buffer_load_dwordx4 a[136:139], v240, s[64:67], s87 offen offset:2048 sc1\n\t"
    "v_mfma_f32_16x16x32_bf16 v[52:55], a[156:159], v[84:87], v[52:55]\n\t"
    "v_mfma_f32_16x16x32_bf16 v[56:59], a[156:159], v[88:91], v[56:59]\n\t"
    "buffer_load_dwordx4 a[140:143], v240, s[64:67], s87 offen offset:3072 sc1\n\t"
    "v_mfma_f32_16x16x32_bf16 v[60:63], a[156:159], v[92:95], v[60:63]\n\t"
    "s_waitcnt vmcnt(16)\n\t"
    "v_mfma_f32_16x16x32_bf16 v[0:3], a[160:163], v[96:99], v[0:3]\n\t"
    "buffer_load_dwordx4 v[64:67], v240, s[76:79], s84 offen offset:2048 sc1\n\t"
    "v_lshlrev_b32_e32 v242, 16, %[st]\n\t"
    "v_mfma_f32_16x16x32_bf16 v[4:7], a[160:163], v[100:103], v[4:7]\n\t"
    "v_and_b32_e32 v244, 0xffff0000, %[st]\n\t"
    "v_mfma_f32_16x16x32_bf16 v[8:11], a[160:163], v[104:107], v[8:11]\n\t"
    "buffer_load_dwordx4 v[68:71], v246, s[76:79], s84 offen offset:2048 sc1\n\t"
    "v_rcp_f32_e32 v244, v244\n\t"
    "v_mfma_f32_16x16x32_bf16 v[12:15], a[160:163], v[108:111], v[12:15]\n\t"
    "v_lshlrev_b32_e32 v251, 16, v224\n\t"
    "v_mfma_f32_16x16x32_bf16 v[16:19], a[164:167], v[96:99], v[16:19]\n\t"
    "buffer_load_dwordx4 v[72:75], v247, s[76:79], s84 offen offset:2048 sc1\n\t"
    "v_and_b32_e32 v245, 0xffff0000, v224\n\t"
    "v_mfma_f32_16x16x32_bf16 v[20:23], a[164:167], v[100:103], v[20:23]\n\t"
    "v_sub_f32_e32 v251, v251, v242\n\t"
    "v_mfma_f32_16x16x32_bf16 v[24:27], a[164:167], v[104:107], v[24:27]\n\t"
    "buffer_load_dwordx4 v[76:79], v248, s[76:79], s84 offen offset:2048 sc1\n\t"
    "v_sub_f32_e32 v245, v245, v242\n\t"
    "v_mfma_f32_16x16x32_bf16 v[28:31], a[164:167], v[108:111], v[28:31]\n\t"
    "v_cvt_pk_bf16_f32 v245, v251, v245\n\t"
    "v_mfma_f32_16x16x32_bf16 v[32:35], a[168:171], v[96:99], v[32:35]\n\t"
    "s_mov_b32 s88, 86016\n\t"
    "buffer_load_dwordx4 a[144:147], v240, s[64:67], s88 offen offset:0 sc1\n\t"
    "v_lshlrev_b32_e32 v251, 16, v245\n\t"
    "v_mfma_f32_16x16x32_bf16 v[36:39], a[168:171], v[100:103], v[36:39]\n\t"
    "v_and_b32_e32 v245, 0xffff0000, v245\n\t"
    "v_mfma_f32_16x16x32_bf16 v[40:43], a[168:171], v[104:107], v[40:43]\n\t"
    "buffer_load_dwordx4 a[148:151], v240, s[64:67], s88 offen offset:1024 sc1\n\t"
    "v_mul_f32_e32 v251, v251, v244\n\t"
    "v_mfma_f32_16x16x32_bf16 v[44:47], a[168:171], v[108:111], v[44:47]\n\t"
    "v_mul_f32_e32 v245, v245, v244\n\t"
    "v_mfma_f32_16x16x32_bf16 v[48:51], a[172:175], v[96:99], v[48:51]\n\t"
    "buffer_load_dwordx4 a[152:155], v240, s[64:67], s88 offen offset:2048 sc1\n\t"
    "v_cvt_pk_bf16_f32 v245, v251, v245\n\t"
    "v_mfma_f32_16x16x32_bf16 v[52:55], a[172:175], v[100:103], v[52:55]\n\t"
    "v_lshlrev_b32_e32 v251, 16, v245\n\t"
    "v_mfma_f32_16x16x32_bf16 v[56:59], a[172:175], v[104:107], v[56:59]\n\t"
    "buffer_load_dwordx4 a[156:159], v240, s[64:67], s88 offen offset:3072 sc1\n\t"
    "v_and_b32_e32 v245, 0xffff0000, v245\n\t"
    "v_mfma_f32_16x16x32_bf16 v[60:63], a[172:175], v[108:111], v[60:63]\n\t"
    "v_mul_f32_e32 v252, 0xbfb8aa3b, v251\n\t"
    "s_waitcnt vmcnt(20)\n\t"
    "v_mfma_f32_16x16x32_bf16 v[0:3], a[176:179], v[112:115], v[0:3]\n\t"
    "buffer_load_dwordx4 v[80:83], v240, s[76:79], s84 offen offset:3072 sc1\n\t"
    "v_mul_f32_e32 v253, 0xbfb8aa3b, v245\n\t"
    "v_mfma_f32_16x16x32_bf16 v[4:7], a[176:179], v[116:119], v[4:7]\n\t"
    "v_exp_f32_e32 v252, v252\n\t"
    "v_mfma_f32_16x16x32_bf16 v[8:11], a[176:179], v[120:123], v[8:11]\n\t"
    "buffer_load_dwordx4 v[84:87], v246, s[76:79], s84 offen offset:3072 sc1\n\t"
    "v_exp_f32_e32 v253, v253\n\t"
    "v_mfma_f32_16x16x32_bf16 v[12:15], a[176:179], v[124:127], v[12:15]\n\t"
    "s_nop 0\n\t"
    "v_mfma_f32_16x16x32_bf16 v[16:19], a[180:183], v[112:115], v[16:19]\n\t"
    "buffer_load_dwordx4 v[88:91], v247, s[76:79], s84 offen offset:3072 sc1\n\t"
    "v_add_f32_e32 v252, 1.0, v252\n\t"
    "v_mfma_f32_16x16x32_bf16 v[20:23], a[180:183], v[116:119], v[20:23]\n\t"
    "v_add_f32_e32 v253, 1.0, v253\n\t"
    "v_mfma_f32_16x16x32_bf16 v[24:27], a[180:183], v[120:123], v[24:27]\n\t"
    "buffer_load_dwordx4 v[92:95], v248, s[76:79], s84 offen offset:3072 sc1\n\t"
    "v_rcp_f32_e32 v252, v252\n\t"
    "v_mfma_f32_16x16x32_bf16 v[28:31], a[180:183], v[124:127], v[28:31]\n\t"
    "v_rcp_f32_e32 v253, v253\n\t"
    "v_mfma_f32_16x16x32_bf16 v[32:35], a[184:187], v[112:115], v[32:35]\n\t"
    "s_mov_b32 s87, 90112\n\t"
    "buffer_load_dwordx4 a[160:163], v240, s[64:67], s87 offen offset:0 sc1\n\t"
    "s_nop 0\n\t"
    "v_mfma_f32_16x16x32_bf16 v[36:39], a[184:187], v[116:119], v[36:39]\n\t"
    "v_mul_f32_e32 v251, v251, v252\n\t"
    "v_mfma_f32_16x16x32_bf16 v[40:43], a[184:187], v[120:123], v[40:43]\n\t"
    "buffer_load_dwordx4 a[164:167], v240, s[64:67], s87 offen offset:1024 sc1\n\t"
    "v_mul_f32_e32 v245, v245, v253\n\t"
    "v_mfma_f32_16x16x32_bf16 v[44:47], a[184:187], v[124:127], v[44:47]\n\t"
    "v_cvt_pk_bf16_f32 v232, v251, v245\n\t"
    "v_mfma_f32_16x16x32_bf16 v[48:51], a[188:191], v[112:115], v[48:51]\n\t"
    "buffer_load_dwordx4 a[168:171], v240, s[64:67], s87 offen offset:2048 sc1\n\t"
    "v_lshlrev_b32_e32 v249, 16, v225\n\t"
    "v_mfma_f32_16x16x32_bf16 v[52:55], a[188:191], v[116:119], v[52:55]\n\t"
    "v_and_b32_e32 v250, 0xffff0000, v225\n\t"
    "v_mfma_f32_16x16x32_bf16 v[56:59], a[188:191], v[120:123], v[56:59]\n\t"
    "buffer_load_dwordx4 a[172:175], v240, s[64:67], s87 offen offset:3072 sc1\n\t"
    "v_sub_f32_e32 v249, v249, v242\n\t"
    "v_mfma_f32_16x16x32_bf16 v[60:63], a[188:191], v[124:127], v[60:63]\n\t"
    "v_sub_f32_e32 v250, v250, v242\n\t"
    "s_waitcnt vmcnt(24)\n\t"
    "v_mfma_f32_16x16x32_bf16 v[0:3], a[192:195], v[192:195], v[0:3]\n\t"
    "s_mov_b32 s84, 8192\n\t"
    "buffer_load_dwordx4 v[96:99], v240, s[76:79], s84 offen offset:0 sc1\n\t"
    "v_cvt_pk_bf16_f32 v250, v249, v250\n\t"
    "v_mfma_f32_16x16x32_bf16 v[4:7], a[192:195], v[196:199], v[4:7]\n\t"
    "v_lshlrev_b32_e32 v249, 16, v250\n\t"
    "v_mfma_f32_16x16x32_bf16 v[8:11], a[192:195], v[200:203], v[8:11]\n\t"
    "buffer_load_dwordx4 v[100:103], v246, s[76:79], s84 offen offset:0 sc1\n\t"
    "v_and_b32_e32 v250, 0xffff0000, v250\n\t"
    "v_mfma_f32_16x16x32_bf16 v[12:15], a[192:195], v[204:207], v[12:15]\n\t"
    "v_mul_f32_e32 v249, v249, v244\n\t"
    "v_mfma_f32_16x16x32_bf16 v[16:19], a[196:199], v[192:195], v[16:19]\n\t"
    "buffer_load_dwordx4 v[104:107], v247, s[76:79], s84 offen offset:0 sc1\n\t"
    "v_mul_f32_e32 v250, v250, v244\n\t"
    "v_mfma_f32_16x16x32_bf16 v[20:23], a[196:199], v[196:199], v[20:23]\n\t"
    "v_cvt_pk_bf16_f32 v250, v249, v250\n\t"
    "v_mfma_f32_16x16x32_bf16 v[24:27], a[196:199], v[200:203], v[24:27]\n\t"
    "buffer_load_dwordx4 v[108:111], v248, s[76:79], s84 offen offset:0 sc1\n\t"
    "v_lshlrev_b32_e32 v249, 16, v250\n\t"
    "v_mfma_f32_16x16x32_bf16 v[28:31], a[196:199], v[204:207], v[28:31]\n\t"
    "v_and_b32_e32 v250, 0xffff0000, v250\n\t"
    "v_mfma_f32_16x16x32_bf16 v[32:35], a[200:203], v[192:195], v[32:35]\n\t"
    "s_mov_b32 s88, 94208\n\t"
    "buffer_load_dwordx4 a[176:179], v240, s[64:67], s88 offen offset:0 sc1\n\t"
    "v_mul_f32_e32 v252, 0xbfb8aa3b, v249\n\t"
    "v_mfma_f32_16x16x32_bf16 v[36:39], a[200:203], v[196:199], v[36:39]\n\t"
    "v_mul_f32_e32 v253, 0xbfb8aa3b, v250\n\t"
    "v_mfma_f32_16x16x32_bf16 v[40:43], a[200:203], v[200:203], v[40:43]\n\t"
    "buffer_load_dwordx4 a[180:183], v240, s[64:67], s88 offen offset:1024 sc1\n\t"
    "v_exp_f32_e32 v252, v252\n\t"
    "v_mfma_f32_16x16x32_bf16 v[44:47], a[200:203], v[204:207], v[44:47]\n\t"
    "v_exp_f32_e32 v253, v253\n\t"
    "v_mfma_f32_16x16x32_bf16 v[48:51], a[204:207], v[192:195], v[48:51]\n\t"
    "buffer_load_dwordx4 a[184:187], v240, s[64:67], s88 offen offset:2048 sc1\n\t"
    "s_nop 0\n\t"
    "v_mfma_f32_16x16x32_bf16 v[52:55], a[204:207], v[196:199], v[52:55]\n\t"
    "v_add_f32_e32 v252, 1.0, v252\n\t"
    "v_mfma_f32_16x16x32_bf16 v[56:59], a[204:207], v[200:203], v[56:59]\n\t"
    "buffer_load_dwordx4 a[188:191], v240, s[64:67], s88 offen offset:3072 sc1\n\t"
    "v_add_f32_e32 v253, 1.0, v253\n\t"
    "v_mfma_f32_16x16x32_bf16 v[60:63], a[204:207], v[204:207], v[60:63]\n\t"
    "v_rcp_f32_e32 v252, v252\n\t"
    "s_waitcnt vmcnt(28)\n\t"
    "v_mfma_f32_16x16x32_bf16 v[0:3], a[208:211], v[208:211], v[0:3]\n\t"
    "buffer_load_dwordx4 v[112:115], v240, s[76:79], s84 offen offset:1024 sc1\n\t"
    "v_rcp_f32_e32 v253, v253\n\t"
    "v_mfma_f32_16x16x32_bf16 v[4:7], a[208:211], v[212:215], v[4:7]\n\t"
    "s_nop 0\n\t"
    "v_mfma_f32_16x16x32_bf16 v[8:11], a[208:211], v[216:219], v[8:11]\n\t"
    "buffer_load_dwordx4 v[116:119], v246, s[76:79], s84 offen offset:1024 sc1\n\t"
    "v_mul_f32_e32 v249, v249, v252\n\t"
    "v_mfma_f32_16x16x32_bf16 v[12:15], a[208:211], v[220:223], v[12:15]\n\t"
    "v_mul_f32_e32 v250, v250, v253\n\t"
    "v_mfma_f32_16x16x32_bf16 v[16:19], a[212:215], v[208:211], v[16:19]\n\t"
    "buffer_load_dwordx4 v[120:123], v247, s[76:79], s84 offen offset:1024 sc1\n\t"
    "v_cvt_pk_bf16_f32 v233, v249, v250\n\t"
    "v_mfma_f32_16x16x32_bf16 v[20:23], a[212:215], v[212:215], v[20:23]\n\t"
    "v_lshlrev_b32_e32 v251, 16, v226\n\t"
    "v_mfma_f32_16x16x32_bf16 v[24:27], a[212:215], v[216:219], v[24:27]\n\t"
    "buffer_load_dwordx4 v[124:127], v248, s[76:79], s84 offen offset:1024 sc1\n\t"
    "v_and_b32_e32 v245, 0xffff0000, v226\n\t"
    "v_mfma_f32_16x16x32_bf16 v[28:31], a[212:215], v[220:223], v[28:31]\n\t"
    "v_sub_f32_e32 v251, v251, v242\n\t"
    "v_mfma_f32_16x16x32_bf16 v[32:35], a[216:219], v[208:211], v[32:35]\n\t"
    "s_mov_b32 s87, 98304\n\t"
    "buffer_load_dwordx4 a[192:195], v240, s[64:67], s87 offen offset:0 sc1\n\t"
    "v_sub_f32_e32 v245, v245, v242\n\t"
    "v_mfma_f32_16x16x32_bf16 v[36:39], a[216:219], v[212:215], v[36:39]\n\t"
    "v_cvt_pk_bf16_f32 v245, v251, v245\n\t"
    "v_mfma_f32_16x16x32_bf16 v[40:43], a[216:219], v[216:219], v[40:43]\n\t"
    "buffer_load_dwordx4 a[196:199], v240, s[64:67], s87 offen offset:1024 sc1\n\t"
    "v_lshlrev_b32_e32 v251, 16, v245\n\t"
    "v_mfma_f32_16x16x32_bf16 v[44:47], a[216:219], v[220:223], v[44:47]\n\t"
    "v_and_b32_e32 v245, 0xffff0000, v245\n\t"
    "v_mfma_f32_16x16x32_bf16 v[48:51], a[220:223], v[208:211], v[48:51]\n\t"
    "buffer_load_dwordx4 a[200:203], v240, s[64:67], s87 offen offset:2048 sc1\n\t"
    "v_mul_f32_e32 v251, v251, v244\n\t"
    "v_mfma_f32_16x16x32_bf16 v[52:55], a[220:223], v[212:215], v[52:55]\n\t"
    "v_mul_f32_e32 v245, v245, v244\n\t"
    "v_mfma_f32_16x16x32_bf16 v[56:59], a[220:223], v[216:219], v[56:59]\n\t"
    "buffer_load_dwordx4 a[204:207], v240, s[64:67], s87 offen offset:3072 sc1\n\t"
    "v_cvt_pk_bf16_f32 v245, v251, v245\n\t"
    "v_mfma_f32_16x16x32_bf16 v[60:63], a[220:223], v[220:223], v[60:63]\n\t"
    "v_lshlrev_b32_e32 v251, 16, v245\n\t"
    "s_waitcnt vmcnt(28)\n\t"
    "v_mfma_f32_16x16x32_bf16 v[0:3], a[224:227], v[64:67], v[0:3]\n\t"
    "buffer_load_dwordx4 v[192:195], v240, s[76:79], s84 offen offset:2048 sc1\n\t"
    "v_and_b32_e32 v245, 0xffff0000, v245\n\t"
    "v_mfma_f32_16x16x32_bf16 v[4:7], a[224:227], v[68:71], v[4:7]\n\t"
    "v_mul_f32_e32 v252, 0xbfb8aa3b, v251\n\t"
    "v_mfma_f32_16x16x32_bf16 v[8:11], a[224:227], v[72:75], v[8:11]\n\t"
    "buffer_load_dwordx4 v[196:199], v246, s[76:79], s84 offen offset:2048 sc1\n\t"
    "v_mul_f32_e32 v253, 0xbfb8aa3b, v245\n\t"
    "v_mfma_f32_16x16x32_bf16 v[12:15], a[224:227], v[76:79], v[12:15]\n\t"
    "v_exp_f32_e32 v252, v252\n\t"
    "v_mfma_f32_16x16x32_bf16 v[16:19], a[228:231], v[64:67], v[16:19]\n\t"
    "buffer_load_dwordx4 v[200:203], v247, s[76:79], s84 offen offset:2048 sc1\n\t"
    "v_exp_f32_e32 v253, v253\n\t"
    "v_mfma_f32_16x16x32_bf16 v[20:23], a[228:231], v[68:71], v[20:23]\n\t"
    "s_nop 0\n\t"
    "v_mfma_f32_16x16x32_bf16 v[24:27], a[228:231], v[72:75], v[24:27]\n\t"
    "buffer_load_dwordx4 v[204:207], v248, s[76:79], s84 offen offset:2048 sc1\n\t"
    "v_add_f32_e32 v252, 1.0, v252\n\t"
    "v_mfma_f32_16x16x32_bf16 v[28:31], a[228:231], v[76:79], v[28:31]\n\t"
    "v_add_f32_e32 v253, 1.0, v253\n\t"
    "v_mfma_f32_16x16x32_bf16 v[32:35], a[232:235], v[64:67], v[32:35]\n\t"
    "s_mov_b32 s88, 102400\n\t"
    "buffer_load_dwordx4 a[208:211], v240, s[64:67], s88 offen offset:0 sc1\n\t"
    "v_rcp_f32_e32 v252, v252\n\t"
    "v_mfma_f32_16x16x32_bf16 v[36:39], a[232:235], v[68:71], v[36:39]\n\t"
    "v_rcp_f32_e32 v253, v253\n\t"
    "v_mfma_f32_16x16x32_bf16 v[40:43], a[232:235], v[72:75], v[40:43]\n\t"
    "buffer_load_dwordx4 a[212:215], v240, s[64:67], s88 offen offset:1024 sc1\n\t"
    "s_nop 0\n\t"
    "v_mfma_f32_16x16x32_bf16 v[44:47], a[232:235], v[76:79], v[44:47]\n\t"
    "v_mul_f32_e32 v251, v251, v252\n\t"
    "v_mfma_f32_16x16x32_bf16 v[48:51], a[236:239], v[64:67], v[48:51]\n\t"
    "buffer_load_dwordx4 a[216:219], v240, s[64:67], s88 offen offset:2048 sc1\n\t"
    "v_mul_f32_e32 v245, v245, v253\n\t"
    "v_mfma_f32_16x16x32_bf16 v[52:55], a[236:239], v[68:71], v[52:55]\n\t"
    "v_cvt_pk_bf16_f32 v234, v251, v245\n\t"
    "v_mfma_f32_16x16x32_bf16 v[56:59], a[236:239], v[72:75], v[56:59]\n\t"
    "buffer_load_dwordx4 a[220:223], v240, s[64:67], s88 offen offset:3072 sc1\n\t"
    "v_lshlrev_b32_e32 v249, 16, v227\n\t"
    "v_mfma_f32_16x16x32_bf16 v[60:63], a[236:239], v[76:79], v[60:63]\n\t"
    "v_and_b32_e32 v250, 0xffff0000, v227\n\t"
    "s_waitcnt vmcnt(28)\n\t"
    "v_mfma_f32_16x16x32_bf16 v[0:3], a[240:243], v[80:83], v[0:3]\n\t"
    "buffer_load_dwordx4 v[208:211], v240, s[76:79], s84 offen offset:3072 sc1\n\t"
    "v_sub_f32_e32 v249, v249, v242\n\t"
    "v_mfma_f32_16x16x32_bf16 v[4:7], a[240:243], v[84:87], v[4:7]\n\t"
    "v_sub_f32_e32 v250, v250, v242\n\t"
    "v_mfma_f32_16x16x32_bf16 v[8:11], a[240:243], v[88:91], v[8:11]\n\t"
    "buffer_load_dwordx4 v[212:215], v246, s[76:79], s84 offen offset:3072 sc1\n\t"
    "v_cvt_pk_bf16_f32 v250, v249, v250\n\t"
    "v_mfma_f32_16x16x32_bf16 v[12:15], a[240:243], v[92:95], v[12:15]\n\t"
    "v_lshlrev_b32_e32 v249, 16, v250\n\t"
    "v_mfma_f32_16x16x32_bf16 v[16:19], a[244:247], v[80:83], v[16:19]\n\t"
    "buffer_load_dwordx4 v[216:219], v247, s[76:79], s84 offen offset:3072 sc1\n\t"
    "v_and_b32_e32 v250, 0xffff0000, v250\n\t"
    "v_mfma_f32_16x16x32_bf16 v[20:23], a[244:247], v[84:87], v[20:23]\n\t"
    "v_mul_f32_e32 v249, v249, v244\n\t"
    "v_mfma_f32_16x16x32_bf16 v[24:27], a[244:247], v[88:91], v[24:27]\n\t"
    "buffer_load_dwordx4 v[220:223], v248, s[76:79], s84 offen offset:3072 sc1\n\t"
    "v_mul_f32_e32 v250, v250, v244\n\t"
    "v_mfma_f32_16x16x32_bf16 v[28:31], a[244:247], v[92:95], v[28:31]\n\t"
    "v_cvt_pk_bf16_f32 v250, v249, v250\n\t"
    "v_mfma_f32_16x16x32_bf16 v[32:35], a[248:251], v[80:83], v[32:35]\n\t"
    "s_mov_b32 s87, 106496\n\t"
    "buffer_load_dwordx4 a[224:227], v240, s[64:67], s87 offen offset:0 sc1\n\t"
    "v_lshlrev_b32_e32 v249, 16, v250\n\t"
    "v_mfma_f32_16x16x32_bf16 v[36:39], a[248:251], v[84:87], v[36:39]\n\t"
    "v_and_b32_e32 v250, 0xffff0000, v250\n\t"
    "v_mfma_f32_16x16x32_bf16 v[40:43], a[248:251], v[88:91], v[40:43]\n\t"
    "buffer_load_dwordx4 a[228:231], v240, s[64:67], s87 offen offset:1024 sc1\n\t"
    "v_mul_f32_e32 v252, 0xbfb8aa3b, v249\n\t"
    "v_mfma_f32_16x16x32_bf16 v[44:47], a[248:251], v[92:95], v[44:47]\n\t"
    "v_mul_f32_e32 v253, 0xbfb8aa3b, v250\n\t"
    "v_mfma_f32_16x16x32_bf16 v[48:51], a[252:255], v[80:83], v[48:51]\n\t"
    "buffer_load_dwordx4 a[232:235], v240, s[64:67], s87 offen offset:2048 sc1\n\t"
    "v_exp_f32_e32 v252, v252\n\t"
    "v_mfma_f32_16x16x32_bf16 v[52:55], a[252:255], v[84:87], v[52:55]\n\t"
    "v_exp_f32_e32 v253, v253\n\t"
    "v_mfma_f32_16x16x32_bf16 v[56:59], a[252:255], v[88:91], v[56:59]\n\t"
    "buffer_load_dwordx4 a[236:239], v240, s[64:67], s87 offen offset:3072 sc1\n\t"
    "s_nop 0\n\t"
    "v_mfma_f32_16x16x32_bf16 v[60:63], a[252:255], v[92:95], v[60:63]\n\t"
    "v_add_f32_e32 v252, 1.0, v252\n\t"
    "s_waitcnt vmcnt(28)\n\t"
    "v_mfma_f32_16x16x32_bf16 v[0:3], a[0:3], v[96:99], v[0:3]\n\t"
    "s_mov_b32 s84, 12288\n\t"
    "buffer_load_dwordx4 v[64:67], v240, s[76:79], s84 offen offset:0 sc1\n\t"
    "v_add_f32_e32 v253, 1.0, v253\n\t"
    "v_mfma_f32_16x16x32_bf16 v[4:7], a[0:3], v[100:103], v[4:7]\n\t"
    "v_rcp_f32_e32 v252, v252\n\t"
    "v_mfma_f32_16x16x32_bf16 v[8:11], a[0:3], v[104:107], v[8:11]\n\t"
    "buffer_load_dwordx4 v[68:71], v246, s[76:79], s84 offen offset:0 sc1\n\t"
    "v_rcp_f32_e32 v253, v253\n\t"
    "v_mfma_f32_16x16x32_bf16 v[12:15], a[0:3], v[108:111], v[12:15]\n\t"
    "s_nop 0\n\t"
    "v_mfma_f32_16x16x32_bf16 v[16:19], a[4:7], v[96:99], v[16:19]\n\t"
    "buffer_load_dwordx4 v[72:75], v247, s[76:79], s84 offen offset:0 sc1\n\t"
    "v_mul_f32_e32 v249, v249, v252\n\t"
    "v_mfma_f32_16x16x32_bf16 v[20:23], a[4:7], v[100:103], v[20:23]\n\t"
    "v_mul_f32_e32 v250, v250, v253\n\t"
    "v_mfma_f32_16x16x32_bf16 v[24:27], a[4:7], v[104:107], v[24:27]\n\t"
    "buffer_load_dwordx4 v[76:79], v248, s[76:79], s84 offen offset:0 sc1\n\t"
    "v_cvt_pk_bf16_f32 v235, v249, v250\n\t"
    "v_mfma_f32_16x16x32_bf16 v[28:31], a[4:7], v[108:111], v[28:31]\n\t"
    "v_lshlrev_b32_e32 v251, 16, v228\n\t"
    "v_mfma_f32_16x16x32_bf16 v[32:35], a[8:11], v[96:99], v[32:35]\n\t"
    "s_mov_b32 s88, 110592\n\t"
    "buffer_load_dwordx4 a[240:243], v240, s[64:67], s88 offen offset:0 sc1\n\t"
    "v_and_b32_e32 v245, 0xffff0000, v228\n\t"
    "v_mfma_f32_16x16x32_bf16 v[36:39], a[8:11], v[100:103], v[36:39]\n\t"
    "v_sub_f32_e32 v251, v251, v242\n\t"
    "v_mfma_f32_16x16x32_bf16 v[40:43], a[8:11], v[104:107], v[40:43]\n\t"
    "buffer_load_dwordx4 a[244:247], v240, s[64:67], s88 offen offset:1024 sc1\n\t"
    "v_sub_f32_e32 v245, v245, v242\n\t"
    "v_mfma_f32_16x16x32_bf16 v[44:47], a[8:11], v[108:111], v[44:47]\n\t"
    "v_cvt_pk_bf16_f32 v245, v251, v245\n\t"
    "v_mfma_f32_16x16x32_bf16 v[48:51], a[12:15], v[96:99], v[48:51]\n\t"
    "buffer_load_dwordx4 a[248:251], v240, s[64:67], s88 offen offset:2048 sc1\n\t"
    "v_lshlrev_b32_e32 v251, 16, v245\n\t"
    "v_mfma_f32_16x16x32_bf16 v[52:55], a[12:15], v[100:103], v[52:55]\n\t"
    "v_and_b32_e32 v245, 0xffff0000, v245\n\t"
    "v_mfma_f32_16x16x32_bf16 v[56:59], a[12:15], v[104:107], v[56:59]\n\t"
    "buffer_load_dwordx4 a[252:255], v240, s[64:67], s88 offen offset:3072 sc1\n\t"
    "v_mul_f32_e32 v251, v251, v244\n\t"
    "v_mfma_f32_16x16x32_bf16 v[60:63], a[12:15], v[108:111], v[60:63]\n\t"
    "v_mul_f32_e32 v245, v245, v244\n\t"
    "s_waitcnt vmcnt(28)\n\t"
    "v_mfma_f32_16x16x32_bf16 v[0:3], a[16:19], v[112:115], v[0:3]\n\t"
    "buffer_load_dwordx4 v[80:83], v240, s[76:79], s84 offen offset:1024 sc1\n\t"
    "v_cvt_pk_bf16_f32 v245, v251, v245\n\t"
    "v_mfma_f32_16x16x32_bf16 v[4:7], a[16:19], v[116:119], v[4:7]\n\t"
    "v_lshlrev_b32_e32 v251, 16, v245\n\t"
    "v_mfma_f32_16x16x32_bf16 v[8:11], a[16:19], v[120:123], v[8:11]\n\t"
    "buffer_load_dwordx4 v[84:87], v246, s[76:79], s84 offen offset:1024 sc1\n\t"
    "v_and_b32_e32 v245, 0xffff0000, v245\n\t"
    "v_mfma_f32_16x16x32_bf16 v[12:15], a[16:19], v[124:127], v[12:15]\n\t"
    "v_mul_f32_e32 v252, 0xbfb8aa3b, v251\n\t"
    "v_mfma_f32_16x16x32_bf16 v[16:19], a[20:23], v[112:115], v[16:19]\n\t"
    "buffer_load_dwordx4 v[88:91], v247, s[76:79], s84 offen offset:1024 sc1\n\t"
    "v_mul_f32_e32 v253, 0xbfb8aa3b, v245\n\t"
    "v_mfma_f32_16x16x32_bf16 v[20:23], a[20:23], v[116:119], v[20:23]\n\t"
    "v_exp_f32_e32 v252, v252\n\t"
    "v_mfma_f32_16x16x32_bf16 v[24:27], a[20:23], v[120:123], v[24:27]\n\t"
    "buffer_load_dwordx4 v[92:95], v248, s[76:79], s84 offen offset:1024 sc1\n\t"
    "v_exp_f32_e32 v253, v253\n\t"
    "v_mfma_f32_16x16x32_bf16 v[28:31], a[20:23], v[124:127], v[28:31]\n\t"
    "s_nop 0\n\t"
    "v_mfma_f32_16x16x32_bf16 v[32:35], a[24:27], v[112:115], v[32:35]\n\t"
    "s_mov_b32 s87, 114688\n\t"
    "buffer_load_dwordx4 a[0:3], v240, s[64:67], s87 offen offset:0 sc1\n\t"
    "v_add_f32_e32 v252, 1.0, v252\n\t"
    "v_mfma_f32_16x16x32_bf16 v[36:39], a[24:27], v[116:119], v[36:39]\n\t"
    "v_add_f32_e32 v253, 1.0, v253\n\t"
    "v_mfma_f32_16x16x32_bf16 v[40:43], a[24:27], v[120:123], v[40:43]\n\t"
    "buffer_load_dwordx4 a[4:7], v240, s[64:67], s87 offen offset:1024 sc1\n\t"
    "v_rcp_f32_e32 v252, v252\n\t"
    "v_mfma_f32_16x16x32_bf16 v[44:47], a[24:27], v[124:127], v[44:47]\n\t"
    "v_rcp_f32_e32 v253, v253\n\t"
    "v_mfma_f32_16x16x32_bf16 v[48:51], a[28:31], v[112:115], v[48:51]\n\t"
    "buffer_load_dwordx4 a[8:11], v240, s[64:67], s87 offen offset:2048 sc1\n\t"
    "s_nop 0\n\t"
    "v_mfma_f32_16x16x32_bf16 v[52:55], a[28:31], v[116:119], v[52:55]\n\t"
    "v_mul_f32_e32 v251, v251, v252\n\t"
    "v_mfma_f32_16x16x32_bf16 v[56:59], a[28:31], v[120:123], v[56:59]\n\t"
    "buffer_load_dwordx4 a[12:15], v240, s[64:67], s87 offen offset:3072 sc1\n\t"
    "v_mul_f32_e32 v245, v245, v253\n\t"
    "v_mfma_f32_16x16x32_bf16 v[60:63], a[28:31], v[124:127], v[60:63]\n\t"
    "v_cvt_pk_bf16_f32 v236, v251, v245\n\t"
    "s_waitcnt vmcnt(28)\n\t"
    "v_mfma_f32_16x16x32_bf16 v[0:3], a[32:35], v[192:195], v[0:3]\n\t"
    "buffer_load_dwordx4 v[96:99], v240, s[76:79], s84 offen offset:2048 sc1\n\t"
    "v_lshlrev_b32_e32 v249, 16, v229\n\t"
    "v_mfma_f32_16x16x32_bf16 v[4:7], a[32:35], v[196:199], v[4:7]\n\t"
    "v_and_b32_e32 v250, 0xffff0000, v229\n\t"
    "v_mfma_f32_16x16x32_bf16 v[8:11], a[32:35], v[200:203], v[8:11]\n\t"
    "buffer_load_dwordx4 v[100:103], v246, s[76:79], s84 offen offset:2048 sc1\n\t"
    "v_sub_f32_e32 v249, v249, v242\n\t"
    "v_mfma_f32_16x16x32_bf16 v[12:15], a[32:35], v[204:207], v[12:15]\n\t"
    "v_sub_f32_e32 v250, v250, v242\n\t"
    "v_mfma_f32_16x16x32_bf16 v[16:19], a[36:39], v[192:195], v[16:19]\n\t"
    "buffer_load_dwordx4 v[104:107], v247, s[76:79], s84 offen offset:2048 sc1\n\t"
    "v_cvt_pk_bf16_f32 v250, v249, v250\n\t"
    "v_mfma_f32_16x16x32_bf16 v[20:23], a[36:39], v[196:199], v[20:23]\n\t"
    "v_lshlrev_b32_e32 v249, 16, v250\n\t"
    "v_mfma_f32_16x16x32_bf16 v[24:27], a[36:39], v[200:203], v[24:27]\n\t"
    "buffer_load_dwordx4 v[108:111], v248, s[76:79], s84 offen offset:2048 sc1\n\t"
    "v_and_b32_e32 v250, 0xffff0000, v250\n\t"
    "v_mfma_f32_16x16x32_bf16 v[28:31], a[36:39], v[204:207], v[28:31]\n\t"
    "v_mul_f32_e32 v249, v249, v244\n\t"
    "v_mfma_f32_16x16x32_bf16 v[32:35], a[40:43], v[192:195], v[32:35]\n\t"
    "s_mov_b32 s88, 118784\n\t"
    "buffer_load_dwordx4 a[16:19], v240, s[64:67], s88 offen offset:0 sc1\n\t"
    "v_mul_f32_e32 v250, v250, v244\n\t"
    "v_mfma_f32_16x16x32_bf16 v[36:39], a[40:43], v[196:199], v[36:39]\n\t"
    "v_cvt_pk_bf16_f32 v250, v249, v250\n\t"
    "v_mfma_f32_16x16x32_bf16 v[40:43], a[40:43], v[200:203], v[40:43]\n\t"
    "buffer_load_dwordx4 a[20:23], v240, s[64:67], s88 offen offset:1024 sc1\n\t"
    "v_lshlrev_b32_e32 v249, 16, v250\n\t"
    "v_mfma_f32_16x16x32_bf16 v[44:47], a[40:43], v[204:207], v[44:47]\n\t"
    "v_and_b32_e32 v250, 0xffff0000, v250\n\t"
    "v_mfma_f32_16x16x32_bf16 v[48:51], a[44:47], v[192:195], v[48:51]\n\t"
    "buffer_load_dwordx4 a[24:27], v240, s[64:67], s88 offen offset:2048 sc1\n\t"
    "v_mul_f32_e32 v252, 0xbfb8aa3b, v249\n\t"
    "v_mfma_f32_16x16x32_bf16 v[52:55], a[44:47], v[196:199], v[52:55]\n\t"
    "v_mul_f32_e32 v253, 0xbfb8aa3b, v250\n\t"
    "v_mfma_f32_16x16x32_bf16 v[56:59], a[44:47], v[200:203], v[56:59]\n\t"
    "buffer_load_dwordx4 a[28:31], v240, s[64:67], s88 offen offset:3072 sc1\n\t"
    "v_exp_f32_e32 v252, v252\n\t"
    "v_mfma_f32_16x16x32_bf16 v[60:63], a[44:47], v[204:207], v[60:63]\n\t"
    "v_exp_f32_e32 v253, v253\n\t"
    "s_waitcnt vmcnt(28)\n\t"
    "v_mfma_f32_16x16x32_bf16 v[0:3], a[48:51], v[208:211], v[0:3]\n\t"
    "buffer_load_dwordx4 v[112:115], v240, s[76:79], s84 offen offset:3072 sc1\n\t"
    "s_nop 0\n\t"
    "v_mfma_f32_16x16x32_bf16 v[4:7], a[48:51], v[212:215], v[4:7]\n\t"
    "v_add_f32_e32 v252, 1.0, v252\n\t"
    "v_mfma_f32_16x16x32_bf16 v[8:11], a[48:51], v[216:219], v[8:11]\n\t"
    "buffer_load_dwordx4 v[116:119], v246, s[76:79], s84 offen offset:3072 sc1\n\t"
    "v_add_f32_e32 v253, 1.0, v253\n\t"
    "v_mfma_f32_16x16x32_bf16 v[12:15], a[48:51], v[220:223], v[12:15]\n\t"
    "v_rcp_f32_e32 v252, v252\n\t"
    "v_mfma_f32_16x16x32_bf16 v[16:19], a[52:55], v[208:211], v[16:19]\n\t"
    "buffer_load_dwordx4 v[120:123], v247, s[76:79], s84 offen offset:3072 sc1\n\t"
    "v_rcp_f32_e32 v253, v253\n\t"
    "v_mfma_f32_16x16x32_bf16 v[20:23], a[52:55], v[212:215], v[20:23]\n\t"
    "s_nop 0\n\t"
    "v_mfma_f32_16x16x32_bf16 v[24:27], a[52:55], v[216:219], v[24:27]\n\t"
    "buffer_load_dwordx4 v[124:127], v248, s[76:79], s84 offen offset:3072 sc1\n\t"
    "v_mul_f32_e32 v249, v249, v252\n\t"
    "v_mfma_f32_16x16x32_bf16 v[28:31], a[52:55], v[220:223], v[28:31]\n\t"
    "v_mul_f32_e32 v250, v250, v253\n\t"
    "v_mfma_f32_16x16x32_bf16 v[32:35], a[56:59], v[208:211], v[32:35]\n\t"
    "s_mov_b32 s87, 122880\n\t"
    "buffer_load_dwordx4 a[32:35], v240, s[64:67], s87 offen offset:0 sc1\n\t"
    "v_cvt_pk_bf16_f32 v237, v249, v250\n\t"
    "v_mfma_f32_16x16x32_bf16 v[36:39], a[56:59], v[212:215], v[36:39]\n\t"
    "v_lshlrev_b32_e32 v251, 16, v230\n\t"
    "v_mfma_f32_16x16x32_bf16 v[40:43], a[56:59], v[216:219], v[40:43]\n\t"
    "buffer_load_dwordx4 a[36:39], v240, s[64:67], s87 offen offset:1024 sc1\n\t"
    "v_and_b32_e32 v245, 0xffff0000, v230\n\t"
    "v_mfma_f32_16x16x32_bf16 v[44:47], a[56:59], v[220:223], v[44:47]\n\t"
    "v_sub_f32_e32 v251, v251, v242\n\t"
    "v_mfma_f32_16x16x32_bf16 v[48:51], a[60:63], v[208:211], v[48:51]\n\t"
    "buffer_load_dwordx4 a[40:43], v240, s[64:67], s87 offen offset:2048 sc1\n\t"
    "v_sub_f32_e32 v245, v245, v242\n\t"
    "v_mfma_f32_16x16x32_bf16 v[52:55], a[60:63], v[212:215], v[52:55]\n\t"
    "v_cvt_pk_bf16_f32 v245, v251, v245\n\t"
    "v_mfma_f32_16x16x32_bf16 v[56:59], a[60:63], v[216:219], v[56:59]\n\t"
    "buffer_load_dwordx4 a[44:47], v240, s[64:67], s87 offen offset:3072 sc1\n\t"
    "v_lshlrev_b32_e32 v251, 16, v245\n\t"
    "v_mfma_f32_16x16x32_bf16 v[60:63], a[60:63], v[220:223], v[60:63]\n\t"
    "v_and_b32_e32 v245, 0xffff0000, v245\n\t"
    "s_waitcnt vmcnt(28)\n\t"
    "v_mfma_f32_16x16x32_bf16 v[0:3], a[64:67], v[64:67], v[0:3]\n\t"
    "s_mov_b32 s84, 16384\n\t"
    "buffer_load_dwordx4 v[192:195], v240, s[76:79], s84 offen offset:0 sc1\n\t"
    "v_mul_f32_e32 v251, v251, v244\n\t"
    "v_mfma_f32_16x16x32_bf16 v[4:7], a[64:67], v[68:71], v[4:7]\n\t"
    "v_mul_f32_e32 v245, v245, v244\n\t"
    "v_mfma_f32_16x16x32_bf16 v[8:11], a[64:67], v[72:75], v[8:11]\n\t"
    "buffer_load_dwordx4 v[196:199], v246, s[76:79], s84 offen offset:0 sc1\n\t"
    "v_cvt_pk_bf16_f32 v245, v251, v245\n\t"
    "v_mfma_f32_16x16x32_bf16 v[12:15], a[64:67], v[76:79], v[12:15]\n\t"
    "v_lshlrev_b32_e32 v251, 16, v245\n\t"
    "v_mfma_f32_16x16x32_bf16 v[16:19], a[68:71], v[64:67], v[16:19]\n\t"
    "buffer_load_dwordx4 v[200:203], v247, s[76:79], s84 offen offset:0 sc1\n\t"
    "v_and_b32_e32 v245, 0xffff0000, v245\n\t"
    "v_mfma_f32_16x16x32_bf16 v[20:23], a[68:71], v[68:71], v[20:23]\n\t"
    "v_mul_f32_e32 v252, 0xbfb8aa3b, v251\n\t"
    "v_mfma_f32_16x16x32_bf16 v[24:27], a[68:71], v[72:75], v[24:27]\n\t"
    "buffer_load_dwordx4 v[204:207], v248, s[76:79], s84 offen offset:0 sc1\n\t"
    "v_mul_f32_e32 v253, 0xbfb8aa3b, v245\n\t"
    "v_mfma_f32_16x16x32_bf16 v[28:31], a[68:71], v[76:79], v[28:31]\n\t"
    "v_exp_f32_e32 v252, v252\n\t"
    "v_mfma_f32_16x16x32_bf16 v[32:35], a[72:75], v[64:67], v[32:35]\n\t"
    "s_mov_b32 s88, 126976\n\t"
    "buffer_load_dwordx4 a[48:51], v240, s[64:67], s88 offen offset:0 sc1\n\t"
    "v_exp_f32_e32 v253, v253\n\t"
    "v_mfma_f32_16x16x32_bf16 v[36:39], a[72:75], v[68:71], v[36:39]\n\t"
    "s_nop 0\n\t"
    "v_mfma_f32_16x16x32_bf16 v[40:43], a[72:75], v[72:75], v[40:43]\n\t"
    "buffer_load_dwordx4 a[52:55], v240, s[64:67], s88 offen offset:1024 sc1\n\t"
    "v_add_f32_e32 v252, 1.0, v252\n\t"
    "v_mfma_f32_16x16x32_bf16 v[44:47], a[72:75], v[76:79], v[44:47]\n\t"
    "v_add_f32_e32 v253, 1.0, v253\n\t"
    "v_mfma_f32_16x16x32_bf16 v[48:51], a[76:79], v[64:67], v[48:51]\n\t"
    "buffer_load_dwordx4 a[56:59], v240, s[64:67], s88 offen offset:2048 sc1\n\t"
    "v_rcp_f32_e32 v252, v252\n\t"
    "v_mfma_f32_16x16x32_bf16 v[52:55], a[76:79], v[68:71], v[52:55]\n\t"
    "v_rcp_f32_e32 v253, v253\n\t"
    "v_mfma_f32_16x16x32_bf16 v[56:59], a[76:79], v[72:75], v[56:59]\n\t"
    "buffer_load_dwordx4 a[60:63], v240, s[64:67], s88 offen offset:3072 sc1\n\t"
    "s_nop 0\n\t"
    "v_mfma_f32_16x16x32_bf16 v[60:63], a[76:79], v[76:79], v[60:63]\n\t"
    "v_mul_f32_e32 v251, v251, v252\n\t"
    "s_waitcnt vmcnt(28)\n\t"
    "v_mfma_f32_16x16x32_bf16 v[0:3], a[80:83], v[80:83], v[0:3]\n\t"
    "buffer_load_dwordx4 v[208:211], v240, s[76:79], s84 offen offset:1024 sc1\n\t"
    "v_mul_f32_e32 v245, v245, v253\n\t"
    "v_mfma_f32_16x16x32_bf16 v[4:7], a[80:83], v[84:87], v[4:7]\n\t"
    "v_cvt_pk_bf16_f32 v238, v251, v245\n\t"
    "v_mfma_f32_16x16x32_bf16 v[8:11], a[80:83], v[88:91], v[8:11]\n\t"
    "v_lshlrev_b32_e32 v249, 16, v231\n\t"
    "v_mfma_f32_16x16x32_bf16 v[12:15], a[80:83], v[92:95], v[12:15]\n\t"
    "v_and_b32_e32 v250, 0xffff0000, v231\n\t"
    "v_mfma_f32_16x16x32_bf16 v[16:19], a[84:87], v[80:83], v[16:19]\n\t"
    "buffer_load_dwordx4 v[212:215], v246, s[76:79], s84 offen offset:1024 sc1\n\t"
    "v_sub_f32_e32 v249, v249, v242\n\t"
    "v_mfma_f32_16x16x32_bf16 v[20:23], a[84:87], v[84:87], v[20:23]\n\t"
    "v_sub_f32_e32 v250, v250, v242\n\t"
    "v_mfma_f32_16x16x32_bf16 v[24:27], a[84:87], v[88:91], v[24:27]\n\t"
    "v_cvt_pk_bf16_f32 v250, v249, v250\n\t"
    "v_mfma_f32_16x16x32_bf16 v[28:31], a[84:87], v[92:95], v[28:31]\n\t"
    "v_lshlrev_b32_e32 v249, 16, v250\n\t"
    "v_mfma_f32_16x16x32_bf16 v[32:35], a[88:91], v[80:83], v[32:35]\n\t"
    "buffer_load_dwordx4 v[216:219], v247, s[76:79], s84 offen offset:1024 sc1\n\t"
    "v_and_b32_e32 v250, 0xffff0000, v250\n\t"
    "v_mfma_f32_16x16x32_bf16 v[36:39], a[88:91], v[84:87], v[36:39]\n\t"
    "v_mul_f32_e32 v249, v249, v244\n\t"
    "v_mfma_f32_16x16x32_bf16 v[40:43], a[88:91], v[88:91], v[40:43]\n\t"
    "v_mul_f32_e32 v250, v250, v244\n\t"
    "v_mfma_f32_16x16x32_bf16 v[44:47], a[88:91], v[92:95], v[44:47]\n\t"
    "v_cvt_pk_bf16_f32 v250, v249, v250\n\t"
    "v_mfma_f32_16x16x32_bf16 v[48:51], a[92:95], v[80:83], v[48:51]\n\t"
    "buffer_load_dwordx4 v[220:223], v248, s[76:79], s84 offen offset:1024 sc1\n\t"
    "v_lshlrev_b32_e32 v249, 16, v250\n\t"
    "v_mfma_f32_16x16x32_bf16 v[52:55], a[92:95], v[84:87], v[52:55]\n\t"
    "v_and_b32_e32 v250, 0xffff0000, v250\n\t"
    "v_mfma_f32_16x16x32_bf16 v[56:59], a[92:95], v[88:91], v[56:59]\n\t"
    "v_mul_f32_e32 v252, 0xbfb8aa3b, v249\n\t"
    "v_mfma_f32_16x16x32_bf16 v[60:63], a[92:95], v[92:95], v[60:63]\n\t"
    "v_mul_f32_e32 v253, 0xbfb8aa3b, v250\n\t"
    "s_waitcnt vmcnt(24)\n\t"
    "v_mfma_f32_16x16x32_bf16 v[0:3], a[96:99], v[96:99], v[0:3]\n\t"
    "buffer_load_dwordx4 v[64:67], v240, s[76:79], s84 offen offset:2048 sc1\n\t"
    "v_exp_f32_e32 v252, v252\n\t"
    "v_mfma_f32_16x16x32_bf16 v[4:7], a[96:99], v[100:103], v[4:7]\n\t"
    "v_exp_f32_e32 v253, v253\n\t"
    "v_mfma_f32_16x16x32_bf16 v[8:11], a[96:99], v[104:107], v[8:11]\n\t"
    "s_nop 0\n\t"
    "v_mfma_f32_16x16x32_bf16 v[12:15], a[96:99], v[108:111], v[12:15]\n\t"
    "v_add_f32_e32 v252, 1.0, v252\n\t"
    "v_mfma_f32_16x16x32_bf16 v[16:19], a[100:103], v[96:99], v[16:19]\n\t"
    "buffer_load_dwordx4 v[68:71], v246, s[76:79], s84 offen offset:2048 sc1\n\t"
    "v_add_f32_e32 v253, 1.0, v253\n\t"
    "v_mfma_f32_16x16x32_bf16 v[20:23], a[100:103], v[100:103], v[20:23]\n\t"
    "v_rcp_f32_e32 v252, v252\n\t"
    "v_mfma_f32_16x16x32_bf16 v[24:27], a[100:103], v[104:107], v[24:27]\n\t"
    "v_rcp_f32_e32 v253, v253\n\t"
    "v_mfma_f32_16x16x32_bf16 v[28:31], a[100:103], v[108:111], v[28:31]\n\t"
    "s_nop 0\n\t"
    "v_mfma_f32_16x16x32_bf16 v[32:35], a[104:107], v[96:99], v[32:35]\n\t"
    "buffer_load_dwordx4 v[72:75], v247, s[76:79], s84 offen offset:2048 sc1\n\t"
    "v_mul_f32_e32 v249, v249, v252\n\t"
    "v_mfma_f32_16x16x32_bf16 v[36:39], a[104:107], v[100:103], v[36:39]\n\t"
    "v_mul_f32_e32 v250, v250, v253\n\t"
    "v_mfma_f32_16x16x32_bf16 v[40:43], a[104:107], v[104:107], v[40:43]\n\t"
    "v_cvt_pk_bf16_f32 v239, v249, v250\n\t"
    "v_mfma_f32_16x16x32_bf16 v[44:47], a[104:107], v[108:111], v[44:47]\n\t"
    "v_mfma_f32_16x16x32_bf16 v[48:51], a[108:111], v[96:99], v[48:51]\n\t"
    "buffer_load_dwordx4 v[76:79], v248, s[76:79], s84 offen offset:2048 sc1\n\t"
    "v_mfma_f32_16x16x32_bf16 v[52:55], a[108:111], v[100:103], v[52:55]\n\t"
    "v_mfma_f32_16x16x32_bf16 v[56:59], a[108:111], v[104:107], v[56:59]\n\t"
    "v_mfma_f32_16x16x32_bf16 v[60:63], a[108:111], v[108:111], v[60:63]\n\t"
    "s_waitcnt vmcnt(20)\n\t"
    "v_mfma_f32_16x16x32_bf16 v[0:3], a[112:115], v[112:115], v[0:3]\n\t"
    "buffer_load_dwordx4 v[80:83], v240, s[76:79], s84 offen offset:3072 sc1\n\t"
    "v_mfma_f32_16x16x32_bf16 v[4:7], a[112:115], v[116:119], v[4:7]\n\t"
    "v_mfma_f32_16x16x32_bf16 v[8:11], a[112:115], v[120:123], v[8:11]\n\t"
    "v_mfma_f32_16x16x32_bf16 v[12:15], a[112:115], v[124:127], v[12:15]\n\t"
    "v_mfma_f32_16x16x32_bf16 v[16:19], a[116:119], v[112:115], v[16:19]\n\t"
    "buffer_load_dwordx4 v[84:87], v246, s[76:79], s84 offen offset:3072 sc1\n\t"
    "v_mfma_f32_16x16x32_bf16 v[20:23], a[116:119], v[116:119], v[20:23]\n\t"
    "v_mfma_f32_16x16x32_bf16 v[24:27], a[116:119], v[120:123], v[24:27]\n\t"
    "v_mfma_f32_16x16x32_bf16 v[28:31], a[116:119], v[124:127], v[28:31]\n\t"
    "v_mfma_f32_16x16x32_bf16 v[32:35], a[120:123], v[112:115], v[32:35]\n\t"
    "buffer_load_dwordx4 v[88:91], v247, s[76:79], s84 offen offset:3072 sc1\n\t"
    "v_mfma_f32_16x16x32_bf16 v[36:39], a[120:123], v[116:119], v[36:39]\n\t"
    "v_mfma_f32_16x16x32_bf16 v[40:43], a[120:123], v[120:123], v[40:43]\n\t"
    "v_mfma_f32_16x16x32_bf16 v[44:47], a[120:123], v[124:127], v[44:47]\n\t"
    "v_mfma_f32_16x16x32_bf16 v[48:51], a[124:127], v[112:115], v[48:51]\n\t"
    "buffer_load_dwordx4 v[92:95], v248, s[76:79], s84 offen offset:3072 sc1\n\t"
    "v_mfma_f32_16x16x32_bf16 v[52:55], a[124:127], v[116:119], v[52:55]\n\t"
    "v_mfma_f32_16x16x32_bf16 v[56:59], a[124:127], v[120:123], v[56:59]\n\t"
    "v_mfma_f32_16x16x32_bf16 v[60:63], a[124:127], v[124:127], v[60:63]\n\t"
    "s_waitcnt vmcnt(16)\n\t"
    "v_mfma_f32_16x16x32_bf16 v[0:3], v[128:131], v[192:195], v[0:3]\n\t"
    "s_mov_b32 s84, 20480\n\t"
    "buffer_load_dwordx4 v[96:99], v240, s[76:79], s84 offen offset:0 sc1\n\t"
    "v_mfma_f32_16x16x32_bf16 v[4:7], v[128:131], v[196:199], v[4:7]\n\t"
    "v_mfma_f32_16x16x32_bf16 v[8:11], v[128:131], v[200:203], v[8:11]\n\t"
    "v_mfma_f32_16x16x32_bf16 v[12:15], v[128:131], v[204:207], v[12:15]\n\t"
    "v_mfma_f32_16x16x32_bf16 v[16:19], v[132:135], v[192:195], v[16:19]\n\t"
    "buffer_load_dwordx4 v[100:103], v246, s[76:79], s84 offen offset:0 sc1\n\t"
    "v_mfma_f32_16x16x32_bf16 v[20:23], v[132:135], v[196:199], v[20:23]\n\t"
    "v_mfma_f32_16x16x32_bf16 v[24:27], v[132:135], v[200:203], v[24:27]\n\t"
    "v_mfma_f32_16x16x32_bf16 v[28:31], v[132:135], v[204:207], v[28:31]\n\t"
    "v_mfma_f32_16x16x32_bf16 v[32:35], v[136:139], v[192:195], v[32:35]\n\t"
    "buffer_load_dwordx4 v[104:107], v247, s[76:79], s84 offen offset:0 sc1\n\t"
    "v_mfma_f32_16x16x32_bf16 v[36:39], v[136:139], v[196:199], v[36:39]\n\t"
    "v_mfma_f32_16x16x32_bf16 v[40:43], v[136:139], v[200:203], v[40:43]\n\t"
    "v_mfma_f32_16x16x32_bf16 v[44:47], v[136:139], v[204:207], v[44:47]\n\t"
    "v_mfma_f32_16x16x32_bf16 v[48:51], v[140:143], v[192:195], v[48:51]\n\t"
    "buffer_load_dwordx4 v[108:111], v248, s[76:79], s84 offen offset:0 sc1\n\t"
    "v_mfma_f32_16x16x32_bf16 v[52:55], v[140:143], v[196:199], v[52:55]\n\t"
    "v_mfma_f32_16x16x32_bf16 v[56:59], v[140:143], v[200:203], v[56:59]\n\t"
    "v_mfma_f32_16x16x32_bf16 v[60:63], v[140:143], v[204:207], v[60:63]\n\t"
    "s_waitcnt vmcnt(12)\n\t"
    "v_mfma_f32_16x16x32_bf16 v[0:3], v[144:147], v[208:211], v[0:3]\n\t"
    "buffer_load_dwordx4 v[112:115], v240, s[76:79], s84 offen offset:1024 sc1\n\t"
    "v_mfma_f32_16x16x32_bf16 v[4:7], v[144:147], v[212:215], v[4:7]\n\t"
    "v_mfma_f32_16x16x32_bf16 v[8:11], v[144:147], v[216:219], v[8:11]\n\t"
    "v_mfma_f32_16x16x32_bf16 v[12:15], v[144:147], v[220:223], v[12:15]\n\t"
    "v_mfma_f32_16x16x32_bf16 v[16:19], v[148:151], v[208:211], v[16:19]\n\t"
    "buffer_load_dwordx4 v[116:119], v246, s[76:79], s84 offen offset:1024 sc1\n\t"
    "v_mfma_f32_16x16x32_bf16 v[20:23], v[148:151], v[212:215], v[20:23]\n\t"
    "v_mfma_f32_16x16x32_bf16 v[24:27], v[148:151], v[216:219], v[24:27]\n\t"
    "v_mfma_f32_16x16x32_bf16 v[28:31], v[148:151], v[220:223], v[28:31]\n\t"
    "v_mfma_f32_16x16x32_bf16 v[32:35], v[152:155], v[208:211], v[32:35]\n\t"
    "buffer_load_dwordx4 v[120:123], v247, s[76:79], s84 offen offset:1024 sc1\n\t"
    "v_mfma_f32_16x16x32_bf16 v[36:39], v[152:155], v[212:215], v[36:39]\n\t"
    "v_mfma_f32_16x16x32_bf16 v[40:43], v[152:155], v[216:219], v[40:43]\n\t"
    "v_mfma_f32_16x16x32_bf16 v[44:47], v[152:155], v[220:223], v[44:47]\n\t"
    "v_mfma_f32_16x16x32_bf16 v[48:51], v[156:159], v[208:211], v[48:51]\n\t"
    "buffer_load_dwordx4 v[124:127], v248, s[76:79], s84 offen offset:1024 sc1\n\t"
    "v_mfma_f32_16x16x32_bf16 v[52:55], v[156:159], v[212:215], v[52:55]\n\t"
    "v_mfma_f32_16x16x32_bf16 v[56:59], v[156:159], v[216:219], v[56:59]\n\t"
    "v_mfma_f32_16x16x32_bf16 v[60:63], v[156:159], v[220:223], v[60:63]\n\t"
    "s_waitcnt vmcnt(12)\n\t"
    "v_mfma_f32_16x16x32_bf16 v[0:3], v[160:163], v[64:67], v[0:3]\n\t"
    "buffer_load_dwordx4 v[192:195], v240, s[76:79], s84 offen offset:2048 sc1\n\t"
    "v_mfma_f32_16x16x32_bf16 v[4:7], v[160:163], v[68:71], v[4:7]\n\t"
    "v_mfma_f32_16x16x32_bf16 v[8:11], v[160:163], v[72:75], v[8:11]\n\t"
    "v_mfma_f32_16x16x32_bf16 v[12:15], v[160:163], v[76:79], v[12:15]\n\t"
    "v_mfma_f32_16x16x32_bf16 v[16:19], v[164:167], v[64:67], v[16:19]\n\t"
    "buffer_load_dwordx4 v[196:199], v246, s[76:79], s84 offen offset:2048 sc1\n\t"
    "v_mfma_f32_16x16x32_bf16 v[20:23], v[164:167], v[68:71], v[20:23]\n\t"
    "v_mfma_f32_16x16x32_bf16 v[24:27], v[164:167], v[72:75], v[24:27]\n\t"
    "v_mfma_f32_16x16x32_bf16 v[28:31], v[164:167], v[76:79], v[28:31]\n\t"
    "v_mfma_f32_16x16x32_bf16 v[32:35], v[168:171], v[64:67], v[32:35]\n\t"
    "buffer_load_dwordx4 v[200:203], v247, s[76:79], s84 offen offset:2048 sc1\n\t"
    "v_mfma_f32_16x16x32_bf16 v[36:39], v[168:171], v[68:71], v[36:39]\n\t"
    "v_mfma_f32_16x16x32_bf16 v[40:43], v[168:171], v[72:75], v[40:43]\n\t"
    "v_mfma_f32_16x16x32_bf16 v[44:47], v[168:171], v[76:79], v[44:47]\n\t"
    "v_mfma_f32_16x16x32_bf16 v[48:51], v[172:175], v[64:67], v[48:51]\n\t"
    "buffer_load_dwordx4 v[204:207], v248, s[76:79], s84 offen offset:2048 sc1\n\t"
    "v_mfma_f32_16x16x32_bf16 v[52:55], v[172:175], v[68:71], v[52:55]\n\t"
    "v_mfma_f32_16x16x32_bf16 v[56:59], v[172:175], v[72:75], v[56:59]\n\t"
    "v_mfma_f32_16x16x32_bf16 v[60:63], v[172:175], v[76:79], v[60:63]\n\t"
    "s_waitcnt vmcnt(12)\n\t"
    "v_mfma_f32_16x16x32_bf16 v[0:3], v[176:179], v[80:83], v[0:3]\n\t"
    "buffer_load_dwordx4 v[208:211], v240, s[76:79], s84 offen offset:3072 sc1\n\t"
    "v_mfma_f32_16x16x32_bf16 v[4:7], v[176:179], v[84:87], v[4:7]\n\t"
    "v_mfma_f32_16x16x32_bf16 v[8:11], v[176:179], v[88:91], v[8:11]\n\t"
    "v_mfma_f32_16x16x32_bf16 v[12:15], v[176:179], v[92:95], v[12:15]\n\t"
    "v_mfma_f32_16x16x32_bf16 v[16:19], v[180:183], v[80:83], v[16:19]\n\t"
    "buffer_load_dwordx4 v[212:215], v246, s[76:79], s84 offen offset:3072 sc1\n\t"
    "v_mfma_f32_16x16x32_bf16 v[20:23], v[180:183], v[84:87], v[20:23]\n\t"
    "v_mfma_f32_16x16x32_bf16 v[24:27], v[180:183], v[88:91], v[24:27]\n\t"
    "v_mfma_f32_16x16x32_bf16 v[28:31], v[180:183], v[92:95], v[28:31]\n\t"
    "v_mfma_f32_16x16x32_bf16 v[32:35], v[184:187], v[80:83], v[32:35]\n\t"
    "buffer_load_dwordx4 v[216:219], v247, s[76:79], s84 offen offset:3072 sc1\n\t"
    "v_mfma_f32_16x16x32_bf16 v[36:39], v[184:187], v[84:87], v[36:39]\n\t"
    "v_mfma_f32_16x16x32_bf16 v[40:43], v[184:187], v[88:91], v[40:43]\n\t"
    "v_mfma_f32_16x16x32_bf16 v[44:47], v[184:187], v[92:95], v[44:47]\n\t"
    "v_mfma_f32_16x16x32_bf16 v[48:51], v[188:191], v[80:83], v[48:51]\n\t"
    "buffer_load_dwordx4 v[220:223], v248, s[76:79], s84 offen offset:3072 sc1\n\t"
    "v_mfma_f32_16x16x32_bf16 v[52:55], v[188:191], v[84:87], v[52:55]\n\t"
    "v_mfma_f32_16x16x32_bf16 v[56:59], v[188:191], v[88:91], v[56:59]\n\t"
    "v_mfma_f32_16x16x32_bf16 v[60:63], v[188:191], v[92:95], v[60:63]\n\t"
    "s_waitcnt vmcnt(12)\n\t"
    "v_mfma_f32_16x16x32_bf16 v[0:3], a[128:131], v[96:99], v[0:3]\n\t"
    "s_mov_b32 s84, 24576\n\t"
    "buffer_load_dwordx4 v[64:67], v240, s[76:79], s84 offen offset:0 sc1\n\t"
    "v_mfma_f32_16x16x32_bf16 v[4:7], a[128:131], v[100:103], v[4:7]\n\t"
    "v_mfma_f32_16x16x32_bf16 v[8:11], a[128:131], v[104:107], v[8:11]\n\t"
    "v_mfma_f32_16x16x32_bf16 v[12:15], a[128:131], v[108:111], v[12:15]\n\t"
    "v_mfma_f32_16x16x32_bf16 v[16:19], a[132:135], v[96:99], v[16:19]\n\t"
    "buffer_load_dwordx4 v[68:71], v246, s[76:79], s84 offen offset:0 sc1\n\t"
    "v_mfma_f32_16x16x32_bf16 v[20:23], a[132:135], v[100:103], v[20:23]\n\t"
    "v_mfma_f32_16x16x32_bf16 v[24:27], a[132:135], v[104:107], v[24:27]\n\t"
    "v_mfma_f32_16x16x32_bf16 v[28:31], a[132:135], v[108:111], v[28:31]\n\t"
    "v_mfma_f32_16x16x32_bf16 v[32:35], a[136:139], v[96:99], v[32:35]\n\t"
    "buffer_load_dwordx4 v[72:75], v247, s[76:79], s84 offen offset:0 sc1\n\t"
    "v_mfma_f32_16x16x32_bf16 v[36:39], a[136:139], v[100:103], v[36:39]\n\t"
    "v_mfma_f32_16x16x32_bf16 v[40:43], a[136:139], v[104:107], v[40:43]\n\t"
    "v_mfma_f32_16x16x32_bf16 v[44:47], a[136:139], v[108:111], v[44:47]\n\t"
    "v_mfma_f32_16x16x32_bf16 v[48:51], a[140:143], v[96:99], v[48:51]\n\t"
    "buffer_load_dwordx4 v[76:79], v248, s[76:79], s84 offen offset:0 sc1\n\t"
    "v_mfma_f32_16x16x32_bf16 v[52:55], a[140:143], v[100:103], v[52:55]\n\t"
    "v_mfma_f32_16x16x32_bf16 v[56:59], a[140:143], v[104:107], v[56:59]\n\t"
    "v_mfma_f32_16x16x32_bf16 v[60:63], a[140:143], v[108:111], v[60:63]\n\t"
    "s_waitcnt vmcnt(12)\n\t"
    "v_mfma_f32_16x16x32_bf16 v[0:3], a[144:147], v[112:115], v[0:3]\n\t"
    "buffer_load_dwordx4 v[80:83], v240, s[76:79], s84 offen offset:1024 sc1\n\t"
    "v_mfma_f32_16x16x32_bf16 v[4:7], a[144:147], v[116:119], v[4:7]\n\t"
    "v_mfma_f32_16x16x32_bf16 v[8:11], a[144:147], v[120:123], v[8:11]\n\t"
    "v_mfma_f32_16x16x32_bf16 v[12:15], a[144:147], v[124:127], v[12:15]\n\t"
    "v_mfma_f32_16x16x32_bf16 v[16:19], a[148:151], v[112:115], v[16:19]\n\t"
    "buffer_load_dwordx4 v[84:87], v246, s[76:79], s84 offen offset:1024 sc1\n\t"
    "v_mfma_f32_16x16x32_bf16 v[20:23], a[148:151], v[116:119], v[20:23]\n\t"
    "v_mfma_f32_16x16x32_bf16 v[24:27], a[148:151], v[120:123], v[24:27]\n\t"
    "v_mfma_f32_16x16x32_bf16 v[28:31], a[148:151], v[124:127], v[28:31]\n\t"
    "v_mfma_f32_16x16x32_bf16 v[32:35], a[152:155], v[112:115], v[32:35]\n\t"
    "buffer_load_dwordx4 v[88:91], v247, s[76:79], s84 offen offset:1024 sc1\n\t"
    "v_mfma_f32_16x16x32_bf16 v[36:39], a[152:155], v[116:119], v[36:39]\n\t"
    "v_mfma_f32_16x16x32_bf16 v[40:43], a[152:155], v[120:123], v[40:43]\n\t"
    "v_mfma_f32_16x16x32_bf16 v[44:47], a[152:155], v[124:127], v[44:47]\n\t"
    "v_mfma_f32_16x16x32_bf16 v[48:51], a[156:159], v[112:115], v[48:51]\n\t"
    "buffer_load_dwordx4 v[92:95], v248, s[76:79], s84 offen offset:1024 sc1\n\t"
    "v_mfma_f32_16x16x32_bf16 v[52:55], a[156:159], v[116:119], v[52:55]\n\t"
    "v_mfma_f32_16x16x32_bf16 v[56:59], a[156:159], v[120:123], v[56:59]\n\t"
    "v_mfma_f32_16x16x32_bf16 v[60:63], a[156:159], v[124:127], v[60:63]\n\t"
    "s_waitcnt vmcnt(12)\n\t"
    "v_mfma_f32_16x16x32_bf16 v[0:3], a[160:163], v[192:195], v[0:3]\n\t"
    "buffer_load_dwordx4 v[96:99], v240, s[76:79], s84 offen offset:2048 sc1\n\t"
    "v_mfma_f32_16x16x32_bf16 v[4:7], a[160:163], v[196:199], v[4:7]\n\t"
    "v_mfma_f32_16x16x32_bf16 v[8:11], a[160:163], v[200:203], v[8:11]\n\t"
    "v_mfma_f32_16x16x32_bf16 v[12:15], a[160:163], v[204:207], v[12:15]\n\t"
    "v_mfma_f32_16x16x32_bf16 v[16:19], a[164:167], v[192:195], v[16:19]\n\t"
    "buffer_load_dwordx4 v[100:103], v246, s[76:79], s84 offen offset:2048 sc1\n\t"
    "v_mfma_f32_16x16x32_bf16 v[20:23], a[164:167], v[196:199], v[20:23]\n\t"
    "v_mfma_f32_16x16x32_bf16 v[24:27], a[164:167], v[200:203], v[24:27]\n\t"
    "v_mfma_f32_16x16x32_bf16 v[28:31], a[164:167], v[204:207], v[28:31]\n\t"
    "v_mfma_f32_16x16x32_bf16 v[32:35], a[168:171], v[192:195], v[32:35]\n\t"
    "buffer_load_dwordx4 v[104:107], v247, s[76:79], s84 offen offset:2048 sc1\n\t"
    "v_mfma_f32_16x16x32_bf16 v[36:39], a[168:171], v[196:199], v[36:39]\n\t"
    "v_mfma_f32_16x16x32_bf16 v[40:43], a[168:171], v[200:203], v[40:43]\n\t"
    "v_mfma_f32_16x16x32_bf16 v[44:47], a[168:171], v[204:207], v[44:47]\n\t"
    "v_mfma_f32_16x16x32_bf16 v[48:51], a[172:175], v[192:195], v[48:51]\n\t"
    "buffer_load_dwordx4 v[108:111], v248, s[76:79], s84 offen offset:2048 sc1\n\t"
    "v_mfma_f32_16x16x32_bf16 v[52:55], a[172:175], v[196:199], v[52:55]\n\t"
    "v_mfma_f32_16x16x32_bf16 v[56:59], a[172:175], v[200:203], v[56:59]\n\t"
    "v_mfma_f32_16x16x32_bf16 v[60:63], a[172:175], v[204:207], v[60:63]\n\t"
    "s_waitcnt vmcnt(12)\n\t"
    "v_mfma_f32_16x16x32_bf16 v[0:3], a[176:179], v[208:211], v[0:3]\n\t"
    "buffer_load_dwordx4 v[112:115], v240, s[76:79], s84 offen offset:3072 sc1\n\t"
    "v_mfma_f32_16x16x32_bf16 v[4:7], a[176:179], v[212:215], v[4:7]\n\t"
    "v_mfma_f32_16x16x32_bf16 v[8:11], a[176:179], v[216:219], v[8:11]\n\t"
    "v_mfma_f32_16x16x32_bf16 v[12:15], a[176:179], v[220:223], v[12:15]\n\t"
    "v_mfma_f32_16x16x32_bf16 v[16:19], a[180:183], v[208:211], v[16:19]\n\t"
    "buffer_load_dwordx4 v[116:119], v246, s[76:79], s84 offen offset:3072 sc1\n\t"
    "v_mfma_f32_16x16x32_bf16 v[20:23], a[180:183], v[212:215], v[20:23]\n\t"
    "v_mfma_f32_16x16x32_bf16 v[24:27], a[180:183], v[216:219], v[24:27]\n\t"
    "v_mfma_f32_16x16x32_bf16 v[28:31], a[180:183], v[220:223], v[28:31]\n\t"
    "v_mfma_f32_16x16x32_bf16 v[32:35], a[184:187], v[208:211], v[32:35]\n\t"
    "buffer_load_dwordx4 v[120:123], v247, s[76:79], s84 offen offset:3072 sc1\n\t"
    "v_mfma_f32_16x16x32_bf16 v[36:39], a[184:187], v[212:215], v[36:39]\n\t"
    "v_mfma_f32_16x16x32_bf16 v[40:43], a[184:187], v[216:219], v[40:43]\n\t"
    "v_mfma_f32_16x16x32_bf16 v[44:47], a[184:187], v[220:223], v[44:47]\n\t"
    "v_mfma_f32_16x16x32_bf16 v[48:51], a[188:191], v[208:211], v[48:51]\n\t"
    "buffer_load_dwordx4 v[124:127], v248, s[76:79], s84 offen offset:3072 sc1\n\t"
    "v_mfma_f32_16x16x32_bf16 v[52:55], a[188:191], v[212:215], v[52:55]\n\t"
    "v_mfma_f32_16x16x32_bf16 v[56:59], a[188:191], v[216:219], v[56:59]\n\t"
    "v_mfma_f32_16x16x32_bf16 v[60:63], a[188:191], v[220:223], v[60:63]\n\t"
    "s_cmp_eq_u32 %[own], 0\n\t"
    "s_cbranch_scc1 L_ny_%=\n\t"
    "s_barrier\n\t"
    "s_add_u32 m0, s96, 0\n\t"
    "s_mov_b32 s85, 0\n\t"
    "buffer_load_dwordx4 v243, s[68:71], s85 offen sc1 lds\n\t"
    "s_add_u32 m0, s96, 4096\n\t"
    "s_mov_b32 s85, 32768\n\t"
    "buffer_load_dwordx4 v243, s[68:71], s85 offen sc1 lds\n\t"
    "s_add_u32 m0, s96, 8192\n\t"
    "s_mov_b32 s85, 65536\n\t"
    "buffer_load_dwordx4 v243, s[68:71], s85 offen sc1 lds\n\t"
    "s_add_u32 m0, s96, 12288\n\t"
    "s_mov_b32 s85, 98304\n\t"
    "buffer_load_dwordx4 v243, s[68:71], s85 offen sc1 lds\n\t"
    "s_add_u32 m0, s96, 16384\n\t"
    "s_mov_b32 s85, 131072\n\t"
    "buffer_load_dwordx4 v243, s[68:71], s85 offen sc1 lds\n\t"
    "s_add_u32 m0, s96, 20480\n\t"
    "s_mov_b32 s85, 163840\n\t"
    "buffer_load_dwordx4 v243, s[68:71], s85 offen sc1 lds\n\t"
    "s_add_u32 m0, s96, 24576\n\t"
    "s_mov_b32 s85, 196608\n\t"
    "buffer_load_dwordx4 v243, s[68:71], s85 offen sc1 lds\n\t"
    "s_add_u32 m0, s96, 28672\n\t"
    "s_mov_b32 s85, 229376\n\t"
    "buffer_load_dwordx4 v243, s[68:71], s85 offen sc1 lds\n\t"
    "s_add_u32 m0, s96, 32768\n\t"
    "s_mov_b32 s85, 262144\n\t"
    "buffer_load_dwordx4 v243, s[68:71], s85 offen sc1 lds\n\t"
    "s_add_u32 m0, s96, 36864\n\t"
    "s_mov_b32 s85, 294912\n\t"
    "buffer_load_dwordx4 v243, s[68:71], s85 offen sc1 lds\n\t"
    "s_add_u32 m0, s96, 40960\n\t"
    "s_mov_b32 s85, 327680\n\t"
    "buffer_load_dwordx4 v243, s[68:71], s85 offen sc1 lds\n\t"
    "s_add_u32 m0, s96, 54272\n\t"
    "s_mov_b32 s85, 360448\n\t"
    "buffer_load_dwordx4 v243, s[68:71], s85 offen sc1 lds\n\t"
    "s_add_u32 m0, s96, 58368\n\t"
    "s_mov_b32 s85, 393216\n\t"
    "buffer_load_dwordx4 v243, s[68:71], s85 offen sc1 lds\n\t"
    "s_add_u32 m0, s96, 62464\n\t"
    "s_mov_b32 s85, 425984\n\t"
    "buffer_load_dwordx4 v243, s[68:71], s85 offen sc1 lds\n\t"
    "s_add_u32 m0, s96, 66560\n\t"
    "s_mov_b32 s85, 458752\n\t"
    "buffer_load_dwordx4 v243, s[68:71], s85 offen sc1 lds\n\t"
    "s_add_u32 m0, s96, 70656\n\t"
    "s_mov_b32 s85, 491520\n\t"
    "buffer_load_dwordx4 v243, s[68:71], s85 offen sc1 lds\n\t"
    "s_add_u32 m0, s96, 74752\n\t"
    "s_mov_b32 s85, 524288\n\t"
    "buffer_load_dwordx4 v243, s[68:71], s85 offen sc1 lds\n\t"
    "s_add_u32 m0, s96, 78848\n\t"
    "s_mov_b32 s85, 557056\n\t"
    "buffer_load_dwordx4 v243, s[68:71], s85 offen sc1 lds\n\t"
    "s_add_u32 m0, s96, 82944\n\t"
    "s_mov_b32 s85, 589824\n\t"
    "buffer_load_dwordx4 v243, s[68:71], s85 offen sc1 lds\n\t"
    "s_add_u32 m0, s96, 87040\n\t"
    "s_mov_b32 s85, 622592\n\t"
    "buffer_load_dwordx4 v243, s[68:71], s85 offen sc1 lds\n\t"
    "s_add_u32 m0, s96, 91136\n\t"
    "s_mov_b32 s85, 655360\n\t"
    "buffer_load_dwordx4 v243, s[68:71], s85 offen sc1 lds\n\t"
    "s_cmp_ge_u32 %[wid], 2\n\t"
    "s_cbranch_scc1 L_ny_%=\n\t"
    "s_add_u32 m0, s96, 95232\n\t"
    "s_mov_b32 s85, 688128\n\t"
    "buffer_load_dwordx4 v243, s[68:71], s85 offen sc1 lds\n\t"
    "L_ny_%=:\n\t"
    "s_waitcnt vmcnt(12)\n\t"
    "v_mfma_f32_16x16x32_bf16 v[0:3], a[192:195], v[64:67], v[0:3]\n\t"
    "s_mov_b32 s84, 28672\n\t"
    "buffer_load_dwordx4 v[192:195], v240, s[76:79], s84 offen offset:0 sc1\n\t"
    "v_mfma_f32_16x16x32_bf16 v[4:7], a[192:195], v[68:71], v[4:7]\n\t"
    "v_mfma_f32_16x16x32_bf16 v[8:11], a[192:195], v[72:75], v[8:11]\n\t"
    "v_mfma_f32_16x16x32_bf16 v[12:15], a[192:195], v[76:79], v[12:15]\n\t"
    "v_mfma_f32_16x16x32_bf16 v[16:19], a[196:199], v[64:67], v[16:19]\n\t"
    "buffer_load_dwordx4 v[196:199], v246, s[76:79], s84 offen offset:0 sc1\n\t"
    "v_mfma_f32_16x16x32_bf16 v[20:23], a[196:199], v[68:71], v[20:23]\n\t"
    "v_mfma_f32_16x16x32_bf16 v[24:27], a[196:199], v[72:75], v[24:27]\n\t"
    "v_mfma_f32_16x16x32_bf16 v[28:31], a[196:199], v[76:79], v[28:31]\n\t"
    "v_mfma_f32_16x16x32_bf16 v[32:35], a[200:203], v[64:67], v[32:35]\n\t"
    "buffer_load_dwordx4 v[200:203], v247, s[76:79], s84 offen offset:0 sc1\n\t"
    "v_mfma_f32_16x16x32_bf16 v[36:39], a[200:203], v[68:71], v[36:39]\n\t"
    "v_mfma_f32_16x16x32_bf16 v[40:43], a[200:203], v[72:75], v[40:43]\n\t"
    "v_mfma_f32_16x16x32_bf16 v[44:47], a[200:203], v[76:79], v[44:47]\n\t"
    "v_mfma_f32_16x16x32_bf16 v[48:51], a[204:207], v[64:67], v[48:51]\n\t"
    "buffer_load_dwordx4 v[204:207], v248, s[76:79], s84 offen offset:0 sc1\n\t"
    "v_mfma_f32_16x16x32_bf16 v[52:55], a[204:207], v[68:71], v[52:55]\n\t"
    "v_mfma_f32_16x16x32_bf16 v[56:59], a[204:207], v[72:75], v[56:59]\n\t"
    "v_mfma_f32_16x16x32_bf16 v[60:63], a[204:207], v[76:79], v[60:63]\n\t"
    "s_waitcnt vmcnt(12)\n\t"
    "v_mfma_f32_16x16x32_bf16 v[0:3], a[208:211], v[80:83], v[0:3]\n\t"
    "buffer_load_dwordx4 v[208:211], v240, s[76:79], s84 offen offset:1024 sc1\n\t"
    "v_mfma_f32_16x16x32_bf16 v[4:7], a[208:211], v[84:87], v[4:7]\n\t"
    "v_mfma_f32_16x16x32_bf16 v[8:11], a[208:211], v[88:91], v[8:11]\n\t"
    "v_mfma_f32_16x16x32_bf16 v[12:15], a[208:211], v[92:95], v[12:15]\n\t"
    "v_mfma_f32_16x16x32_bf16 v[16:19], a[212:215], v[80:83], v[16:19]\n\t"
    "buffer_load_dwordx4 v[212:215], v246, s[76:79], s84 offen offset:1024 sc1\n\t"
    "v_mfma_f32_16x16x32_bf16 v[20:23], a[212:215], v[84:87], v[20:23]\n\t"
    "v_mfma_f32_16x16x32_bf16 v[24:27], a[212:215], v[88:91], v[24:27]\n\t"
    "v_mfma_f32_16x16x32_bf16 v[28:31], a[212:215], v[92:95], v[28:31]\n\t"
    "v_mfma_f32_16x16x32_bf16 v[32:35], a[216:219], v[80:83], v[32:35]\n\t"
    "buffer_load_dwordx4 v[216:219], v247, s[76:79], s84 offen offset:1024 sc1\n\t"
    "v_mfma_f32_16x16x32_bf16 v[36:39], a[216:219], v[84:87], v[36:39]\n\t"
    "v_mfma_f32_16x16x32_bf16 v[40:43], a[216:219], v[88:91], v[40:43]\n\t"
    "v_mfma_f32_16x16x32_bf16 v[44:47], a[216:219], v[92:95], v[44:47]\n\t"
    "v_mfma_f32_16x16x32_bf16 v[48:51], a[220:223], v[80:83], v[48:51]\n\t"
    "buffer_load_dwordx4 v[220:223], v248, s[76:79], s84 offen offset:1024 sc1\n\t"
    "v_mfma_f32_16x16x32_bf16 v[52:55], a[220:223], v[84:87], v[52:55]\n\t"
    "v_mfma_f32_16x16x32_bf16 v[56:59], a[220:223], v[88:91], v[56:59]\n\t"
    "v_mfma_f32_16x16x32_bf16 v[60:63], a[220:223], v[92:95], v[60:63]\n\t"
    "s_waitcnt vmcnt(12)\n\t"
    "v_mfma_f32_16x16x32_bf16 v[0:3], a[224:227], v[96:99], v[0:3]\n\t"
    "buffer_load_dwordx4 v[64:67], v240, s[76:79], s84 offen offset:2048 sc1\n\t"
    "v_mfma_f32_16x16x32_bf16 v[4:7], a[224:227], v[100:103], v[4:7]\n\t"
    "v_mfma_f32_16x16x32_bf16 v[8:11], a[224:227], v[104:107], v[8:11]\n\t"
    "v_mfma_f32_16x16x32_bf16 v[12:15], a[224:227], v[108:111], v[12:15]\n\t"
    "v_mfma_f32_16x16x32_bf16 v[16:19], a[228:231], v[96:99], v[16:19]\n\t"
    "buffer_load_dwordx4 v[68:71], v246, s[76:79], s84 offen offset:2048 sc1\n\t"
    "v_mfma_f32_16x16x32_bf16 v[20:23], a[228:231], v[100:103], v[20:23]\n\t"
    "v_mfma_f32_16x16x32_bf16 v[24:27], a[228:231], v[104:107], v[24:27]\n\t"
    "v_mfma_f32_16x16x32_bf16 v[28:31], a[228:231], v[108:111], v[28:31]\n\t"
    "v_mfma_f32_16x16x32_bf16 v[32:35], a[232:235], v[96:99], v[32:35]\n\t"
    "buffer_load_dwordx4 v[72:75], v247, s[76:79], s84 offen offset:2048 sc1\n\t"
    "v_mfma_f32_16x16x32_bf16 v[36:39], a[232:235], v[100:103], v[36:39]\n\t"
    "v_mfma_f32_16x16x32_bf16 v[40:43], a[232:235], v[104:107], v[40:43]\n\t"
    "v_mfma_f32_16x16x32_bf16 v[44:47], a[232:235], v[108:111], v[44:47]\n\t"
    "v_mfma_f32_16x16x32_bf16 v[48:51], a[236:239], v[96:99], v[48:51]\n\t"
    "buffer_load_dwordx4 v[76:79], v248, s[76:79], s84 offen offset:2048 sc1\n\t"
    "v_mfma_f32_16x16x32_bf16 v[52:55], a[236:239], v[100:103], v[52:55]\n\t"
    "v_mfma_f32_16x16x32_bf16 v[56:59], a[236:239], v[104:107], v[56:59]\n\t"
    "v_mfma_f32_16x16x32_bf16 v[60:63], a[236:239], v[108:111], v[60:63]\n\t"
    "s_waitcnt vmcnt(12)\n\t"
    "v_mfma_f32_16x16x32_bf16 v[0:3], a[240:243], v[112:115], v[0:3]\n\t"
    "buffer_load_dwordx4 v[80:83], v240, s[76:79], s84 offen offset:3072 sc1\n\t"
    "v_mfma_f32_16x16x32_bf16 v[4:7], a[240:243], v[116:119], v[4:7]\n\t"
    "v_mfma_f32_16x16x32_bf16 v[8:11], a[240:243], v[120:123], v[8:11]\n\t"
    "v_mfma_f32_16x16x32_bf16 v[12:15], a[240:243], v[124:127], v[12:15]\n\t"
    "v_mfma_f32_16x16x32_bf16 v[16:19], a[244:247], v[112:115], v[16:19]\n\t"
    "buffer_load_dwordx4 v[84:87], v246, s[76:79], s84 offen offset:3072 sc1\n\t"
    "v_mfma_f32_16x16x32_bf16 v[20:23], a[244:247], v[116:119], v[20:23]\n\t"
    "v_mfma_f32_16x16x32_bf16 v[24:27], a[244:247], v[120:123], v[24:27]\n\t"
    "v_mfma_f32_16x16x32_bf16 v[28:31], a[244:247], v[124:127], v[28:31]\n\t"
    "v_mfma_f32_16x16x32_bf16 v[32:35], a[248:251], v[112:115], v[32:35]\n\t"
    "buffer_load_dwordx4 v[88:91], v247, s[76:79], s84 offen offset:3072 sc1\n\t"
    "v_mfma_f32_16x16x32_bf16 v[36:39], a[248:251], v[116:119], v[36:39]\n\t"
    "v_mfma_f32_16x16x32_bf16 v[40:43], a[248:251], v[120:123], v[40:43]\n\t"
    "v_mfma_f32_16x16x32_bf16 v[44:47], a[248:251], v[124:127], v[44:47]\n\t"
    "v_mfma_f32_16x16x32_bf16 v[48:51], a[252:255], v[112:115], v[48:51]\n\t"
    "buffer_load_dwordx4 v[92:95], v248, s[76:79], s84 offen offset:3072 sc1\n\t"
    "v_mfma_f32_16x16x32_bf16 v[52:55], a[252:255], v[116:119], v[52:55]\n\t"
    "v_mfma_f32_16x16x32_bf16 v[56:59], a[252:255], v[120:123], v[56:59]\n\t"
    "v_mfma_f32_16x16x32_bf16 v[60:63], a[252:255], v[124:127], v[60:63]\n\t"
    "s_waitcnt vmcnt(12)\n\t"
    "v_mfma_f32_16x16x32_bf16 v[0:3], a[0:3], v[192:195], v[0:3]\n\t"
    "v_mfma_f32_16x16x32_bf16 v[4:7], a[0:3], v[196:199], v[4:7]\n\t"
    "v_mfma_f32_16x16x32_bf16 v[8:11], a[0:3], v[200:203], v[8:11]\n\t"
    "v_mfma_f32_16x16x32_bf16 v[12:15], a[0:3], v[204:207], v[12:15]\n\t"
    "v_mfma_f32_16x16x32_bf16 v[16:19], a[4:7], v[192:195], v[16:19]\n\t"
    "v_mfma_f32_16x16x32_bf16 v[20:23], a[4:7], v[196:199], v[20:23]\n\t"
    "v_mfma_f32_16x16x32_bf16 v[24:27], a[4:7], v[200:203], v[24:27]\n\t"
    "v_mfma_f32_16x16x32_bf16 v[28:31], a[4:7], v[204:207], v[28:31]\n\t"
    "v_mfma_f32_16x16x32_bf16 v[32:35], a[8:11], v[192:195], v[32:35]\n\t"
    "v_mfma_f32_16x16x32_bf16 v[36:39], a[8:11], v[196:199], v[36:39]\n\t"
    "v_mfma_f32_16x16x32_bf16 v[40:43], a[8:11], v[200:203], v[40:43]\n\t"
    "v_mfma_f32_16x16x32_bf16 v[44:47], a[8:11], v[204:207], v[44:47]\n\t"
    "v_mfma_f32_16x16x32_bf16 v[48:51], a[12:15], v[192:195], v[48:51]\n\t"
    "v_mfma_f32_16x16x32_bf16 v[52:55], a[12:15], v[196:199], v[52:55]\n\t"
    "v_mfma_f32_16x16x32_bf16 v[56:59], a[12:15], v[200:203], v[56:59]\n\t"
    "v_mfma_f32_16x16x32_bf16 v[60:63], a[12:15], v[204:207], v[60:63]\n\t"
    "s_waitcnt vmcnt(8)\n\t"
    "v_mfma_f32_16x16x32_bf16 v[0:3], a[16:19], v[208:211], v[0:3]\n\t"
    "v_mfma_f32_16x16x32_bf16 v[4:7], a[16:19], v[212:215], v[4:7]\n\t"
    "v_mfma_f32_16x16x32_bf16 v[8:11], a[16:19], v[216:219], v[8:11]\n\t"
    "v_mfma_f32_16x16x32_bf16 v[12:15], a[16:19], v[220:223], v[12:15]\n\t"
    "v_mfma_f32_16x16x32_bf16 v[16:19], a[20:23], v[208:211], v[16:19]\n\t"
    "v_mfma_f32_16x16x32_bf16 v[20:23], a[20:23], v[212:215], v[20:23]\n\t"
    "v_mfma_f32_16x16x32_bf16 v[24:27], a[20:23], v[216:219], v[24:27]\n\t"
    "v_mfma_f32_16x16x32_bf16 v[28:31], a[20:23], v[220:223], v[28:31]\n\t"
    "v_mfma_f32_16x16x32_bf16 v[32:35], a[24:27], v[208:211], v[32:35]\n\t"
    "v_mfma_f32_16x16x32_bf16 v[36:39], a[24:27], v[212:215], v[36:39]\n\t"
    "v_mfma_f32_16x16x32_bf16 v[40:43], a[24:27], v[216:219], v[40:43]\n\t"
    "v_mfma_f32_16x16x32_bf16 v[44:47], a[24:27], v[220:223], v[44:47]\n\t"
    "v_mfma_f32_16x16x32_bf16 v[48:51], a[28:31], v[208:211], v[48:51]\n\t"
    "v_mfma_f32_16x16x32_bf16 v[52:55], a[28:31], v[212:215], v[52:55]\n\t"
    "v_mfma_f32_16x16x32_bf16 v[56:59], a[28:31], v[216:219], v[56:59]\n\t"
    "v_mfma_f32_16x16x32_bf16 v[60:63], a[28:31], v[220:223], v[60:63]\n\t"
    "s_waitcnt vmcnt(4)\n\t"
    "v_mfma_f32_16x16x32_bf16 v[0:3], a[32:35], v[64:67], v[0:3]\n\t"
    "v_mfma_f32_16x16x32_bf16 v[4:7], a[32:35], v[68:71], v[4:7]\n\t"
    "v_mfma_f32_16x16x32_bf16 v[8:11], a[32:35], v[72:75], v[8:11]\n\t"
    "v_mfma_f32_16x16x32_bf16 v[12:15], a[32:35], v[76:79], v[12:15]\n\t"
    "v_mfma_f32_16x16x32_bf16 v[16:19], a[36:39], v[64:67], v[16:19]\n\t"
    "v_mfma_f32_16x16x32_bf16 v[20:23], a[36:39], v[68:71], v[20:23]\n\t"
    "v_mfma_f32_16x16x32_bf16 v[24:27], a[36:39], v[72:75], v[24:27]\n\t"
    "v_mfma_f32_16x16x32_bf16 v[28:31], a[36:39], v[76:79], v[28:31]\n\t"
    "v_mfma_f32_16x16x32_bf16 v[32:35], a[40:43], v[64:67], v[32:35]\n\t"
    "v_mfma_f32_16x16x32_bf16 v[36:39], a[40:43], v[68:71], v[36:39]\n\t"
    "v_mfma_f32_16x16x32_bf16 v[40:43], a[40:43], v[72:75], v[40:43]\n\t"
    "v_mfma_f32_16x16x32_bf16 v[44:47], a[40:43], v[76:79], v[44:47]\n\t"
    "v_mfma_f32_16x16x32_bf16 v[48:51], a[44:47], v[64:67], v[48:51]\n\t"
    "v_mfma_f32_16x16x32_bf16 v[52:55], a[44:47], v[68:71], v[52:55]\n\t"
    "v_mfma_f32_16x16x32_bf16 v[56:59], a[44:47], v[72:75], v[56:59]\n\t"
    "v_mfma_f32_16x16x32_bf16 v[60:63], a[44:47], v[76:79], v[60:63]\n\t"
    "s_waitcnt vmcnt(0)\n\t"
    "v_mfma_f32_16x16x32_bf16 v[0:3], a[48:51], v[80:83], v[0:3]\n\t"
    "v_mfma_f32_16x16x32_bf16 v[4:7], a[48:51], v[84:87], v[4:7]\n\t"
    "v_mfma_f32_16x16x32_bf16 v[8:11], a[48:51], v[88:91], v[8:11]\n\t"
    "v_mfma_f32_16x16x32_bf16 v[12:15], a[48:51], v[92:95], v[12:15]\n\t"
    "v_mfma_f32_16x16x32_bf16 v[16:19], a[52:55], v[80:83], v[16:19]\n\t"
    "v_mfma_f32_16x16x32_bf16 v[20:23], a[52:55], v[84:87], v[20:23]\n\t"
    "v_mfma_f32_16x16x32_bf16 v[24:27], a[52:55], v[88:91], v[24:27]\n\t"
    "v_mfma_f32_16x16x32_bf16 v[28:31], a[52:55], v[92:95], v[28:31]\n\t"
    "v_mfma_f32_16x16x32_bf16 v[32:35], a[56:59], v[80:83], v[32:35]\n\t"
    "v_mfma_f32_16x16x32_bf16 v[36:39], a[56:59], v[84:87], v[36:39]\n\t"
    "v_mfma_f32_16x16x32_bf16 v[40:43], a[56:59], v[88:91], v[40:43]\n\t"
    "v_mfma_f32_16x16x32_bf16 v[44:47], a[56:59], v[92:95], v[44:47]\n\t"
    "v_mfma_f32_16x16x32_bf16 v[48:51], a[60:63], v[80:83], v[48:51]\n\t"
    "v_mfma_f32_16x16x32_bf16 v[52:55], a[60:63], v[84:87], v[52:55]\n\t"
    "v_mfma_f32_16x16x32_bf16 v[56:59], a[60:63], v[88:91], v[56:59]\n\t"
    "v_mfma_f32_16x16x32_bf16 v[60:63], a[60:63], v[92:95], v[60:63]\n\t"
    "v_xor_b32_e32 v246, 64, v241\n\t"
    "v_xor_b32_e32 v247, 128, v241\n\t"
    "v_xor_b32_e32 v248, 192, v241\n\t"
    "s_nop 4\n\t"
    "ds_write_b128 v241, v[0:3] offset:0\n\t"
    "ds_write_b128 v241, v[4:7] offset:4096\n\t"
    "ds_write_b128 v241, v[8:11] offset:8192\n\t"
    "ds_write_b128 v241, v[12:15] offset:12288\n\t"
    "ds_write_b128 v246, v[16:19] offset:0\n\t"
    "ds_write_b128 v246, v[20:23] offset:4096\n\t"
    "ds_write_b128 v246, v[24:27] offset:8192\n\t"
    "ds_write_b128 v246, v[28:31] offset:12288\n\t"
    "ds_write_b128 v247, v[32:35] offset:0\n\t"
    "ds_write_b128 v247, v[36:39] offset:4096\n\t"
    "ds_write_b128 v247, v[40:43] offset:8192\n\t"
    "ds_write_b128 v247, v[44:47] offset:12288\n\t"
    "ds_write_b128 v248, v[48:51] offset:0\n\t"
    "ds_write_b128 v248, v[52:55] offset:4096\n\t"
    "ds_write_b128 v248, v[56:59] offset:8192\n\t"
    "ds_write_b128 v248, v[60:63] offset:12288\n\t"
    "s_waitcnt vmcnt(0) lgkmcnt(0)\n\t"
    "s_barrier\n\t"
    : "+{v[224:227]}"(xa), "+{v[228:231]}"(xc), "=&{v[232:235]}"(ga), "=&{v[236:239]}"(gc), [st] "+{v254}"(st0)
    : [wid] "s"(wid), [h] "s"(h), [b] "s"(b), [bx] "s"(bx), [tag] "s"(tag), [ka] "s"(ka), [own] "s"(own)
    : "v0", "v1", "v2", "v3", "v4", "v5", "v6", "v7", "v8", "v9", "v10", "v11", "v12", "v13", "v14", "v15", "v16", "v17", "v18", "v19", "v20", "v21", "v22", "v23", "v24", "v25", "v26", "v27", "v28", "v29", "v30", "v31", "v32", "v33", "v34", "v35", "v36", "v37", "v38", "v39", "v40", "v41", "v42", "v43", "v44", "v45", "v46", "v47", "v48", "v49", "v50", "v51", "v52", "v53", "v54", "v55", "v56", "v57", "v58", "v59", "v60", "v61", "v62", "v63", "v64", "v65", "v66", "v67", "v68", "v69", "v70", "v71", "v72", "v73", "v74", "v75", "v76", "v77", "v78", "v79", "v80", "v81", "v82", "v83", "v84", "v85", "v86", "v87", "v88", "v89", "v90", "v91", "v92", "v93", "v94", "v95", "v96", "v97", "v98", "v99", "v100", "v101", "v102", "v103", "v104", "v105", "v106", "v107", "v108", "v109", "v110", "v111", "v112", "v113", "v114", "v115", "v116", "v117", "v118", "v119", "v120", "v121", "v122", "v123", "v124", "v125", "v126", "v127", "v128", "v129", "v130", "v131", "v132", "v133", "v134", "v135", "v136", "v137", "v138", "v139", "v140", "v141", "v142", "v143", "v144", "v145", "v146", "v147", "v148", "v149", "v150", "v151", "v152", "v153", "v154", "v155", "v156", "v157", "v158", "v159", "v160", "v161", "v162", "v163", "v164", "v165", "v166", "v167", "v168", "v169", "v170", "v171", "v172", "v173", "v174", "v175", "v176", "v177", "v178", "v179", "v180", "v181", "v182", "v183", "v184", "v185", "v186", "v187", "v188", "v189", "v190", "v191", "v192", "v193", "v194", "v195", "v196", "v197", "v198", "v199", "v200", "v201", "v202", "v203", "v204", "v205", "v206", "v207", "v208", "v209", "v210", "v211", "v212", "v213", "v214", "v215", "v216", "v217", "v218", "v219", "v220", "v221", "v222", "v223", "v240", "v241", "v242", "v243", "v244", "v245", "v246", "v247", "v248", "v249", "v250", "v251", "v252", "v253", "v255", "a0", "a1", "a2", "a3", "a4", "a5", "a6", "a7", "a8", "a9", "a10", "a11", "a12", "a13", "a14", "a15", "a16", "a17", "a18", "a19", "a20", "a21", "a22", "a23", "a24", "a25", "a26", "a27", "a28", "a29", "a30", "a31", "a32", "a33", "a34", "a35", "a36", "a37", "a38", "a39", "a40", "a41", "a42", "a43", "a44", "a45", "a46", "a47", "a48", "a49", "a50", "a51", "a52", "a53", "a54", "a55", "a56", "a57", "a58", "a59", "a60", "a61", "a62", "a63", "a64", "a65", "a66", "a67", "a68", "a69", "a70", "a71", "a72", "a73", "a74", "a75", "a76", "a77", "a78", "a79", "a80", "a81", "a82", "a83", "a84", "a85", "a86", "a87", "a88", "a89", "a90", "a91", "a92", "a93", "a94", "a95", "a96", "a97", "a98", "a99", "a100", "a101", "a102", "a103", "a104", "a105", "a106", "a107", "a108", "a109", "a110", "a111", "a112", "a113", "a114", "a115", "a116", "a117", "a118", "a119", "a120", "a121", "a122", "a123", "a124", "a125", "a126", "a127", "a128", "a129", "a130", "a131", "a132", "a133", "a134", "a135", "a136", "a137", "a138", "a139", "a140", "a141", "a142", "a143", "a144", "a145", "a146", "a147", "a148", "a149", "a150", "a151", "a152", "a153", "a154", "a155", "a156", "a157", "a158", "a159", "a160", "a161", "a162", "a163", "a164", "a165", "a166", "a167", "a168", "a169", "a170", "a171", "a172", "a173", "a174", "a175", "a176", "a177", "a178", "a179", "a180", "a181", "a182", "a183", "a184", "a185", "a186", "a187", "a188", "a189", "a190", "a191", "a192", "a193", "a194", "a195", "a196", "a197", "a198", "a199", "a200", "a201", "a202", "a203", "a204", "a205", "a206", "a207", "a208", "a209", "a210", "a211", "a212", "a213", "a214", "a215", "a216", "a217", "a218", "a219", "a220", "a221", "a222", "a223", "a224", "a225", "a226", "a227", "a228", "a229", "a230", "a231", "a232", "a233", "a234", "a235", "a236", "a237", "a238", "a239", "a240", "a241", "a242", "a243", "a244", "a245", "a246", "a247", "a248", "a249", "a250", "a251", "a252", "a253", "a254", "a255", "s33", "s34", "s35", "s36", "s37", "s38", "s39", "s40", "s41", "s42", "s43", "s44", "s45", "s46", "s47", "s48", "s49", "s50", "s51", "s52", "s53", "s54", "s55", "s56", "s57", "s58", "s59", "s60", "s61", "s62", "s63", "s64", "s65", "s66", "s67", "s68", "s69", "s70", "s71", "s72", "s73", "s74", "s75", "s76", "s77", "s78", "s79", "s80", "s81", "s82", "s83", "s84", "s85", "s86", "s87", "s88", "s89", "s90", "s91", "s92", "s93", "s94", "s95", "s96", "s97", "s98", "s99", "vcc", "scc", "memory");
  }
  const u32 lane = lane_id(), tid = 64 * wid + lane, q = tid >> 2, n0 = 16 * (tid & 3), row = 64 * h + q, col = 64 * b + n0;
  ka = again(ka); ws = gp<char>(ka[9]); out = gp<u16>(ka[10]);
  if (own) {
    const u32 cp = tid & 31, jg = tid >> 5; float y0 = 0, y1 = 0; bool ok = true;
#pragma unroll
    for (u32 i = 0; i < 22; ++i) { const u32 j = min(jg + 8 * i, (u32)NFF - 1); const u32x4 g = *(const u32x4*)(lds + 512 * j + (i < 11 ? 0 : 0xD400 - 0xB000) + 16 * cp);
      const bool in = i < 21 || jg < 4; ok &= g[1] == tag && g[3] == tag; y0 += in ? __uint_as_float(g[0]) : 0.f; y1 += in ? __uint_as_float(g[2]) : 0.f; }
    if (!ok) {
      y0 = y1 = 0;
#pragma unroll 1
      for (u32 j = jg; j < NFF; j += 8) { u64 g0 = 0, g1 = 0; const gu64* p = (const gu64*)(ws + WS_YP) + j * 512 + 64 * b + 2 * cp;
        for (int spin = 0; spin < SPIN; ++spin) { g0 = __hip_atomic_load(p, RLX); g1 = __hip_atomic_load(p + 1, RLX); if ((u32)(g0 >> 32) == tag && (u32)(g1 >> 32) == tag) break; }
        y0 += __uint_as_float((u32)g0); y1 += __uint_as_float((u32)g1); }
    }
    const float xs = lo16(*(const u16*)(lds + L_XT + 128 * (idx & 63) + 2 * lane));
    float* yb = (float*)(lds + L_XT + ((idx & 32) ? 0 : 4096));
    *(f32x2*)(yb + 64 * jg + 2 * cp) = f32x2{y0, y1};
    bar();
    if (wid == ((idx & 63) >> 4)) {
      const u32 sr = __builtin_amdgcn_readlane(st0, 4 * (idx & 15));
      float y = 0;
#pragma unroll
      for (int j = 0; j < 8; ++j) y += yb[64 * j + lane];
      u16* gb = (u16*)(yb + 512);
      gb[lane] = (u16)pk(silu(rbf(xnorm(xs, lo16(sr), rcp(hi16(sr))) + rbf(rbf(y) * __uint_as_float(rsb)))), 0.f);
      asm volatile("s_waitcnt lgkmcnt(0)" ::: "memory");
      const u32x4 g0 = *(const u32x4*)(gb + n0), g1 = *(const u32x4*)(gb + n0 + 8);
      if (row == idx) { ga = g0; gc = g1; }
    }
  }
  float s[16];
#pragma unroll
  for (int i = 0; i < 4; ++i) {
    const u32 po = L_PT + 256 * q + 16 * ((4 * (tid & 3) + i) ^ (q & 15));
    f32x4 a = *(const f32x4*)(lds + po);
#pragma unroll
    for (int w = 1; w < 4; ++w) a += *(const f32x4*)(lds + po + 16384 * w);
    s[4 * i] = a[0]; s[4 * i + 1] = a[1]; s[4 * i + 2] = a[2]; s[4 * i + 3] = a[3];
  }
  u32 o[8];
#pragma unroll
  for (int i = 0; i < 8; ++i) { const u32 w = i < 4 ? xa[i & 3] : xc[i & 3], g = i < 4 ? ga[i & 3] : gc[i & 3], sb = pk(s[2 * i], s[2 * i + 1]), t = pk(lo16(sb) * lo16(g), hi16(sb) * hi16(g));
    o[i] = pk(lo16(w) + lo16(t), hi16(w) + hi16(t)); }
  {
    const u32x4 o0 = {o[0], o[1], o[2], o[3]}, o1 = {o[4], o[5], o[6], o[7]}; const u32 off = (row * 512 + col) * 2;
    asm volatile("global_store_dwordx4 %0, %1, %3 sc1\n\tglobal_store_dwordx4 %0, %2, %3 offset:16 sc1\n\ts_nop 1" :: "v"(off), "v"(o0), "v"(o1), "s"(out) : "memory");
  }
  asm volatile("Lmha_end:" ::: "memory");
}

extern "C" void kernel_launch(void* const* d_in, const int* in_sizes, int n_in, void* d_out, int out_size, void* d_ws, size_t ws_size, hipStream_t stream) {
  (void)hipFuncSetAttribute((const void*)mha_mod_kernel, hipFuncAttributeMaxDynamicSharedMemorySize, LDS_BYTES);
  mha_mod_kernel<<<256, 256, LDS_BYTES, stream>>>((const u16*)d_in[0], (const u16*)d_in[1], (const u16*)d_in[2], (const u16*)d_in[3], (const u16*)d_in[4], (const u16*)d_in[6],
                                                  (const u16*)d_in[7], (const u16*)d_in[8], (const u16*)d_in[9], (char*)d_ws, (u16*)d_out);
}
```
